```python
import torch

def _softmax(x, axis=-1):
    return torch.softmax(x, dim=axis)
def _gelu(x):
    return torch.nn.functional.gelu(x, approximate="tanh")
def _silu(x):
    return torch.nn.functional.silu(x)
def _sigmoid(x):
    return torch.sigmoid(x)
def _layernorm(x, axis=-1, eps=1e-5):
    m = x.mean(dim=axis, keepdim=True)
    v = x.var(dim=axis, keepdim=True, unbiased=False)
    return (x - m) / torch.sqrt(v + eps)
def _scatter_add(tgt, idx, src, axis=0):
    return tgt.clone().index_add_(axis, idx, src)
def _segment_sum(src, seg, n):
    out = torch.zeros((n,) + src.shape[1:], dtype=src.dtype)
    return out.index_add_(0, seg, src)
def _pad(x, pw):
    flat = tuple(v for pair in reversed(pw) for v in pair)
    return torch.nn.functional.pad(x, flat)

kernel_name = 'parallel_sum__sliding_block__dense_activation_relu__prompt'

def setup_inputs():

    _w = (lambda a: (lambda t: t.to(torch.bfloat16) if t.is_floating_point() else t)(torch.as_tensor(a)))
    def _A_setup():
        B, S, D = 1, 2048, 1024
        return {
            "x": _w((lambda *s: torch.randn(*s, dtype=torch.bfloat16) * 0.05)(B, S, D)),
            "wqkv": _w((lambda *s: torch.randn(*s, dtype=torch.bfloat16) * 0.05)(D, 3 * D)),
            "wo": _w((lambda *s: torch.randn(*s, dtype=torch.bfloat16) * 0.05)(D, D)),
        }
    _A_in = _A_setup()

    def _B_setup():
        B, S, D, Dff = 1, 2048, 1024, 3584
        return {
            "x": _w((lambda *s: torch.randn(*s, dtype=torch.bfloat16) * 0.05)(B, S, D)),
            "w1": _w((lambda *s: torch.randn(*s, dtype=torch.bfloat16) * 0.05)(D, Dff)),
            "w2": _w((lambda *s: torch.randn(*s, dtype=torch.bfloat16) * 0.05)(Dff, D)),
        }
    _B_in = _B_setup()

    return {
        "A_x": _A_in["x"],
        "A_wqkv": _A_in["wqkv"],
        "A_wo": _A_in["wo"],
        "B_x": _B_in["x"],
        "B_w1": _B_in["w1"],
        "B_w2": _B_in["w2"],
    }

def reference(A_x, A_wqkv, A_wo, B_x, B_w1, B_w2):
    def _A_ref(x, wqkv, wo):
        B, S, D = x.shape
        H = 32; Dh = D // H; W = 256
        qkv = (lambda x, *d: x.permute(*d))((x @ wqkv).reshape(B, S, 3, H, Dh), 2, 0, 3, 1, 4)
        q, k, v = qkv[0], qkv[1], qkv[2]
        scores = torch.einsum("bhqd,bhkd->bhqk", q, k) * (Dh ** -0.5)
        idx = torch.arange(S)
        mask = (idx[:, None] - idx[None, :] >= 0) & (idx[:, None] - idx[None, :] < W)
        scores = torch.where(mask[None, None], scores, -1e9)
        p = _softmax(scores, axis=-1)
        o = torch.einsum("bhqk,bhkd->bhqd", p, v)
        return (lambda x, *d: x.permute(*d))(o, 0, 2, 1, 3).reshape(B, S, D) @ wo

    def _B_ref(x, w1, w2):
        h = torch.einsum("bsd,df->bsf", x, w1)
        zero = torch.zeros_like(h)
        h = torch.maximum(h, zero)
        return torch.einsum("bsf,fd->bsd", h, w2)

    x = A_x
    xn = _layernorm(x, axis=-1)
    return x + _A_ref(xn, A_wqkv, A_wo) + _B_ref(xn, B_w1, B_w2)
```

```cpp
#include <hip/hip_runtime.h>
#include <hip/hip_bf16.h>
#include <stdint.h>

typedef unsigned short u16;
typedef __attribute__((ext_vector_type(8))) short bf16x8;
typedef __attribute__((ext_vector_type(4))) float f32x4;

constexpr int S = 2048, D = 1024, H = 32, DH = 32, W = 256, DFF = 3584;

__device__ __forceinline__ float bf2f(u16 v) { return __uint_as_float(((unsigned)v) << 16); }
__device__ __forceinline__ u16 f2bf(float f) { __hip_bfloat16 b = __float2bfloat16(f); return *reinterpret_cast<u16*>(&b); }

__global__ __launch_bounds__(256) void ln_kernel(const u16* __restrict__ x, u16* __restrict__ xn) {
  int row = blockIdx.x * 4 + (threadIdx.x >> 6), lane = threadIdx.x & 63;
  const u16* xr = x + (size_t)row * D;
  float v[16];
  for (int i = 0; i < 2; ++i) {
    bf16x8 t = *reinterpret_cast<const bf16x8*>(xr + i * 512 + lane * 8);
    for (int j = 0; j < 8; ++j) v[i * 8 + j] = bf2f((u16)t[j]);
  }
  float s = 0;
  for (int j = 0; j < 16; ++j) s += v[j];
  for (int o = 32; o; o >>= 1) s += __shfl_xor(s, o);
  float m = s * (1.0f / D), q = 0;
  for (int j = 0; j < 16; ++j) { float d = v[j] - m; q += d * d; }
  for (int o = 32; o; o >>= 1) q += __shfl_xor(q, o);
  float r = rsqrtf(q * (1.0f / D) + 1e-5f);
  for (int i = 0; i < 2; ++i) {
    bf16x8 t;
    for (int j = 0; j < 8; ++j) t[j] = (short)f2bf((v[i * 8 + j] - m) * r);
    *reinterpret_cast<bf16x8*>(xn + (size_t)row * D + i * 512 + lane * 8) = t;
  }
}

template <int MODE>
__global__ __launch_bounds__(256) void gemm_kernel(const u16* __restrict__ A, int lda, const u16* __restrict__ B, int ldb, int K,
                                                   const u16* __restrict__ A2, int lda2, const u16* __restrict__ B2, int ldb2, int K2,
                                                   u16* __restrict__ C, int ldc, const u16* __restrict__ res) {
  __shared__ u16 As[64][40];
  __shared__ u16 Bs[64][40];
  int tid = threadIdx.x, lane = tid & 63, wid = tid >> 6;
  int m0 = blockIdx.y * 64, n0 = blockIdx.x * 64;
  int wr = wid >> 1, wc = wid & 1;
  f32x4 acc[2][2] = {};
  for (int pass = 0; pass < 2; ++pass) {
    const u16* a = pass ? A2 : A; const u16* b = pass ? B2 : B;
    int la = pass ? lda2 : lda, lb = pass ? ldb2 : ldb, kk = pass ? K2 : K;
    for (int k0 = 0; k0 < kk; k0 += 32) {
      {
        int r = tid >> 2, c = (tid & 3) * 8;
        *reinterpret_cast<bf16x8*>(&As[r][c]) = *reinterpret_cast<const bf16x8*>(a + (size_t)(m0 + r) * la + k0 + c);
        int kr = tid >> 3, nc = (tid & 7) * 8;
        bf16x8 t = *reinterpret_cast<const bf16x8*>(b + (size_t)(k0 + kr) * lb + n0 + nc);
        for (int j = 0; j < 8; ++j) Bs[nc + j][kr] = (u16)t[j];
      }
      __syncthreads();
      bf16x8 af[2], bfr[2];
      for (int i = 0; i < 2; ++i) {
        af[i] = *reinterpret_cast<const bf16x8*>(&As[wr * 32 + i * 16 + (lane & 15)][(lane >> 4) * 8]);
        bfr[i] = *reinterpret_cast<const bf16x8*>(&Bs[wc * 32 + i * 16 + (lane & 15)][(lane >> 4) * 8]);
      }
      for (int i = 0; i < 2; ++i) for (int j = 0; j < 2; ++j)
        acc[i][j] = __builtin_amdgcn_mfma_f32_16x16x32_bf16(af[i], bfr[j], acc[i][j], 0, 0, 0);
      __syncthreads();
    }
  }
  for (int i = 0; i < 2; ++i) for (int j = 0; j < 2; ++j) for (int r = 0; r < 4; ++r) {
    int row = m0 + wr * 32 + i * 16 + (lane >> 4) * 4 + r, col = n0 + wc * 32 + j * 16 + (lane & 15);
    float v = acc[i][j][r];
    if (MODE == 1) v = fmaxf(v, 0.f);
    if (MODE == 2) v += bf2f(res[(size_t)row * ldc + col]);
    C[(size_t)row * ldc + col] = f2bf(v);
  }
}

__global__ __launch_bounds__(256) void attn_kernel(const u16* __restrict__ qkv, u16* __restrict__ o) {
  int gw = blockIdx.x * 4 + (threadIdx.x >> 6), lane = threadIdx.x & 63;
  int q = gw >> 5, h = gw & 31;
  float qv[32];
  for (int d = 0; d < 32; ++d) qv[d] = bf2f(qkv[(size_t)q * 3072 + h * 32 + d]);
  float sc[4], mx = -1e30f;
  for (int i = 0; i < 4; ++i) {
    int j = q - 255 + i * 64 + lane;
    float s = -1e30f;
    if (j >= 0) { s = 0; for (int d = 0; d < 32; ++d) s += qv[d] * bf2f(qkv[(size_t)j * 3072 + 1024 + h * 32 + d]); s *= 0.17677669529663687f; }
    sc[i] = s; mx = fmaxf(mx, s);
  }
  for (int t = 32; t; t >>= 1) mx = fmaxf(mx, __shfl_xor(mx, t));
  float sum = 0;
  for (int i = 0; i < 4; ++i) { int j = q - 255 + i * 64 + lane; sc[i] = j >= 0 ? __expf(sc[i] - mx) : 0.f; sum += sc[i]; }
  for (int t = 32; t; t >>= 1) sum += __shfl_xor(sum, t);
  float inv = 1.f / sum;
  for (int d = 0; d < 32; ++d) {
    float a = 0;
    for (int i = 0; i < 4; ++i) { int j = q - 255 + i * 64 + lane; if (j >= 0) a += sc[i] * bf2f(qkv[(size_t)j * 3072 + 2048 + h * 32 + d]); }
    for (int t = 32; t; t >>= 1) a += __shfl_xor(a, t);
    if (lane == 0) o[(size_t)q * 1024 + h * 32 + d] = f2bf(a * inv);
  }
}

extern "C" void kernel_launch(void* const* d_in, const int* in_sizes, int n_in, void* d_out, int out_size, void* d_ws, size_t ws_size, hipStream_t stream) {
  const u16* x = (const u16*)d_in[0]; const u16* wqkv = (const u16*)d_in[1]; const u16* wo = (const u16*)d_in[2];
  const u16* w1 = (const u16*)d_in[4]; const u16* w2 = (const u16*)d_in[5];
  u16* out = (u16*)d_out;
  u16* xn = (u16*)d_ws; u16* qkv = xn + S * D; u16* hbuf = qkv + S * 3072; u16* o = hbuf + S * DFF;
  ln_kernel<<<S / 4, 256, 0, stream>>>(x, xn);
  gemm_kernel<0><<<dim3(3072 / 64, S / 64), 256, 0, stream>>>(xn, D, wqkv, 3072, D, nullptr, 0, nullptr, 0, 0, qkv, 3072, nullptr);
  gemm_kernel<1><<<dim3(DFF / 64, S / 64), 256, 0, stream>>>(xn, D, w1, DFF, D, nullptr, 0, nullptr, 0, 0, hbuf, DFF, nullptr);
  attn_kernel<<<S * H / 4, 256, 0, stream>>>(qkv, o);
  gemm_kernel<2><<<dim3(D / 64, S / 64), 256, 0, stream>>>(o, D, wo, D, D, hbuf, DFF, w2, D, DFF, out, D, x);
}
```

```cpp
#include <hip/hip_runtime.h>
#include <hip/hip_bf16.h>
#include <stdint.h>

typedef unsigned short u16;
typedef __attribute__((ext_vector_type(8))) short bf16x8;
typedef __attribute__((ext_vector_type(4))) float f32x4;

constexpr int S = 2048, D = 1024, H = 32, DH = 32, W = 256, DFF = 3584;

__device__ __forceinline__ float bf2f(u16 v) { return __uint_as_float(((unsigned)v) << 16); }
__device__ __forceinline__ u16 f2bf(float f) { __hip_bfloat16 b = __float2bfloat16(f); return *reinterpret_cast<u16*>(&b); }

__global__ __launch_bounds__(256) void ln_kernel(const u16* __restrict__ x, u16* __restrict__ xn) {
  int row = blockIdx.x * 4 + (threadIdx.x >> 6), lane = threadIdx.x & 63;
  const u16* xr = x + (size_t)row * D;
  float v[16];
  for (int i = 0; i < 2; ++i) {
    bf16x8 t = *reinterpret_cast<const bf16x8*>(xr + i * 512 + lane * 8);
    for (int j = 0; j < 8; ++j) v[i * 8 + j] = bf2f((u16)t[j]);
  }
  float s = 0;
  for (int j = 0; j < 16; ++j) s += v[j];
  for (int o = 32; o; o >>= 1) s += __shfl_xor(s, o);
  float m = s * (1.0f / D), q = 0;
  for (int j = 0; j < 16; ++j) { float d = v[j] - m; q += d * d; }
  for (int o = 32; o; o >>= 1) q += __shfl_xor(q, o);
  float r = rsqrtf(q * (1.0f / D) + 1e-5f);
  for (int i = 0; i < 2; ++i) {
    bf16x8 t;
    for (int j = 0; j < 8; ++j) t[j] = (short)f2bf((v[i * 8 + j] - m) * r);
    *reinterpret_cast<bf16x8*>(xn + (size_t)row * D + i * 512 + lane * 8) = t;
  }
}

template <int MODE>
__global__ __launch_bounds__(256) void gemm_kernel(const u16* __restrict__ A, int lda, const u16* __restrict__ B, int ldb, int K,
                                                   const u16* __restrict__ A2, int lda2, const u16* __restrict__ B2, int ldb2, int K2,
                                                   u16* __restrict__ C, int ldc, const u16* __restrict__ res) {
  __shared__ u16 As[64][40];
  __shared__ u16 Bs[64][40];
  int tid = threadIdx.x, lane = tid & 63, wid = tid >> 6;
  int m0 = blockIdx.y * 64, n0 = blockIdx.x * 64;
  int wr = wid >> 1, wc = wid & 1;
  f32x4 acc[2][2] = {};
  for (int pass = 0; pass < 2; ++pass) {
    const u16* a = pass ? A2 : A; const u16* b = pass ? B2 : B;
    int la = pass ? lda2 : lda, lb = pass ? ldb2 : ldb, kk = pass ? K2 : K;
    for (int k0 = 0; k0 < kk; k0 += 32) {
      {
        int r = tid >> 2, c = (tid & 3) * 8;
        *reinterpret_cast<bf16x8*>(&As[r][c]) = *reinterpret_cast<const bf16x8*>(a + (size_t)(m0 + r) * la + k0 + c);
        int kr = tid >> 3, nc = (tid & 7) * 8;
        bf16x8 t = *reinterpret_cast<const bf16x8*>(b + (size_t)(k0 + kr) * lb + n0 + nc);
        for (int j = 0; j < 8; ++j) Bs[nc + j][kr] = (u16)t[j];
      }
      __syncthreads();
      bf16x8 af[2], bfr[2];
      for (int i = 0; i < 2; ++i) {
        af[i] = *reinterpret_cast<const bf16x8*>(&As[wr * 32 + i * 16 + (lane & 15)][(lane >> 4) * 8]);
        bfr[i] = *reinterpret_cast<const bf16x8*>(&Bs[wc * 32 + i * 16 + (lane & 15)][(lane >> 4) * 8]);
      }
      for (int i = 0; i < 2; ++i) for (int j = 0; j < 2; ++j)
        acc[i][j] = __builtin_amdgcn_mfma_f32_16x16x32_bf16(af[i], bfr[j], acc[i][j], 0, 0, 0);
      __syncthreads();
    }
  }
  for (int i = 0; i < 2; ++i) for (int j = 0; j < 2; ++j) for (int r = 0; r < 4; ++r) {
    int row = m0 + wr * 32 + i * 16 + (lane >> 4) * 4 + r, col = n0 + wc * 32 + j * 16 + (lane & 15);
    float v = acc[i][j][r];
    if (MODE == 1) v = fmaxf(v, 0.f);
    if (MODE == 2) v += bf2f(res[(size_t)row * ldc + col]);
    C[(size_t)row * ldc + col] = f2bf(v);
  }
}

__global__ __launch_bounds__(256) void attn_kernel(const u16* __restrict__ qkv, u16* __restrict__ o) {
  int gw = blockIdx.x * 4 + (threadIdx.x >> 6), lane = threadIdx.x & 63;
  int q = gw >> 5, h = gw & 31;
  float qv[32];
  for (int d = 0; d < 32; ++d) qv[d] = bf2f(qkv[(size_t)q * 3072 + h * 32 + d]);
  float sc[4], mx = -1e30f;
  for (int i = 0; i < 4; ++i) {
    int j = q - 255 + i * 64 + lane;
    float s = -1e30f;
    if (j >= 0) { s = 0; for (int d = 0; d < 32; ++d) s += qv[d] * bf2f(qkv[(size_t)j * 3072 + 1024 + h * 32 + d]); s *= 0.17677669529663687f; }
    sc[i] = s; mx = fmaxf(mx, s);
  }
  for (int t = 32; t; t >>= 1) mx = fmaxf(mx, __shfl_xor(mx, t));
  float sum = 0;
  for (int i = 0; i < 4; ++i) { int j = q - 255 + i * 64 + lane; sc[i] = j >= 0 ? __expf(sc[i] - mx) : 0.f; sum += sc[i]; }
  for (int t = 32; t; t >>= 1) sum += __shfl_xor(sum, t);
  float inv = 1.f / sum;
  for (int d = 0; d < 32; ++d) {
    float a = 0;
    for (int i = 0; i < 4; ++i) { int j = q - 255 + i * 64 + lane; if (j >= 0) a += sc[i] * bf2f(qkv[(size_t)j * 3072 + 2048 + h * 32 + d]); }
    for (int t = 32; t; t >>= 1) a += __shfl_xor(a, t);
    if (lane == 0) o[(size_t)q * 1024 + h * 32 + d] = f2bf(a * inv);
  }
}


__global__ void probe_kernel(const char* p, int warm) {
  unsigned long long t0, t1; unsigned tot = 0, dummy = 0;
  for (int i = 0; i < 16; ++i) {
    const char* q = p + i * 131072;
    unsigned a;
    if (warm) { asm volatile("global_load_dword %0, %1, off\n\ts_waitcnt vmcnt(0)" : "=v"(a) : "v"(q) : "memory"); dummy ^= a; }
    q += 1024;
    asm volatile("s_memtime %0\n\ts_waitcnt lgkmcnt(0)" : "=s"(t0) :: "memory");
    asm volatile("global_load_dword %0, %1, off\n\ts_waitcnt vmcnt(0)" : "=v"(a) : "v"(q) : "memory"); dummy ^= a;
    asm volatile("s_memtime %0\n\ts_waitcnt lgkmcnt(0)" : "=s"(t1) :: "memory");
    tot += (unsigned)(t1 - t0);
  }
  asm volatile("" :: "v"(dummy));
  unsigned long long r0 = __builtin_amdgcn_s_memrealtime();
  unsigned long long lim = (unsigned long long)(tot / 16) * 100; if (lim > 500000) lim = 500000;
  while (__builtin_amdgcn_s_memrealtime() - r0 < lim) __builtin_amdgcn_s_sleep(8);
}

extern "C" void kernel_launch(void* const* d_in, const int* in_sizes, int n_in, void* d_out, int out_size, void* d_ws, size_t ws_size, hipStream_t stream) {
  const u16* x = (const u16*)d_in[0]; const u16* wqkv = (const u16*)d_in[1]; const u16* wo = (const u16*)d_in[2];
  const u16* w1 = (const u16*)d_in[4]; const u16* w2 = (const u16*)d_in[5];
  u16* out = (u16*)d_out;
  u16* xn = (u16*)d_ws; u16* qkv = xn + S * D; u16* hbuf = qkv + S * 3072; u16* o = hbuf + S * DFF;
  probe_kernel<<<1, 64, 0, stream>>>((const char*)(w2), 1);
  ln_kernel<<<S / 4, 256, 0, stream>>>(x, xn);
  gemm_kernel<0><<<dim3(3072 / 64, S / 64), 256, 0, stream>>>(xn, D, wqkv, 3072, D, nullptr, 0, nullptr, 0, 0, qkv, 3072, nullptr);
  gemm_kernel<1><<<dim3(DFF / 64, S / 64), 256, 0, stream>>>(xn, D, w1, DFF, D, nullptr, 0, nullptr, 0, 0, hbuf, DFF, nullptr);
  attn_kernel<<<S * H / 4, 256, 0, stream>>>(qkv, o);
  gemm_kernel<2><<<dim3(D / 64, S / 64), 256, 0, stream>>>(o, D, wo, D, D, hbuf, DFF, w2, D, DFF, out, D, x);
}
```

```cpp
#include <hip/hip_runtime.h>
#include <hip/hip_bf16.h>

__global__ __launch_bounds__(256, 1) void fused_block_kernel(const void* x, const void* wqkv, const void* wo, const void* w1,
                                                             const void* w2, void* out, void* ws) {
  __shared__ __attribute__((aligned(1024))) char lds[163840];
  const auto kargs = __builtin_amdgcn_kernarg_segment_ptr();
  const unsigned wave = __builtin_amdgcn_readfirstlane(threadIdx.x >> 6);
  asm volatile("" : : "s"((unsigned)(size_t)lds));
  asm volatile(
    "s_load_dwordx8 s[4:11], %0, 0\n"
    "s_load_dwordx4 s[12:15], %0, 32\n"
    "s_load_dwordx2 s[16:17], %0, 48\n"
    "s_mov_b32 s89, %1\n"
    "s_mov_b32 s50, %2\n"
    "v_mbcnt_lo_u32_b32 v207, -1, 0\n"
    "v_mbcnt_hi_u32_b32 v207, -1, v207\n"
    "s_getpc_b64 s[40:41]\n"
    ".Lpc0:\n"
    "s_and_b32 s41, s41, 0xffff\n"
    "s_mov_b32 s42, .Lend-.Lpc0\n"
    "s_mov_b32 s43, 0x20000\n"
    "s_lshr_b32 s90, s89, 3\n"
    "s_lshl_b32 s91, s90, 2\n"
    "s_add_u32 s76, s91, s50\n"
    "s_lshl_b32 s91, s76, 10\n"
    "v_lshlrev_b32 v240, 4, v207\n"
    "buffer_load_dwordx4 a[0:3], v240, s[40:43], s91 offen\n"
    "s_and_b32 s91, s89, 1\n"
    "s_bfe_u32 s51, s89, 0x20001\n"
    "s_lshr_b32 s92, s90, 2\n"
    "s_lshl_b32 s48, s91, 3\n"
    "s_add_u32 s48, s48, s92\n"
    "s_and_b32 s92, s90, 3\n"
    "s_lshl_b32 s49, s51, 2\n"
    "s_add_u32 s49, s49, s92\n"
    "v_and_b32 v249, 7, v207\n"
    "v_lshrrev_b32 v250, 3, v207\n"
    "v_lshlrev_b32 v251, 11, v249\n"
    "v_lshl_add_u32 v251, v250, 4, v251\n"
    "s_lshl_b32 s92, s48, 4\n"
    "s_add_u32 s92, s92, s49\n"
    "s_lshl_b32 s93, s92, 14\n"
    "s_lshl_b32 s94, s50, 9\n"
    "s_add_u32 s93, s93, s94\n"
    "s_mov_b32 s22, 0x400000\n"
    "s_mov_b32 s23, 0x20000\n"
    "s_waitcnt lgkmcnt(0)\n"
    "s_mov_b32 s20, s4\n"
    "s_and_b32 s21, s5, 0xffff\n"
    "buffer_load_dwordx4 v[0:3], v251, s[20:23], s93 offen\n"
    "buffer_load_dwordx4 v[4:7], v251, s[20:23], s93 offen offset:128\n"
    "buffer_load_dwordx4 v[8:11], v251, s[20:23], s93 offen offset:256\n"
    "buffer_load_dwordx4 v[12:15], v251, s[20:23], s93 offen offset:384\n"
    "s_lshr_b32 s52, s48, 1\n"
    "s_and_b32 s53, s90, 7\n"
    "s_lshr_b32 s54, s50, 1\n"
    "s_and_b32 s55, s50, 1\n"
    "s_lshl_b32 s91, s91, 7\n"
    "s_add_u32 s77, s91, s76\n"
    "s_mov_b32 s44, s16\n"
    "s_and_b32 s45, s17, 0xffff\n"
    "s_mov_b32 s46, 0x3200000\n"
    "s_mov_b32 s47, 0x20000\n"
    "s_mov_b32 s24, s6\n"
    "s_and_b32 s25, s7, 0xffff\n"
    "s_mov_b32 s26, 0x600000\n"
    "s_mov_b32 s27, 0x20000\n"
    "s_lshl_b32 s89, s48, 4\n"
    "v_add_u32 v120, s89, v207\n"
    "v_subrev_u32 v121, 18, v207\n"
    "v_cmp_le_i32 vcc, 0, v121\n"
    "s_nop 1\n"
    "v_addc_co_u32 v121, vcc, 0, v121, vcc\n"
    "v_add_u32 v121, s48, v121\n"
    "v_cmp_gt_u32 s[90:91], 16, v121\n"
    "v_lshlrev_b32 v121, 4, v121\n"
    "v_add_u32 v121, s49, v121\n"
    "v_subrev_u32 v122, 20, v207\n"
    "v_lshrrev_b32 v123, 2, v122\n"
    "v_and_b32 v122, 3, v122\n"
    "s_lshl_b32 s92, s52, 5\n"
    "s_lshl_b32 s93, s51, 2\n"
    "s_add_u32 s92, s92, s93\n"
    "v_lshl_add_u32 v122, v123, 4, v122\n"
    "v_add_u32 v122, s92, v122\n"
    "v_cmp_gt_u32 vcc, 20, v207\n"
    "s_nop 1\n"
    "v_cndmask_b32 v122, v122, v121, vcc\n"
    "v_cmp_gt_u32 vcc, 16, v207\n"
    "s_nop 1\n"
    "v_cndmask_b32 v122, v122, v120, vcc\n"
    "s_and_b32 s89, s90, 0xf0000\n"
    "s_or_b32 s89, s89, 0xff0ffff\n"
    "s_cmp_eq_u32 s50, 0\n"
    "s_cselect_b32 s80, s89, 0\n"
    "s_lshl_b32 s94, 1, s49\n"
    "s_andn2_b32 s91, s80, s94\n"
    "s_and_b32 s81, s91, 0xffff\n"
    "s_and_b32 s82, s80, 0x30000\n"
    "s_and_b32 s83, s80, 0xff00000\n"
    "s_and_b32 s92, s49, 3\n"
    "s_lshl_b32 s92, 0x1111, s92\n"
    "s_and_b32 s84, s81, s92\n"
    "s_or_b32 s84, s84, s83\n"
    "s_and_b32 s81, s81, 0xffff\n"
    "v_lshlrev_b32 v122, 7, v122\n"
    "v_add_u32 v122, 0x3000000, v122\n"
    "s_mov_b32 s90, s80\n"
    "s_mov_b32 s91, 0\n"
    "v_mov_b32 v123, 0x80000000\n"
    "v_cndmask_b32 v245, v123, v122, s[90:91]\n"
    "s_and_b32 s90, s80, s94\n"
    "v_cndmask_b32 v247, v123, v122, s[90:91]\n"
    "buffer_load_dword v244, v245, s[44:47], 0 offen sc1\n"
    "v_mov_b32 v248, 0x101\n"
    "s_mul_i32 s90, s55, 0x30000\n"
    "s_lshl_b32 s91, s49, 7\n"
    "s_add_u32 s90, s90, s91\n"
    "s_lshl_b32 s91, s54, 11\n"
    "s_add_u32 s90, s90, s91\n"
    "s_add_u32 s90, s90, 0x800\n"
    "s_lshl_b32 s85, s50, 12\n"
    "s_lshr_b32 s91, s48, 3\n"
    "s_mul_i32 s91, s91, 0x60000\n"
    "s_add_u32 s90, s90, s91\n"
    "s_lshl_b32 s91, s91, 1\n"
    "s_sub_u32 s99, 0x60000, s91\n"
    "s_add_u32 s93, s48, 0\n"
    "s_and_b32 s93, s93, 7\n"
    "s_mul_i32 s93, s93, 0xc0000\n"
    "s_add_u32 s56, s93, s90\n"
    "s_add_u32 s93, s48, 1\n"
    "s_and_b32 s93, s93, 7\n"
    "s_mul_i32 s93, s93, 0xc0000\n"
    "s_add_u32 s57, s93, s90\n"
    "s_add_u32 s93, s48, 2\n"
    "s_and_b32 s93, s93, 7\n"
    "s_mul_i32 s93, s93, 0xc0000\n"
    "s_add_u32 s58, s93, s90\n"
    "s_add_u32 s93, s48, 3\n"
    "s_and_b32 s93, s93, 7\n"
    "s_mul_i32 s93, s93, 0xc0000\n"
    "s_add_u32 s59, s93, s90\n"
    "s_add_u32 s93, s48, 4\n"
    "s_and_b32 s93, s93, 7\n"
    "s_mul_i32 s93, s93, 0xc0000\n"
    "s_add_u32 s60, s93, s90\n"
    "s_add_u32 s93, s48, 5\n"
    "s_and_b32 s93, s93, 7\n"
    "s_mul_i32 s93, s93, 0xc0000\n"
    "s_add_u32 s61, s93, s90\n"
    "s_add_u32 s93, s48, 6\n"
    "s_and_b32 s93, s93, 7\n"
    "s_mul_i32 s93, s93, 0xc0000\n"
    "s_add_u32 s62, s93, s90\n"
    "s_add_u32 s93, s48, 7\n"
    "s_and_b32 s93, s93, 7\n"
    "s_mul_i32 s93, s93, 0xc0000\n"
    "s_add_u32 s63, s93, s90\n"
    "v_lshrrev_b32 v249, 3, v207\n"
    "v_bfe_u32 v250, v207, 4, 1\n"
    "v_bfe_u32 v251, v207, 1, 2\n"
    "v_xor_b32 v250, v250, v251\n"
    "v_lshlrev_b32 v250, 5, v250\n"
    "v_and_b32 v251, 1, v207\n"
    "v_lshl_add_u32 v250, v251, 4, v250\n"
    "s_mov_b32 s89, 0x1800\n"
    "v_mad_u32_u24 v249, v249, s89, v250\n"
    "v_add_u32 v236, 0, v249\n"
    "v_add_u32 v237, 0xbc00, v249\n"
    "v_xor_b32 v237, 64, v237\n"
    "v_add_u32 v238, 0x17800, v249\n"
    "v_add_u32 v239, 0x23400, v249\n"
    "v_xor_b32 v239, 64, v239\n"
    "s_mov_b32 s40, s14\n"
    "s_and_b32 s41, s15, 0xffff\n"
    "s_mov_b32 s42, 0x400000\n"
    "s_mov_b32 s43, 0x20000\n"
    "s_lshl_b32 s89, s52, 3\n"
    "s_add_u32 s89, s89, s53\n"
    "s_lshl_b32 s89, s89, 18\n"
    "s_add_u32 s89, s89, s85\n"
    "s_add_u32 s14, s89, 0x2000000\n"
    "s_lshl_b32 s90, s51, 16\n"
    "s_add_u32 s15, s14, s90\n"
    "s_add_u32 m0, s85, 0\n"
    "s_nop 0\n"
    "buffer_load_dwordx4 v236, s[24:27], s56 offen lds\n"
    "buffer_load_dwordx4 v237, s[24:27], s56 offen offset:1024 lds\n"
    "buffer_load_dwordx4 v238, s[24:27], s56 offen offset:2048 lds\n"
    "buffer_load_dwordx4 v239, s[24:27], s56 offen offset:3072 lds\n"
    "s_mov_b32 s28, s10\n"
    "s_and_b32 s29, s11, 0xffff\n"
    "s_mov_b32 s30, 0x700000\n"
    "s_mov_b32 s31, 0x20000\n"
    "s_mov_b32 s32, s12\n"
    "s_and_b32 s33, s13, 0xffff\n"
    "s_mov_b32 s34, 0x700000\n"
    "s_mov_b32 s35, 0x20000\n"
    "s_mov_b32 s36, s8\n"
    "s_and_b32 s37, s9, 0xffff\n"
    "s_mov_b32 s38, 0x200000\n"
    "s_mov_b32 s39, 0x20000\n"
    "s_mov_b32 s13, s77\n"
    "s_lshr_b32 s91, s49, 1\n"
    "s_mul_i32 s91, s91, 0x380\n"
    "s_and_b32 s92, s49, 1\n"
    "s_mul_i32 s92, s92, 0x180\n"
    "s_add_u32 s91, s91, s92\n"
    "s_lshr_b32 s94, s48, 3\n"
    "s_lshl_b32 s92, s94, 1\n"
    "s_xor_b32 s92, s92, s50\n"
    "s_mul_i32 s92, s92, 0x38000\n"
    "s_add_u32 s91, s91, s92\n"
    "s_add_u32 s93, s48, 0\n"
    "s_and_b32 s93, s93, 7\n"
    "s_mul_i32 s93, s93, 0xe0000\n"
    "s_add_u32 s64, s93, s91\n"
    "s_add_u32 s93, s48, 1\n"
    "s_and_b32 s93, s93, 7\n"
    "s_mul_i32 s93, s93, 0xe0000\n"
    "s_add_u32 s65, s93, s91\n"
    "s_add_u32 s93, s48, 2\n"
    "s_and_b32 s93, s93, 7\n"
    "s_mul_i32 s93, s93, 0xe0000\n"
    "s_add_u32 s66, s93, s91\n"
    "s_add_u32 s93, s48, 3\n"
    "s_and_b32 s93, s93, 7\n"
    "s_mul_i32 s93, s93, 0xe0000\n"
    "s_add_u32 s67, s93, s91\n"
    "s_add_u32 s93, s48, 4\n"
    "s_and_b32 s93, s93, 7\n"
    "s_mul_i32 s93, s93, 0xe0000\n"
    "s_add_u32 s68, s93, s91\n"
    "s_add_u32 s93, s48, 5\n"
    "s_and_b32 s93, s93, 7\n"
    "s_mul_i32 s93, s93, 0xe0000\n"
    "s_add_u32 s69, s93, s91\n"
    "s_add_u32 s93, s48, 6\n"
    "s_and_b32 s93, s93, 7\n"
    "s_mul_i32 s93, s93, 0xe0000\n"
    "s_add_u32 s70, s93, s91\n"
    "s_add_u32 s93, s48, 7\n"
    "s_and_b32 s93, s93, 7\n"
    "s_mul_i32 s93, s93, 0xe0000\n"
    "s_add_u32 s71, s93, s91\n"
    "s_lshl_b32 s92, s94, 12\n"
    "v_add_u32 v252, s92, v240\n"
    "v_xor_b32 v253, 0x1000, v252\n"
    "s_lshl_b32 s92, s48, 2\n"
    "s_add_u32 s92, s92, s50\n"
    "s_lshl_b32 s91, s92, 16\n"
    "s_add_u32 s93, s48, 0\n"
    "s_and_b32 s93, s93, 7\n"
    "s_mul_i32 s93, s93, 0x2000\n"
    "s_add_u32 s72, s93, s91\n"
    "s_add_u32 s93, s48, 1\n"
    "s_and_b32 s93, s93, 7\n"
    "s_mul_i32 s93, s93, 0x2000\n"
    "s_add_u32 s73, s93, s91\n"
    "s_add_u32 s93, s48, 2\n"
    "s_and_b32 s93, s93, 7\n"
    "s_mul_i32 s93, s93, 0x2000\n"
    "s_add_u32 s74, s93, s91\n"
    "s_add_u32 s93, s48, 3\n"
    "s_and_b32 s93, s93, 7\n"
    "s_mul_i32 s93, s93, 0x2000\n"
    "s_add_u32 s75, s93, s91\n"
    "s_add_u32 s93, s48, 4\n"
    "s_and_b32 s93, s93, 7\n"
    "s_mul_i32 s93, s93, 0x2000\n"
    "s_add_u32 s76, s93, s91\n"
    "s_add_u32 s93, s48, 5\n"
    "s_and_b32 s93, s93, 7\n"
    "s_mul_i32 s93, s93, 0x2000\n"
    "s_add_u32 s77, s93, s91\n"
    "s_add_u32 s93, s48, 6\n"
    "s_and_b32 s93, s93, 7\n"
    "s_mul_i32 s93, s93, 0x2000\n"
    "s_add_u32 s78, s93, s91\n"
    "s_add_u32 s93, s48, 7\n"
    "s_and_b32 s93, s93, 7\n"
    "s_mul_i32 s93, s93, 0x2000\n"
    "s_add_u32 s79, s93, s91\n"
    "s_xor_b32 s19, s54, s94\n"
    "s_sub_u32 s19, s19, s94\n"
    "s_mul_i32 s19, s19, 0x60000\n"
    "s_lshl_b32 s89, s54, 11\n"
    "s_sub_u32 s19, s19, s89\n"
    "s_sub_u32 s19, s19, 0x800\n"
    "s_lshl_b32 s89, s49, 18\n"
    "s_lshl_b32 s90, s92, 11\n"
    "s_add_u32 s89, s89, s90\n"
    "s_add_u32 s86, s89, 0x400000\n"
    "s_add_u32 s87, s89, 0x800000\n"
    "s_lshl_b32 s89, s51, 3\n"
    "s_add_u32 s89, s89, s52\n"
    "s_lshl_b32 s89, s89, 2\n"
    "s_add_u32 s12, s89, s50\n"
    "s_mul_i32 s12, s12, 0x24000\n"
    "s_add_u32 s12, s12, 0xc00000\n"
    "s_and_b32 s90, s48, 1\n"
    "s_lshl_b32 s90, s90, 1\n"
    "s_add_u32 s90, s90, s54\n"
    "s_add_u32 s89, s89, s90\n"
    "s_mul_i32 s89, s89, 0x24000\n"
    "s_lshl_b32 s90, s55, 11\n"
    "s_add_u32 s89, s89, s90\n"
    "s_add_u32 s88, s89, 0xc00000\n"
    "s_bfe_u32 s89, s49, 0x10001\n"
    "s_mul_i32 s89, s89, 0xe000\n"
    "s_and_b32 s90, s49, 1\n"
    "s_mul_i32 s90, s90, 0x6000\n"
    "s_add_u32 s89, s89, s90\n"
    "s_add_u32 s16, s88, s89\n"
    "s_mov_b32 s17, 0x3e8293ee\n"
    "s_lshl_b32 s89, s52, 3\n"
    "s_add_u32 s89, s89, s53\n"
    "s_lshl_b32 s89, s89, 18\n"
    "s_add_u32 s89, s89, s85\n"
    "s_add_u32 s14, s89, 0x2000000\n"
    "s_lshl_b32 s90, s51, 16\n"
    "s_add_u32 s15, s14, s90\n"
    "v_lshrrev_b32 v249, 4, v207\n"
    "v_bfe_u32 v250, v207, 2, 2\n"
    "v_lshl_add_u32 v251, v249, 3, v250\n"
    "v_lshlrev_b32 v251, 7, v251\n"
    "v_lshrrev_b32 v250, 1, v250\n"
    "v_and_b32 v249, 1, v249\n"
    "v_lshl_add_u32 v250, v249, 1, v250\n"
    "v_lshl_add_u32 v251, v250, 5, v251\n"
    "v_and_b32 v249, 3, v207\n"
    "v_lshl_add_u32 v251, v249, 3, v251\n"
    "v_xor_b32 v224, 0, v251\n"
    "v_add_u32 v228, 0x10000, v224\n"
    "v_add_u32 v232, 0x20000, v224\n"
    "v_xor_b32 v225, 32, v251\n"
    "v_add_u32 v229, 0x10000, v225\n"
    "v_add_u32 v233, 0x20000, v225\n"
    "v_xor_b32 v226, 64, v251\n"
    "v_add_u32 v230, 0x10000, v226\n"
    "v_add_u32 v234, 0x20000, v226\n"
    "v_xor_b32 v227, 0x60, v251\n"
    "v_add_u32 v231, 0x10000, v227\n"
    "v_add_u32 v235, 0x20000, v227\n"
    "v_add_u32 v241, 0x1000, v240\n"
    "s_lshl_b32 s89, s52, 3\n"
    "s_add_u32 s89, s89, s53\n"
    "s_lshl_b32 s89, s89, 11\n"
    "s_lshl_b32 s90, s50, 7\n"
    "s_add_u32 s89, s89, s90\n"
    "s_add_u32 s89, s89, 0x3010000\n"
    "v_add_u32 v249, s51, v207\n"
    "v_and_b32 v249, 3, v249\n"
    "v_lshlrev_b32 v249, 9, v249\n"
    "v_lshl_add_u32 v249, v207, 2, v249\n"
    "v_add_u32 v205, s89, v249\n"
    "s_lshl_b32 s90, s51, 9\n"
    "s_add_u32 s89, s89, s90\n"
    "v_mov_b32 v249, s89\n"
    "v_mov_b32 v250, 0x80000000\n"
    "v_cmp_eq_u32 vcc, 0, v207\n"
    "s_nop 1\n"
    "v_cndmask_b32 v243, v250, v249, vcc\n"
    "v_mov_b32 v124, 0\n"
    "v_mov_b32 v125, 0\n"
    "v_mov_b32 v126, 0\n"
    "v_mov_b32 v127, 0\n"
    "buffer_store_dwordx4 v[124:127], v243, s[44:47], 0 offen sc1\n"
    "v_lshrrev_b32 v62, 5, v207\n"
    "v_bfe_u32 v251, v207, 3, 2\n"
    "v_and_b32 v60, 7, v207\n"
    "v_lshlrev_b32 v62, 11, v62\n"
    "v_lshl_add_u32 v62, v251, 8, v62\n"
    "v_lshl_add_u32 v62, v60, 4, v62\n"
    "s_lshr_b32 s91, s49, 2\n"
    "s_lshl_b32 s90, s48, 2\n"
    "s_add_u32 s91, s91, s90\n"
    "s_lshl_b32 s91, s91, 16\n"
    "s_lshl_b32 s90, s50, 14\n"
    "s_add_u32 s91, s91, s90\n"
    "s_bfe_u32 s90, s49, 0x10001\n"
    "s_lshl_b32 s90, s90, 10\n"
    "s_add_u32 s91, s91, s90\n"
    "s_and_b32 s90, s49, 1\n"
    "s_lshl_b32 s90, s90, 7\n"
    "s_add_u32 s91, s91, s90\n"
    "v_mov_b32 v61, 0x3f803f80\n"
    "s_waitcnt vmcnt(6)\n"
    "v_dot2_f32_bf16 v48, v0, v61, 0\n"
    "v_dot2_f32_bf16 v52, v0, v0, 0\n"
    "v_dot2_f32_bf16 v49, v1, v61, 0\n"
    "v_dot2_f32_bf16 v53, v1, v1, 0\n"
    "v_dot2_f32_bf16 v48, v2, v61, v48\n"
    "v_dot2_f32_bf16 v52, v2, v2, v52\n"
    "v_dot2_f32_bf16 v49, v3, v61, v49\n"
    "v_dot2_f32_bf16 v53, v3, v3, v53\n"
    "v_dot2_f32_bf16 v48, v4, v61, v48\n"
    "v_dot2_f32_bf16 v52, v4, v4, v52\n"
    "v_dot2_f32_bf16 v49, v5, v61, v49\n"
    "v_dot2_f32_bf16 v53, v5, v5, v53\n"
    "v_dot2_f32_bf16 v48, v6, v61, v48\n"
    "v_dot2_f32_bf16 v52, v6, v6, v52\n"
    "v_dot2_f32_bf16 v49, v7, v61, v49\n"
    "v_dot2_f32_bf16 v53, v7, v7, v53\n"
    "v_dot2_f32_bf16 v48, v8, v61, v48\n"
    "v_dot2_f32_bf16 v52, v8, v8, v52\n"
    "v_dot2_f32_bf16 v49, v9, v61, v49\n"
    "v_dot2_f32_bf16 v53, v9, v9, v53\n"
    "v_dot2_f32_bf16 v48, v10, v61, v48\n"
    "v_dot2_f32_bf16 v52, v10, v10, v52\n"
    "v_dot2_f32_bf16 v49, v11, v61, v49\n"
    "v_dot2_f32_bf16 v53, v11, v11, v53\n"
    "v_dot2_f32_bf16 v48, v12, v61, v48\n"
    "v_dot2_f32_bf16 v52, v12, v12, v52\n"
    "v_dot2_f32_bf16 v49, v13, v61, v49\n"
    "v_dot2_f32_bf16 v53, v13, v13, v53\n"
    "v_dot2_f32_bf16 v48, v14, v61, v48\n"
    "v_dot2_f32_bf16 v52, v14, v14, v52\n"
    "v_dot2_f32_bf16 v49, v15, v61, v49\n"
    "v_dot2_f32_bf16 v53, v15, v15, v53\n"
    "v_and_b32 v249, 7, v207\n"
    "v_lshlrev_b32 v249, 5, v249\n"
    "v_lshlrev_b32 v16, 16, v0\n"
    "v_add_f32 v56, v48, v49\n"
    "v_add_f32 v57, v52, v53\n"
    "v_and_b32 v17, 0xffff0000, v0\n"
    "v_lshlrev_b32 v18, 16, v1\n"
    "v_add_f32_dpp v58, v56, v56 row_ror:8 row_mask:0xf bank_mask:0xf\n"
    "v_add_f32_dpp v59, v57, v57 row_ror:8 row_mask:0xf bank_mask:0xf\n"
    "v_mov_b32 v56, v58\n"
    "v_mov_b32 v57, v59\n"
    "v_add_u32 v249, 0x27f00, v249\n"
    "v_permlane16_swap_b32 v56, v58\n"
    "v_permlane16_swap_b32 v57, v59\n"
    "v_add_f32 v58, v56, v58\n"
    "v_add_f32 v59, v57, v59\n"
    "v_mov_b32 v56, v58\n"
    "v_mov_b32 v57, v59\n"
    "s_lshl_b32 s89, s50, 3\n"
    "v_permlane32_swap_b32 v56, v58\n"
    "v_permlane32_swap_b32 v57, v59\n"
    "v_add_f32 v58, v56, v58\n"
    "v_add_f32 v59, v57, v59\n"
    "v_add_u32 v250, s89, v249\n"
    "ds_write_b64 v250, v[58:59]\n"
    "s_waitcnt vmcnt(5)\n"
    "v_lshrrev_b32 v63, 8, v244\n"
    "v_add_u32 v63, 1, v63\n"
    "v_lshlrev_b32 v63, 8, v63\n"
    "buffer_store_dword v63, v247, s[44:47], 0 offen sc1\n"
    "v_and_b32 v19, 0xffff0000, v1\n"
    "v_lshlrev_b32 v20, 16, v2\n"
    "v_and_b32 v21, 0xffff0000, v2\n"
    "v_lshlrev_b32 v22, 16, v3\n"
    "v_and_b32 v23, 0xffff0000, v3\n"
    "v_lshlrev_b32 v24, 16, v4\n"
    "s_waitcnt lgkmcnt(0)\n"
    "s_barrier\n"
    "ds_read_b128 v[48:51], v249\n"
    "ds_read_b128 v[52:55], v249 offset:16\n"
    "v_and_b32 v25, 0xffff0000, v4\n"
    "v_lshlrev_b32 v26, 16, v5\n"
    "v_and_b32 v27, 0xffff0000, v5\n"
    "v_lshlrev_b32 v28, 16, v6\n"
    "v_and_b32 v29, 0xffff0000, v6\n"
    "v_lshlrev_b32 v30, 16, v7\n"
    "v_and_b32 v31, 0xffff0000, v7\n"
    "v_lshlrev_b32 v32, 16, v8\n"
    "v_and_b32 v33, 0xffff0000, v8\n"
    "v_lshlrev_b32 v34, 16, v9\n"
    "v_and_b32 v35, 0xffff0000, v9\n"
    "v_lshlrev_b32 v36, 16, v10\n"
    "v_and_b32 v37, 0xffff0000, v10\n"
    "v_lshlrev_b32 v38, 16, v11\n"
    "v_and_b32 v39, 0xffff0000, v11\n"
    "v_lshlrev_b32 v40, 16, v12\n"
    "v_and_b32 v41, 0xffff0000, v12\n"
    "v_lshlrev_b32 v42, 16, v13\n"
    "v_and_b32 v43, 0xffff0000, v13\n"
    "v_lshlrev_b32 v44, 16, v14\n"
    "v_and_b32 v45, 0xffff0000, v14\n"
    "v_lshlrev_b32 v46, 16, v15\n"
    "v_and_b32 v47, 0xffff0000, v15\n"
    "s_waitcnt lgkmcnt(0)\n"
    "v_pk_add_f32 v[48:49], v[48:49], v[50:51]\n"
    "v_pk_add_f32 v[52:53], v[52:53], v[54:55]\n"
    "v_pk_add_f32 v[48:49], v[48:49], v[52:53]\n"
    "v_mul_f32 v48, 0x3a800000, v48\n"
    "v_mul_f32 v49, 0x3a800000, v49\n"
    "v_fma_f32 v49, -v48, v48, v49\n"
    "v_max_f32 v49, 0, v49\n"
    "v_add_f32 v49, 0x3727c5ac, v49\n"
    "v_rsq_f32 v50, v49\n"
    "v_sub_f32 v48, 0, v48\n"
    "v_mul_f32 v52, v48, v50\n"
    "v_pk_fma_f32 v[16:17], v[16:17], v[50:51], v[52:53] op_sel_hi:[1,0,0]\n"
    "v_pk_fma_f32 v[18:19], v[18:19], v[50:51], v[52:53] op_sel_hi:[1,0,0]\n"
    "v_pk_fma_f32 v[20:21], v[20:21], v[50:51], v[52:53] op_sel_hi:[1,0,0]\n"
    "v_pk_fma_f32 v[22:23], v[22:23], v[50:51], v[52:53] op_sel_hi:[1,0,0]\n"
    "v_cvt_pk_bf16_f32 v0, v16, v17\n"
    "v_cvt_pk_bf16_f32 v1, v18, v19\n"
    "v_cvt_pk_bf16_f32 v2, v20, v21\n"
    "v_cvt_pk_bf16_f32 v3, v22, v23\n"
    "buffer_store_dwordx4 v[0:3], v62, s[44:47], s91 offen sc1\n"
    "v_pk_fma_f32 v[24:25], v[24:25], v[50:51], v[52:53] op_sel_hi:[1,0,0]\n"
    "v_pk_fma_f32 v[26:27], v[26:27], v[50:51], v[52:53] op_sel_hi:[1,0,0]\n"
    "v_pk_fma_f32 v[28:29], v[28:29], v[50:51], v[52:53] op_sel_hi:[1,0,0]\n"
    "v_pk_fma_f32 v[30:31], v[30:31], v[50:51], v[52:53] op_sel_hi:[1,0,0]\n"
    "v_cvt_pk_bf16_f32 v4, v24, v25\n"
    "v_cvt_pk_bf16_f32 v5, v26, v27\n"
    "v_cvt_pk_bf16_f32 v6, v28, v29\n"
    "v_cvt_pk_bf16_f32 v7, v30, v31\n"
    "s_add_u32 s91, s91, 0x1000\n"
    "buffer_store_dwordx4 v[4:7], v62, s[44:47], s91 offen sc1\n"
    "v_pk_fma_f32 v[32:33], v[32:33], v[50:51], v[52:53] op_sel_hi:[1,0,0]\n"
    "v_pk_fma_f32 v[34:35], v[34:35], v[50:51], v[52:53] op_sel_hi:[1,0,0]\n"
    "v_pk_fma_f32 v[36:37], v[36:37], v[50:51], v[52:53] op_sel_hi:[1,0,0]\n"
    "v_pk_fma_f32 v[38:39], v[38:39], v[50:51], v[52:53] op_sel_hi:[1,0,0]\n"
    "v_cvt_pk_bf16_f32 v8, v32, v33\n"
    "v_cvt_pk_bf16_f32 v9, v34, v35\n"
    "v_cvt_pk_bf16_f32 v10, v36, v37\n"
    "v_cvt_pk_bf16_f32 v11, v38, v39\n"
    "s_add_u32 s91, s91, 0x1000\n"
    "buffer_store_dwordx4 v[8:11], v62, s[44:47], s91 offen sc1\n"
    "v_pk_fma_f32 v[40:41], v[40:41], v[50:51], v[52:53] op_sel_hi:[1,0,0]\n"
    "v_pk_fma_f32 v[42:43], v[42:43], v[50:51], v[52:53] op_sel_hi:[1,0,0]\n"
    "v_pk_fma_f32 v[44:45], v[44:45], v[50:51], v[52:53] op_sel_hi:[1,0,0]\n"
    "v_pk_fma_f32 v[46:47], v[46:47], v[50:51], v[52:53] op_sel_hi:[1,0,0]\n"
    "v_cvt_pk_bf16_f32 v12, v40, v41\n"
    "v_cvt_pk_bf16_f32 v13, v42, v43\n"
    "v_cvt_pk_bf16_f32 v14, v44, v45\n"
    "v_cvt_pk_bf16_f32 v15, v46, v47\n"
    "s_add_u32 s91, s91, 0x1000\n"
    "buffer_store_dwordx4 v[12:15], v62, s[44:47], s91 offen sc1\n"
    "s_add_u32 m0, s85, 0x4000\n"
    "s_add_u32 s98, s56, s99\n"
    "buffer_load_dwordx4 v236, s[24:27], s98 offen lds\n"
    "buffer_load_dwordx4 v237, s[24:27], s98 offen offset:1024 lds\n"
    "buffer_load_dwordx4 v238, s[24:27], s98 offen offset:2048 lds\n"
    "buffer_load_dwordx4 v239, s[24:27], s98 offen offset:3072 lds\n"
    "s_waitcnt vmcnt(4)\n"
    "s_barrier\n"
    "buffer_atomic_add v248, v247, s[44:47], 0 offen\n"
    "s_add_u32 m0, s85, 0x8000\n"
    "s_nop 0\n"
    "buffer_load_dwordx4 v236, s[24:27], s57 offen lds\n"
    "buffer_load_dwordx4 v237, s[24:27], s57 offen offset:1024 lds\n"
    "buffer_load_dwordx4 v238, s[24:27], s57 offen offset:2048 lds\n"
    "buffer_load_dwordx4 v239, s[24:27], s57 offen offset:3072 lds\n"
    "s_add_u32 m0, s85, 0xc000\n"
    "s_add_u32 s98, s57, s99\n"
    "buffer_load_dwordx4 v236, s[24:27], s98 offen lds\n"
    "buffer_load_dwordx4 v237, s[24:27], s98 offen offset:1024 lds\n"
    "buffer_load_dwordx4 v238, s[24:27], s98 offen offset:2048 lds\n"
    "buffer_load_dwordx4 v239, s[24:27], s98 offen offset:3072 lds\n"
    "s_add_u32 m0, s85, 0x10000\n"
    "s_nop 0\n"
    "buffer_load_dwordx4 v236, s[24:27], s58 offen lds\n"
    "buffer_load_dwordx4 v237, s[24:27], s58 offen offset:1024 lds\n"
    "buffer_load_dwordx4 v238, s[24:27], s58 offen offset:2048 lds\n"
    "buffer_load_dwordx4 v239, s[24:27], s58 offen offset:3072 lds\n"
    "s_add_u32 m0, s85, 0x14000\n"
    "s_add_u32 s98, s58, s99\n"
    "buffer_load_dwordx4 v236, s[24:27], s98 offen lds\n"
    "buffer_load_dwordx4 v237, s[24:27], s98 offen offset:1024 lds\n"
    "buffer_load_dwordx4 v238, s[24:27], s98 offen offset:2048 lds\n"
    "buffer_load_dwordx4 v239, s[24:27], s98 offen offset:3072 lds\n"
    "s_add_u32 m0, s85, 0x18000\n"
    "s_nop 0\n"
    "buffer_load_dwordx4 v236, s[24:27], s59 offen lds\n"
    "buffer_load_dwordx4 v237, s[24:27], s59 offen offset:1024 lds\n"
    "buffer_load_dwordx4 v238, s[24:27], s59 offen offset:2048 lds\n"
    "buffer_load_dwordx4 v239, s[24:27], s59 offen offset:3072 lds\n"
    "s_add_u32 m0, s85, 0x1c000\n"
    "s_add_u32 s98, s59, s99\n"
    "buffer_load_dwordx4 v236, s[24:27], s98 offen lds\n"
    "buffer_load_dwordx4 v237, s[24:27], s98 offen offset:1024 lds\n"
    "buffer_load_dwordx4 v238, s[24:27], s98 offen offset:2048 lds\n"
    "buffer_load_dwordx4 v239, s[24:27], s98 offen offset:3072 lds\n"
    "s_add_u32 m0, s85, 0x20000\n"
    "s_nop 0\n"
    "buffer_load_dwordx4 v236, s[24:27], s60 offen lds\n"
    "buffer_load_dwordx4 v237, s[24:27], s60 offen offset:1024 lds\n"
    "buffer_load_dwordx4 v238, s[24:27], s60 offen offset:2048 lds\n"
    "buffer_load_dwordx4 v239, s[24:27], s60 offen offset:3072 lds\n"
    "buffer_load_dword v246, v245, s[44:47], 0 offen sc1\n"
    "s_waitcnt vmcnt(0)\n"
    "v_xor_b32 v249, v246, v244\n"
    "v_and_b32 v250, 63, v246\n"
    "v_cmp_lt_u32 vcc, 0xff, v249\n"
    "v_cmp_le_u32 s[96:97], 1, v250\n"
    "s_and_b32 s96, vcc_lo, s96\n"
    "s_andn2_b32 s96, s81, s96\n"
    "s_cbranch_scc1 .Lslow_xn\n"
    ".Lret_xn:\n"
    "s_barrier\n"
    "buffer_load_dwordx4 a[0:3], v252, s[44:47], s72 offen sc1\n"
    "buffer_load_dwordx4 a[4:7], v252, s[44:47], s72 offen offset:1024 sc1\n"
    "buffer_load_dwordx4 a[8:11], v252, s[44:47], s72 offen offset:2048 sc1\n"
    "buffer_load_dwordx4 a[12:15], v252, s[44:47], s72 offen offset:3072 sc1\n"
    "buffer_load_dwordx4 a[16:19], v253, s[44:47], s72 offen sc1\n"
    "buffer_load_dwordx4 a[20:23], v253, s[44:47], s72 offen offset:1024 sc1\n"
    "buffer_load_dwordx4 a[24:27], v253, s[44:47], s72 offen offset:2048 sc1\n"
    "buffer_load_dwordx4 a[28:31], v253, s[44:47], s72 offen offset:3072 sc1\n"
    "ds_read_b64_tr_b16 v[160:161], v224\n"
    "ds_read_b64_tr_b16 v[162:163], v224 offset:512\n"
    "ds_read_b64_tr_b16 v[164:165], v225\n"
    "ds_read_b64_tr_b16 v[166:167], v225 offset:512\n"
    "ds_read_b64_tr_b16 v[168:169], v226\n"
    "ds_read_b64_tr_b16 v[170:171], v226 offset:512\n"
    "ds_read_b64_tr_b16 v[172:173], v227\n"
    "ds_read_b64_tr_b16 v[174:175], v227 offset:512\n"
    "ds_read_b64_tr_b16 v[176:177], v224 offset:8192\n"
    "ds_read_b64_tr_b16 v[178:179], v224 offset:8704\n"
    "ds_read_b64_tr_b16 v[180:181], v225 offset:8192\n"
    "ds_read_b64_tr_b16 v[182:183], v225 offset:8704\n"
    "ds_read_b64_tr_b16 v[184:185], v226 offset:8192\n"
    "ds_read_b64_tr_b16 v[186:187], v226 offset:8704\n"
    "s_waitcnt vmcnt(6) lgkmcnt(10)\n"
    "v_mfma_f32_16x16x32_bf16 v[0:3], v[160:163], a[0:3], 0\n"
    "ds_read_b64_tr_b16 v[188:189], v227 offset:8192\n"
    "buffer_load_dwordx4 a[32:35], v252, s[44:47], s73 offen sc1\n"
    "v_mfma_f32_16x16x32_bf16 v[16:19], v[160:163], a[4:7], 0\n"
    "ds_read_b64_tr_b16 v[190:191], v227 offset:8704\n"
    "v_mfma_f32_16x16x32_bf16 v[20:23], v[164:167], a[4:7], 0\n"
    "ds_read_b64_tr_b16 v[160:161], v224 offset:4096\n"
    "v_mfma_f32_16x16x32_bf16 v[4:7], v[164:167], a[0:3], 0\n"
    "ds_read_b64_tr_b16 v[162:163], v224 offset:4608\n"
    "s_waitcnt lgkmcnt(10)\n"
    "v_mfma_f32_16x16x32_bf16 v[8:11], v[168:171], a[0:3], 0\n"
    "ds_read_b64_tr_b16 v[164:165], v225 offset:4096\n"
    "v_mfma_f32_16x16x32_bf16 v[24:27], v[168:171], a[4:7], 0\n"
    "ds_read_b64_tr_b16 v[166:167], v225 offset:4608\n"
    "buffer_load_dwordx4 a[36:39], v252, s[44:47], s73 offen offset:1024 sc1\n"
    "v_mfma_f32_16x16x32_bf16 v[28:31], v[172:175], a[4:7], 0\n"
    "ds_read_b64_tr_b16 v[168:169], v226 offset:4096\n"
    "v_mfma_f32_16x16x32_bf16 v[12:15], v[172:175], a[0:3], 0\n"
    "ds_read_b64_tr_b16 v[170:171], v226 offset:4608\n"
    "s_waitcnt lgkmcnt(10)\n"
    "v_mfma_f32_16x16x32_bf16 v[32:35], a[0:3], v[176:179], 0\n"
    "ds_read_b64_tr_b16 v[172:173], v227 offset:4096\n"
    "v_mfma_f32_16x16x32_bf16 v[48:51], a[4:7], v[176:179], 0\n"
    "ds_read_b64_tr_b16 v[174:175], v227 offset:4608\n"
    "v_mfma_f32_16x16x32_bf16 v[52:55], a[4:7], v[180:183], 0\n"
    "ds_read_b64_tr_b16 v[176:177], v224 offset:12288\n"
    "buffer_load_dwordx4 a[40:43], v252, s[44:47], s73 offen offset:2048 sc1\n"
    "v_mfma_f32_16x16x32_bf16 v[36:39], a[0:3], v[180:183], 0\n"
    "ds_read_b64_tr_b16 v[178:179], v224 offset:12800\n"
    "s_waitcnt lgkmcnt(10)\n"
    "v_mfma_f32_16x16x32_bf16 v[40:43], a[0:3], v[184:187], 0\n"
    "ds_read_b64_tr_b16 v[180:181], v225 offset:12288\n"
    "v_mfma_f32_16x16x32_bf16 v[56:59], a[4:7], v[184:187], 0\n"
    "ds_read_b64_tr_b16 v[182:183], v225 offset:12800\n"
    "v_mfma_f32_16x16x32_bf16 v[60:63], a[4:7], v[188:191], 0\n"
    "ds_read_b64_tr_b16 v[184:185], v226 offset:12288\n"
    "v_mfma_f32_16x16x32_bf16 v[44:47], a[0:3], v[188:191], 0\n"
    "ds_read_b64_tr_b16 v[186:187], v226 offset:12800\n"
    "s_waitcnt vmcnt(7) lgkmcnt(10)\n"
    "v_mfma_f32_16x16x32_bf16 v[0:3], v[160:163], a[8:11], v[0:3]\n"
    "ds_read_b64_tr_b16 v[188:189], v227 offset:12288\n"
    "buffer_load_dwordx4 a[44:47], v252, s[44:47], s73 offen offset:3072 sc1\n"
    "v_mfma_f32_16x16x32_bf16 v[16:19], v[160:163], a[12:15], v[16:19]\n"
    "ds_read_b64_tr_b16 v[190:191], v227 offset:12800\n"
    "v_mfma_f32_16x16x32_bf16 v[20:23], v[164:167], a[12:15], v[20:23]\n"
    "ds_read_b64_tr_b16 v[160:161], v224 offset:16384\n"
    "v_mfma_f32_16x16x32_bf16 v[4:7], v[164:167], a[8:11], v[4:7]\n"
    "ds_read_b64_tr_b16 v[162:163], v224 offset:16896\n"
    "s_waitcnt lgkmcnt(10)\n"
    "v_mfma_f32_16x16x32_bf16 v[8:11], v[168:171], a[8:11], v[8:11]\n"
    "ds_read_b64_tr_b16 v[164:165], v225 offset:16384\n"
    "v_mfma_f32_16x16x32_bf16 v[24:27], v[168:171], a[12:15], v[24:27]\n"
    "ds_read_b64_tr_b16 v[166:167], v225 offset:16896\n"
    "buffer_load_dwordx4 a[48:51], v253, s[44:47], s73 offen sc1\n"
    "v_mfma_f32_16x16x32_bf16 v[28:31], v[172:175], a[12:15], v[28:31]\n"
    "ds_read_b64_tr_b16 v[168:169], v226 offset:16384\n"
    "v_mfma_f32_16x16x32_bf16 v[12:15], v[172:175], a[8:11], v[12:15]\n"
    "ds_read_b64_tr_b16 v[170:171], v226 offset:16896\n"
    "s_waitcnt lgkmcnt(10)\n"
    "v_mfma_f32_16x16x32_bf16 v[32:35], a[8:11], v[176:179], v[32:35]\n"
    "ds_read_b64_tr_b16 v[172:173], v227 offset:16384\n"
    "v_mfma_f32_16x16x32_bf16 v[48:51], a[12:15], v[176:179], v[48:51]\n"
    "ds_read_b64_tr_b16 v[174:175], v227 offset:16896\n"
    "v_mfma_f32_16x16x32_bf16 v[52:55], a[12:15], v[180:183], v[52:55]\n"
    "ds_read_b64_tr_b16 v[176:177], v224 offset:24576\n"
    "buffer_load_dwordx4 a[52:55], v253, s[44:47], s73 offen offset:1024 sc1\n"
    "v_mfma_f32_16x16x32_bf16 v[36:39], a[8:11], v[180:183], v[36:39]\n"
    "ds_read_b64_tr_b16 v[178:179], v224 offset:25088\n"
    "s_waitcnt lgkmcnt(10)\n"
    "v_mfma_f32_16x16x32_bf16 v[40:43], a[8:11], v[184:187], v[40:43]\n"
    "ds_read_b64_tr_b16 v[180:181], v225 offset:24576\n"
    "v_mfma_f32_16x16x32_bf16 v[56:59], a[12:15], v[184:187], v[56:59]\n"
    "ds_read_b64_tr_b16 v[182:183], v225 offset:25088\n"
    "v_mfma_f32_16x16x32_bf16 v[60:63], a[12:15], v[188:191], v[60:63]\n"
    "ds_read_b64_tr_b16 v[184:185], v226 offset:24576\n"
    "v_mfma_f32_16x16x32_bf16 v[44:47], a[8:11], v[188:191], v[44:47]\n"
    "ds_read_b64_tr_b16 v[186:187], v226 offset:25088\n"
    "s_waitcnt vmcnt(8) lgkmcnt(10)\n"
    "v_mfma_f32_16x16x32_bf16 v[0:3], v[160:163], a[16:19], v[0:3]\n"
    "ds_read_b64_tr_b16 v[188:189], v227 offset:24576\n"
    "buffer_load_dwordx4 a[56:59], v253, s[44:47], s73 offen offset:2048 sc1\n"
    "v_mfma_f32_16x16x32_bf16 v[16:19], v[160:163], a[20:23], v[16:19]\n"
    "ds_read_b64_tr_b16 v[190:191], v227 offset:25088\n"
    "v_mfma_f32_16x16x32_bf16 v[20:23], v[164:167], a[20:23], v[20:23]\n"
    "ds_read_b64_tr_b16 v[160:161], v224 offset:20480\n"
    "v_mfma_f32_16x16x32_bf16 v[4:7], v[164:167], a[16:19], v[4:7]\n"
    "ds_read_b64_tr_b16 v[162:163], v224 offset:20992\n"
    "s_waitcnt lgkmcnt(10)\n"
    "v_mfma_f32_16x16x32_bf16 v[8:11], v[168:171], a[16:19], v[8:11]\n"
    "ds_read_b64_tr_b16 v[164:165], v225 offset:20480\n"
    "v_mfma_f32_16x16x32_bf16 v[24:27], v[168:171], a[20:23], v[24:27]\n"
    "ds_read_b64_tr_b16 v[166:167], v225 offset:20992\n"
    "buffer_load_dwordx4 a[60:63], v253, s[44:47], s73 offen offset:3072 sc1\n"
    "v_mfma_f32_16x16x32_bf16 v[28:31], v[172:175], a[20:23], v[28:31]\n"
    "ds_read_b64_tr_b16 v[168:169], v226 offset:20480\n"
    "v_mfma_f32_16x16x32_bf16 v[12:15], v[172:175], a[16:19], v[12:15]\n"
    "ds_read_b64_tr_b16 v[170:171], v226 offset:20992\n"
    "s_waitcnt lgkmcnt(10)\n"
    "v_mfma_f32_16x16x32_bf16 v[32:35], a[16:19], v[176:179], v[32:35]\n"
    "ds_read_b64_tr_b16 v[172:173], v227 offset:20480\n"
    "v_mfma_f32_16x16x32_bf16 v[48:51], a[20:23], v[176:179], v[48:51]\n"
    "ds_read_b64_tr_b16 v[174:175], v227 offset:20992\n"
    "v_mfma_f32_16x16x32_bf16 v[52:55], a[20:23], v[180:183], v[52:55]\n"
    "ds_read_b64_tr_b16 v[176:177], v224 offset:28672\n"
    "buffer_load_dwordx4 a[64:67], v252, s[44:47], s74 offen sc1\n"
    "v_mfma_f32_16x16x32_bf16 v[36:39], a[16:19], v[180:183], v[36:39]\n"
    "ds_read_b64_tr_b16 v[178:179], v224 offset:29184\n"
    "s_waitcnt lgkmcnt(10)\n"
    "v_mfma_f32_16x16x32_bf16 v[40:43], a[16:19], v[184:187], v[40:43]\n"
    "ds_read_b64_tr_b16 v[180:181], v225 offset:28672\n"
    "v_mfma_f32_16x16x32_bf16 v[56:59], a[20:23], v[184:187], v[56:59]\n"
    "ds_read_b64_tr_b16 v[182:183], v225 offset:29184\n"
    "v_mfma_f32_16x16x32_bf16 v[60:63], a[20:23], v[188:191], v[60:63]\n"
    "ds_read_b64_tr_b16 v[184:185], v226 offset:28672\n"
    "v_mfma_f32_16x16x32_bf16 v[44:47], a[16:19], v[188:191], v[44:47]\n"
    "ds_read_b64_tr_b16 v[186:187], v226 offset:29184\n"
    "s_waitcnt vmcnt(9) lgkmcnt(10)\n"
    "v_mfma_f32_16x16x32_bf16 v[0:3], v[160:163], a[24:27], v[0:3]\n"
    "ds_read_b64_tr_b16 v[188:189], v227 offset:28672\n"
    "v_mfma_f32_16x16x32_bf16 v[16:19], v[160:163], a[28:31], v[16:19]\n"
    "ds_read_b64_tr_b16 v[190:191], v227 offset:29184\n"
    "s_barrier\n"
    "v_mfma_f32_16x16x32_bf16 v[20:23], v[164:167], a[28:31], v[20:23]\n"
    "ds_read_b64_tr_b16 v[160:161], v224 offset:32768\n"
    "buffer_load_dwordx4 a[68:71], v252, s[44:47], s74 offen offset:1024 sc1\n"
    "v_mfma_f32_16x16x32_bf16 v[4:7], v[164:167], a[24:27], v[4:7]\n"
    "ds_read_b64_tr_b16 v[162:163], v224 offset:33280\n"
    "s_waitcnt lgkmcnt(10)\n"
    "v_mfma_f32_16x16x32_bf16 v[8:11], v[168:171], a[24:27], v[8:11]\n"
    "ds_read_b64_tr_b16 v[164:165], v225 offset:32768\n"
    "v_mfma_f32_16x16x32_bf16 v[24:27], v[168:171], a[28:31], v[24:27]\n"
    "ds_read_b64_tr_b16 v[166:167], v225 offset:33280\n"
    "buffer_load_dwordx4 a[72:75], v252, s[44:47], s74 offen offset:2048 sc1\n"
    "v_mfma_f32_16x16x32_bf16 v[28:31], v[172:175], a[28:31], v[28:31]\n"
    "ds_read_b64_tr_b16 v[168:169], v226 offset:32768\n"
    "v_mfma_f32_16x16x32_bf16 v[12:15], v[172:175], a[24:27], v[12:15]\n"
    "ds_read_b64_tr_b16 v[170:171], v226 offset:33280\n"
    "s_waitcnt lgkmcnt(10)\n"
    "v_mfma_f32_16x16x32_bf16 v[32:35], a[24:27], v[176:179], v[32:35]\n"
    "ds_read_b64_tr_b16 v[172:173], v227 offset:32768\n"
    "v_mfma_f32_16x16x32_bf16 v[48:51], a[28:31], v[176:179], v[48:51]\n"
    "ds_read_b64_tr_b16 v[174:175], v227 offset:33280\n"
    "v_mfma_f32_16x16x32_bf16 v[52:55], a[28:31], v[180:183], v[52:55]\n"
    "ds_read_b64_tr_b16 v[176:177], v224 offset:40960\n"
    "buffer_load_dwordx4 a[76:79], v252, s[44:47], s74 offen offset:3072 sc1\n"
    "v_mfma_f32_16x16x32_bf16 v[36:39], a[24:27], v[180:183], v[36:39]\n"
    "ds_read_b64_tr_b16 v[178:179], v224 offset:41472\n"
    "s_waitcnt lgkmcnt(10)\n"
    "v_mfma_f32_16x16x32_bf16 v[40:43], a[24:27], v[184:187], v[40:43]\n"
    "ds_read_b64_tr_b16 v[180:181], v225 offset:40960\n"
    "v_mfma_f32_16x16x32_bf16 v[56:59], a[28:31], v[184:187], v[56:59]\n"
    "ds_read_b64_tr_b16 v[182:183], v225 offset:41472\n"
    "v_mfma_f32_16x16x32_bf16 v[60:63], a[28:31], v[188:191], v[60:63]\n"
    "ds_read_b64_tr_b16 v[184:185], v226 offset:40960\n"
    "v_mfma_f32_16x16x32_bf16 v[44:47], a[24:27], v[188:191], v[44:47]\n"
    "ds_read_b64_tr_b16 v[186:187], v226 offset:41472\n"
    "s_waitcnt vmcnt(10) lgkmcnt(10)\n"
    "v_mfma_f32_16x16x32_bf16 v[0:3], v[160:163], a[32:35], v[0:3]\n"
    "ds_read_b64_tr_b16 v[188:189], v227 offset:40960\n"
    "buffer_load_dwordx4 a[80:83], v253, s[44:47], s74 offen sc1\n"
    "v_mfma_f32_16x16x32_bf16 v[16:19], v[160:163], a[36:39], v[16:19]\n"
    "ds_read_b64_tr_b16 v[190:191], v227 offset:41472\n"
    "v_mfma_f32_16x16x32_bf16 v[20:23], v[164:167], a[36:39], v[20:23]\n"
    "ds_read_b64_tr_b16 v[160:161], v224 offset:36864\n"
    "v_mfma_f32_16x16x32_bf16 v[4:7], v[164:167], a[32:35], v[4:7]\n"
    "ds_read_b64_tr_b16 v[162:163], v224 offset:37376\n"
    "s_waitcnt lgkmcnt(10)\n"
    "v_mfma_f32_16x16x32_bf16 v[8:11], v[168:171], a[32:35], v[8:11]\n"
    "ds_read_b64_tr_b16 v[164:165], v225 offset:36864\n"
    "v_mfma_f32_16x16x32_bf16 v[24:27], v[168:171], a[36:39], v[24:27]\n"
    "ds_read_b64_tr_b16 v[166:167], v225 offset:37376\n"
    "buffer_load_dwordx4 a[84:87], v253, s[44:47], s74 offen offset:1024 sc1\n"
    "v_mfma_f32_16x16x32_bf16 v[28:31], v[172:175], a[36:39], v[28:31]\n"
    "ds_read_b64_tr_b16 v[168:169], v226 offset:36864\n"
    "v_mfma_f32_16x16x32_bf16 v[12:15], v[172:175], a[32:35], v[12:15]\n"
    "ds_read_b64_tr_b16 v[170:171], v226 offset:37376\n"
    "s_waitcnt lgkmcnt(10)\n"
    "v_mfma_f32_16x16x32_bf16 v[32:35], a[32:35], v[176:179], v[32:35]\n"
    "ds_read_b64_tr_b16 v[172:173], v227 offset:36864\n"
    "v_mfma_f32_16x16x32_bf16 v[48:51], a[36:39], v[176:179], v[48:51]\n"
    "ds_read_b64_tr_b16 v[174:175], v227 offset:37376\n"
    "v_mfma_f32_16x16x32_bf16 v[52:55], a[36:39], v[180:183], v[52:55]\n"
    "ds_read_b64_tr_b16 v[176:177], v224 offset:45056\n"
    "buffer_load_dwordx4 a[88:91], v253, s[44:47], s74 offen offset:2048 sc1\n"
    "v_mfma_f32_16x16x32_bf16 v[36:39], a[32:35], v[180:183], v[36:39]\n"
    "ds_read_b64_tr_b16 v[178:179], v224 offset:45568\n"
    "s_waitcnt lgkmcnt(10)\n"
    "v_mfma_f32_16x16x32_bf16 v[40:43], a[32:35], v[184:187], v[40:43]\n"
    "ds_read_b64_tr_b16 v[180:181], v225 offset:45056\n"
    "v_mfma_f32_16x16x32_bf16 v[56:59], a[36:39], v[184:187], v[56:59]\n"
    "ds_read_b64_tr_b16 v[182:183], v225 offset:45568\n"
    "v_mfma_f32_16x16x32_bf16 v[60:63], a[36:39], v[188:191], v[60:63]\n"
    "ds_read_b64_tr_b16 v[184:185], v226 offset:45056\n"
    "v_mfma_f32_16x16x32_bf16 v[44:47], a[32:35], v[188:191], v[44:47]\n"
    "ds_read_b64_tr_b16 v[186:187], v226 offset:45568\n"
    "s_waitcnt vmcnt(11) lgkmcnt(10)\n"
    "v_mfma_f32_16x16x32_bf16 v[0:3], v[160:163], a[40:43], v[0:3]\n"
    "ds_read_b64_tr_b16 v[188:189], v227 offset:45056\n"
    "buffer_load_dwordx4 a[92:95], v253, s[44:47], s74 offen offset:3072 sc1\n"
    "v_mfma_f32_16x16x32_bf16 v[16:19], v[160:163], a[44:47], v[16:19]\n"
    "ds_read_b64_tr_b16 v[190:191], v227 offset:45568\n"
    "v_mfma_f32_16x16x32_bf16 v[20:23], v[164:167], a[44:47], v[20:23]\n"
    "ds_read_b64_tr_b16 v[160:161], v224 offset:49152\n"
    "v_mfma_f32_16x16x32_bf16 v[4:7], v[164:167], a[40:43], v[4:7]\n"
    "ds_read_b64_tr_b16 v[162:163], v224 offset:49664\n"
    "s_waitcnt lgkmcnt(10)\n"
    "v_mfma_f32_16x16x32_bf16 v[8:11], v[168:171], a[40:43], v[8:11]\n"
    "ds_read_b64_tr_b16 v[164:165], v225 offset:49152\n"
    "v_mfma_f32_16x16x32_bf16 v[24:27], v[168:171], a[44:47], v[24:27]\n"
    "ds_read_b64_tr_b16 v[166:167], v225 offset:49664\n"
    "buffer_load_dwordx4 a[96:99], v252, s[44:47], s75 offen sc1\n"
    "v_mfma_f32_16x16x32_bf16 v[28:31], v[172:175], a[44:47], v[28:31]\n"
    "ds_read_b64_tr_b16 v[168:169], v226 offset:49152\n"
    "v_mfma_f32_16x16x32_bf16 v[12:15], v[172:175], a[40:43], v[12:15]\n"
    "ds_read_b64_tr_b16 v[170:171], v226 offset:49664\n"
    "s_waitcnt lgkmcnt(10)\n"
    "v_mfma_f32_16x16x32_bf16 v[32:35], a[40:43], v[176:179], v[32:35]\n"
    "ds_read_b64_tr_b16 v[172:173], v227 offset:49152\n"
    "v_mfma_f32_16x16x32_bf16 v[48:51], a[44:47], v[176:179], v[48:51]\n"
    "ds_read_b64_tr_b16 v[174:175], v227 offset:49664\n"
    "v_mfma_f32_16x16x32_bf16 v[52:55], a[44:47], v[180:183], v[52:55]\n"
    "ds_read_b64_tr_b16 v[176:177], v224 offset:57344\n"
    "buffer_load_dwordx4 a[100:103], v252, s[44:47], s75 offen offset:1024 sc1\n"
    "v_mfma_f32_16x16x32_bf16 v[36:39], a[40:43], v[180:183], v[36:39]\n"
    "ds_read_b64_tr_b16 v[178:179], v224 offset:57856\n"
    "s_waitcnt lgkmcnt(10)\n"
    "v_mfma_f32_16x16x32_bf16 v[40:43], a[40:43], v[184:187], v[40:43]\n"
    "ds_read_b64_tr_b16 v[180:181], v225 offset:57344\n"
    "v_mfma_f32_16x16x32_bf16 v[56:59], a[44:47], v[184:187], v[56:59]\n"
    "ds_read_b64_tr_b16 v[182:183], v225 offset:57856\n"
    "v_mfma_f32_16x16x32_bf16 v[60:63], a[44:47], v[188:191], v[60:63]\n"
    "ds_read_b64_tr_b16 v[184:185], v226 offset:57344\n"
    "v_mfma_f32_16x16x32_bf16 v[44:47], a[40:43], v[188:191], v[44:47]\n"
    "ds_read_b64_tr_b16 v[186:187], v226 offset:57856\n"
    "s_waitcnt vmcnt(12) lgkmcnt(10)\n"
    "v_mfma_f32_16x16x32_bf16 v[0:3], v[160:163], a[48:51], v[0:3]\n"
    "ds_read_b64_tr_b16 v[188:189], v227 offset:57344\n"
    "buffer_load_dwordx4 a[104:107], v252, s[44:47], s75 offen offset:2048 sc1\n"
    "v_mfma_f32_16x16x32_bf16 v[16:19], v[160:163], a[52:55], v[16:19]\n"
    "ds_read_b64_tr_b16 v[190:191], v227 offset:57856\n"
    "v_mfma_f32_16x16x32_bf16 v[20:23], v[164:167], a[52:55], v[20:23]\n"
    "ds_read_b64_tr_b16 v[160:161], v224 offset:53248\n"
    "v_mfma_f32_16x16x32_bf16 v[4:7], v[164:167], a[48:51], v[4:7]\n"
    "ds_read_b64_tr_b16 v[162:163], v224 offset:53760\n"
    "s_waitcnt lgkmcnt(10)\n"
    "v_mfma_f32_16x16x32_bf16 v[8:11], v[168:171], a[48:51], v[8:11]\n"
    "ds_read_b64_tr_b16 v[164:165], v225 offset:53248\n"
    "v_mfma_f32_16x16x32_bf16 v[24:27], v[168:171], a[52:55], v[24:27]\n"
    "ds_read_b64_tr_b16 v[166:167], v225 offset:53760\n"
    "buffer_load_dwordx4 a[108:111], v252, s[44:47], s75 offen offset:3072 sc1\n"
    "v_mfma_f32_16x16x32_bf16 v[28:31], v[172:175], a[52:55], v[28:31]\n"
    "ds_read_b64_tr_b16 v[168:169], v226 offset:53248\n"
    "v_mfma_f32_16x16x32_bf16 v[12:15], v[172:175], a[48:51], v[12:15]\n"
    "ds_read_b64_tr_b16 v[170:171], v226 offset:53760\n"
    "s_waitcnt lgkmcnt(10)\n"
    "v_mfma_f32_16x16x32_bf16 v[32:35], a[48:51], v[176:179], v[32:35]\n"
    "ds_read_b64_tr_b16 v[172:173], v227 offset:53248\n"
    "v_mfma_f32_16x16x32_bf16 v[48:51], a[52:55], v[176:179], v[48:51]\n"
    "ds_read_b64_tr_b16 v[174:175], v227 offset:53760\n"
    "v_mfma_f32_16x16x32_bf16 v[52:55], a[52:55], v[180:183], v[52:55]\n"
    "ds_read_b64_tr_b16 v[176:177], v224 offset:61440\n"
    "buffer_load_dwordx4 a[112:115], v253, s[44:47], s75 offen sc1\n"
    "v_mfma_f32_16x16x32_bf16 v[36:39], a[48:51], v[180:183], v[36:39]\n"
    "ds_read_b64_tr_b16 v[178:179], v224 offset:61952\n"
    "s_waitcnt lgkmcnt(10)\n"
    "v_mfma_f32_16x16x32_bf16 v[40:43], a[48:51], v[184:187], v[40:43]\n"
    "ds_read_b64_tr_b16 v[180:181], v225 offset:61440\n"
    "v_mfma_f32_16x16x32_bf16 v[56:59], a[52:55], v[184:187], v[56:59]\n"
    "ds_read_b64_tr_b16 v[182:183], v225 offset:61952\n"
    "v_mfma_f32_16x16x32_bf16 v[60:63], a[52:55], v[188:191], v[60:63]\n"
    "ds_read_b64_tr_b16 v[184:185], v226 offset:61440\n"
    "v_mfma_f32_16x16x32_bf16 v[44:47], a[48:51], v[188:191], v[44:47]\n"
    "ds_read_b64_tr_b16 v[186:187], v226 offset:61952\n"
    "s_waitcnt vmcnt(13) lgkmcnt(10)\n"
    "v_mfma_f32_16x16x32_bf16 v[0:3], v[160:163], a[56:59], v[0:3]\n"
    "ds_read_b64_tr_b16 v[188:189], v227 offset:61440\n"
    "v_mfma_f32_16x16x32_bf16 v[16:19], v[160:163], a[60:63], v[16:19]\n"
    "ds_read_b64_tr_b16 v[190:191], v227 offset:61952\n"
    "s_barrier\n"
    "v_mfma_f32_16x16x32_bf16 v[20:23], v[164:167], a[60:63], v[20:23]\n"
    "ds_read_b64_tr_b16 v[160:161], v228\n"
    "buffer_load_dwordx4 a[116:119], v253, s[44:47], s75 offen offset:1024 sc1\n"
    "v_mfma_f32_16x16x32_bf16 v[4:7], v[164:167], a[56:59], v[4:7]\n"
    "ds_read_b64_tr_b16 v[162:163], v228 offset:512\n"
    "s_waitcnt lgkmcnt(10)\n"
    "v_mfma_f32_16x16x32_bf16 v[8:11], v[168:171], a[56:59], v[8:11]\n"
    "ds_read_b64_tr_b16 v[164:165], v229\n"
    "v_mfma_f32_16x16x32_bf16 v[24:27], v[168:171], a[60:63], v[24:27]\n"
    "ds_read_b64_tr_b16 v[166:167], v229 offset:512\n"
    "buffer_load_dwordx4 a[120:123], v253, s[44:47], s75 offen offset:2048 sc1\n"
    "v_mfma_f32_16x16x32_bf16 v[28:31], v[172:175], a[60:63], v[28:31]\n"
    "ds_read_b64_tr_b16 v[168:169], v230\n"
    "v_mfma_f32_16x16x32_bf16 v[12:15], v[172:175], a[56:59], v[12:15]\n"
    "ds_read_b64_tr_b16 v[170:171], v230 offset:512\n"
    "s_waitcnt lgkmcnt(10)\n"
    "v_mfma_f32_16x16x32_bf16 v[32:35], a[56:59], v[176:179], v[32:35]\n"
    "ds_read_b64_tr_b16 v[172:173], v231\n"
    "v_mfma_f32_16x16x32_bf16 v[48:51], a[60:63], v[176:179], v[48:51]\n"
    "ds_read_b64_tr_b16 v[174:175], v231 offset:512\n"
    "v_mfma_f32_16x16x32_bf16 v[52:55], a[60:63], v[180:183], v[52:55]\n"
    "ds_read_b64_tr_b16 v[176:177], v228 offset:8192\n"
    "buffer_load_dwordx4 a[124:127], v253, s[44:47], s75 offen offset:3072 sc1\n"
    "v_mfma_f32_16x16x32_bf16 v[36:39], a[56:59], v[180:183], v[36:39]\n"
    "ds_read_b64_tr_b16 v[178:179], v228 offset:8704\n"
    "s_waitcnt lgkmcnt(10)\n"
    "v_mfma_f32_16x16x32_bf16 v[40:43], a[56:59], v[184:187], v[40:43]\n"
    "ds_read_b64_tr_b16 v[180:181], v229 offset:8192\n"
    "v_mfma_f32_16x16x32_bf16 v[56:59], a[60:63], v[184:187], v[56:59]\n"
    "ds_read_b64_tr_b16 v[182:183], v229 offset:8704\n"
    "v_mfma_f32_16x16x32_bf16 v[60:63], a[60:63], v[188:191], v[60:63]\n"
    "ds_read_b64_tr_b16 v[184:185], v230 offset:8192\n"
    "v_mfma_f32_16x16x32_bf16 v[44:47], a[56:59], v[188:191], v[44:47]\n"
    "ds_read_b64_tr_b16 v[186:187], v230 offset:8704\n"
    "s_waitcnt vmcnt(14) lgkmcnt(10)\n"
    "v_mfma_f32_16x16x32_bf16 v[0:3], v[160:163], a[64:67], v[0:3]\n"
    "ds_read_b64_tr_b16 v[188:189], v231 offset:8192\n"
    "buffer_load_dwordx4 a[128:131], v252, s[44:47], s76 offen sc1\n"
    "v_mfma_f32_16x16x32_bf16 v[16:19], v[160:163], a[68:71], v[16:19]\n"
    "ds_read_b64_tr_b16 v[190:191], v231 offset:8704\n"
    "v_mfma_f32_16x16x32_bf16 v[20:23], v[164:167], a[68:71], v[20:23]\n"
    "ds_read_b64_tr_b16 v[160:161], v228 offset:4096\n"
    "v_mfma_f32_16x16x32_bf16 v[4:7], v[164:167], a[64:67], v[4:7]\n"
    "ds_read_b64_tr_b16 v[162:163], v228 offset:4608\n"
    "s_waitcnt lgkmcnt(10)\n"
    "v_mfma_f32_16x16x32_bf16 v[8:11], v[168:171], a[64:67], v[8:11]\n"
    "ds_read_b64_tr_b16 v[164:165], v229 offset:4096\n"
    "v_mfma_f32_16x16x32_bf16 v[24:27], v[168:171], a[68:71], v[24:27]\n"
    "ds_read_b64_tr_b16 v[166:167], v229 offset:4608\n"
    "buffer_load_dwordx4 a[132:135], v252, s[44:47], s76 offen offset:1024 sc1\n"
    "v_mfma_f32_16x16x32_bf16 v[28:31], v[172:175], a[68:71], v[28:31]\n"
    "ds_read_b64_tr_b16 v[168:169], v230 offset:4096\n"
    "v_mfma_f32_16x16x32_bf16 v[12:15], v[172:175], a[64:67], v[12:15]\n"
    "ds_read_b64_tr_b16 v[170:171], v230 offset:4608\n"
    "s_waitcnt lgkmcnt(10)\n"
    "v_mfma_f32_16x16x32_bf16 v[32:35], a[64:67], v[176:179], v[32:35]\n"
    "ds_read_b64_tr_b16 v[172:173], v231 offset:4096\n"
    "v_mfma_f32_16x16x32_bf16 v[48:51], a[68:71], v[176:179], v[48:51]\n"
    "ds_read_b64_tr_b16 v[174:175], v231 offset:4608\n"
    "v_mfma_f32_16x16x32_bf16 v[52:55], a[68:71], v[180:183], v[52:55]\n"
    "ds_read_b64_tr_b16 v[176:177], v228 offset:12288\n"
    "buffer_load_dwordx4 a[136:139], v252, s[44:47], s76 offen offset:2048 sc1\n"
    "v_mfma_f32_16x16x32_bf16 v[36:39], a[64:67], v[180:183], v[36:39]\n"
    "ds_read_b64_tr_b16 v[178:179], v228 offset:12800\n"
    "s_waitcnt lgkmcnt(10)\n"
    "v_mfma_f32_16x16x32_bf16 v[40:43], a[64:67], v[184:187], v[40:43]\n"
    "ds_read_b64_tr_b16 v[180:181], v229 offset:12288\n"
    "v_mfma_f32_16x16x32_bf16 v[56:59], a[68:71], v[184:187], v[56:59]\n"
    "ds_read_b64_tr_b16 v[182:183], v229 offset:12800\n"
    "v_mfma_f32_16x16x32_bf16 v[60:63], a[68:71], v[188:191], v[60:63]\n"
    "ds_read_b64_tr_b16 v[184:185], v230 offset:12288\n"
    "v_mfma_f32_16x16x32_bf16 v[44:47], a[64:67], v[188:191], v[44:47]\n"
    "ds_read_b64_tr_b16 v[186:187], v230 offset:12800\n"
    "s_waitcnt vmcnt(15) lgkmcnt(10)\n"
    "v_mfma_f32_16x16x32_bf16 v[0:3], v[160:163], a[72:75], v[0:3]\n"
    "ds_read_b64_tr_b16 v[188:189], v231 offset:12288\n"
    "s_add_u32 m0, s85, 0x24000\n"
    "v_mfma_f32_16x16x32_bf16 v[16:19], v[160:163], a[76:79], v[16:19]\n"
    "ds_read_b64_tr_b16 v[190:191], v231 offset:12800\n"
    "s_add_u32 s98, s60, s99\n"
    "v_mfma_f32_16x16x32_bf16 v[20:23], v[164:167], a[76:79], v[20:23]\n"
    "ds_read_b64_tr_b16 v[160:161], v228 offset:16384\n"
    "buffer_load_dwordx4 v236, s[24:27], s98 offen lds\n"
    "v_mfma_f32_16x16x32_bf16 v[4:7], v[164:167], a[72:75], v[4:7]\n"
    "ds_read_b64_tr_b16 v[162:163], v228 offset:16896\n"
    "s_waitcnt lgkmcnt(10)\n"
    "v_mfma_f32_16x16x32_bf16 v[8:11], v[168:171], a[72:75], v[8:11]\n"
    "ds_read_b64_tr_b16 v[164:165], v229 offset:16384\n"
    "buffer_load_dwordx4 a[140:143], v252, s[44:47], s76 offen offset:3072 sc1\n"
    "v_mfma_f32_16x16x32_bf16 v[24:27], v[168:171], a[76:79], v[24:27]\n"
    "ds_read_b64_tr_b16 v[166:167], v229 offset:16896\n"
    "buffer_load_dwordx4 a[144:147], v253, s[44:47], s76 offen sc1\n"
    "v_mfma_f32_16x16x32_bf16 v[28:31], v[172:175], a[76:79], v[28:31]\n"
    "ds_read_b64_tr_b16 v[168:169], v230 offset:16384\n"
    "v_mfma_f32_16x16x32_bf16 v[12:15], v[172:175], a[72:75], v[12:15]\n"
    "ds_read_b64_tr_b16 v[170:171], v230 offset:16896\n"
    "s_waitcnt lgkmcnt(10)\n"
    "v_mfma_f32_16x16x32_bf16 v[32:35], a[72:75], v[176:179], v[32:35]\n"
    "ds_read_b64_tr_b16 v[172:173], v231 offset:16384\n"
    "buffer_load_dwordx4 v237, s[24:27], s98 offen offset:1024 lds\n"
    "v_mfma_f32_16x16x32_bf16 v[48:51], a[76:79], v[176:179], v[48:51]\n"
    "ds_read_b64_tr_b16 v[174:175], v231 offset:16896\n"
    "v_mfma_f32_16x16x32_bf16 v[52:55], a[76:79], v[180:183], v[52:55]\n"
    "ds_read_b64_tr_b16 v[176:177], v228 offset:24576\n"
    "v_mfma_f32_16x16x32_bf16 v[36:39], a[72:75], v[180:183], v[36:39]\n"
    "ds_read_b64_tr_b16 v[178:179], v228 offset:25088\n"
    "s_waitcnt lgkmcnt(10)\n"
    "v_mfma_f32_16x16x32_bf16 v[40:43], a[72:75], v[184:187], v[40:43]\n"
    "ds_read_b64_tr_b16 v[180:181], v229 offset:24576\n"
    "v_mfma_f32_16x16x32_bf16 v[56:59], a[76:79], v[184:187], v[56:59]\n"
    "ds_read_b64_tr_b16 v[182:183], v229 offset:25088\n"
    "buffer_load_dwordx4 a[148:151], v253, s[44:47], s76 offen offset:1024 sc1\n"
    "v_mfma_f32_16x16x32_bf16 v[60:63], a[76:79], v[188:191], v[60:63]\n"
    "ds_read_b64_tr_b16 v[184:185], v230 offset:24576\n"
    "buffer_load_dwordx4 v238, s[24:27], s98 offen offset:2048 lds\n"
    "v_mfma_f32_16x16x32_bf16 v[44:47], a[72:75], v[188:191], v[44:47]\n"
    "ds_read_b64_tr_b16 v[186:187], v230 offset:25088\n"
    "s_waitcnt vmcnt(19) lgkmcnt(10)\n"
    "v_mfma_f32_16x16x32_bf16 v[0:3], v[160:163], a[80:83], v[0:3]\n"
    "ds_read_b64_tr_b16 v[188:189], v231 offset:24576\n"
    "v_mfma_f32_16x16x32_bf16 v[16:19], v[160:163], a[84:87], v[16:19]\n"
    "ds_read_b64_tr_b16 v[190:191], v231 offset:25088\n"
    "v_mfma_f32_16x16x32_bf16 v[20:23], v[164:167], a[84:87], v[20:23]\n"
    "ds_read_b64_tr_b16 v[160:161], v228 offset:20480\n"
    "v_mfma_f32_16x16x32_bf16 v[4:7], v[164:167], a[80:83], v[4:7]\n"
    "ds_read_b64_tr_b16 v[162:163], v228 offset:20992\n"
    "s_waitcnt lgkmcnt(10)\n"
    "v_mfma_f32_16x16x32_bf16 v[8:11], v[168:171], a[80:83], v[8:11]\n"
    "ds_read_b64_tr_b16 v[164:165], v229 offset:20480\n"
    "buffer_load_dwordx4 v239, s[24:27], s98 offen offset:3072 lds\n"
    "v_mfma_f32_16x16x32_bf16 v[24:27], v[168:171], a[84:87], v[24:27]\n"
    "ds_read_b64_tr_b16 v[166:167], v229 offset:20992\n"
    "buffer_load_dwordx4 a[152:155], v253, s[44:47], s76 offen offset:2048 sc1\n"
    "v_mfma_f32_16x16x32_bf16 v[28:31], v[172:175], a[84:87], v[28:31]\n"
    "ds_read_b64_tr_b16 v[168:169], v230 offset:20480\n"
    "v_mfma_f32_16x16x32_bf16 v[12:15], v[172:175], a[80:83], v[12:15]\n"
    "ds_read_b64_tr_b16 v[170:171], v230 offset:20992\n"
    "s_waitcnt lgkmcnt(10)\n"
    "v_mfma_f32_16x16x32_bf16 v[32:35], a[80:83], v[176:179], v[32:35]\n"
    "ds_read_b64_tr_b16 v[172:173], v231 offset:20480\n"
    "v_mfma_f32_16x16x32_bf16 v[48:51], a[84:87], v[176:179], v[48:51]\n"
    "ds_read_b64_tr_b16 v[174:175], v231 offset:20992\n"
    "v_mfma_f32_16x16x32_bf16 v[52:55], a[84:87], v[180:183], v[52:55]\n"
    "ds_read_b64_tr_b16 v[176:177], v228 offset:28672\n"
    "v_mfma_f32_16x16x32_bf16 v[36:39], a[80:83], v[180:183], v[36:39]\n"
    "ds_read_b64_tr_b16 v[178:179], v228 offset:29184\n"
    "s_waitcnt lgkmcnt(10)\n"
    "v_mfma_f32_16x16x32_bf16 v[40:43], a[80:83], v[184:187], v[40:43]\n"
    "ds_read_b64_tr_b16 v[180:181], v229 offset:28672\n"
    "v_mfma_f32_16x16x32_bf16 v[56:59], a[84:87], v[184:187], v[56:59]\n"
    "ds_read_b64_tr_b16 v[182:183], v229 offset:29184\n"
    "buffer_load_dwordx4 a[156:159], v253, s[44:47], s76 offen offset:3072 sc1\n"
    "v_mfma_f32_16x16x32_bf16 v[60:63], a[84:87], v[188:191], v[60:63]\n"
    "ds_read_b64_tr_b16 v[184:185], v230 offset:28672\n"
    "v_mfma_f32_16x16x32_bf16 v[44:47], a[80:83], v[188:191], v[44:47]\n"
    "ds_read_b64_tr_b16 v[186:187], v230 offset:29184\n"
    "s_waitcnt vmcnt(20) lgkmcnt(10)\n"
    "v_mfma_f32_16x16x32_bf16 v[0:3], v[160:163], a[88:91], v[0:3]\n"
    "ds_read_b64_tr_b16 v[188:189], v231 offset:28672\n"
    "v_mfma_f32_16x16x32_bf16 v[16:19], v[160:163], a[92:95], v[16:19]\n"
    "ds_read_b64_tr_b16 v[190:191], v231 offset:29184\n"
    "s_barrier\n"
    "v_mfma_f32_16x16x32_bf16 v[20:23], v[164:167], a[92:95], v[20:23]\n"
    "ds_read_b64_tr_b16 v[160:161], v228 offset:32768\n"
    "s_add_u32 m0, s85, 0\n"
    "v_mfma_f32_16x16x32_bf16 v[4:7], v[164:167], a[88:91], v[4:7]\n"
    "ds_read_b64_tr_b16 v[162:163], v228 offset:33280\n"
    "s_waitcnt lgkmcnt(10)\n"
    "v_mfma_f32_16x16x32_bf16 v[8:11], v[168:171], a[88:91], v[8:11]\n"
    "ds_read_b64_tr_b16 v[164:165], v229 offset:32768\n"
    "buffer_load_dwordx4 v236, s[24:27], s61 offen lds\n"
    "v_mfma_f32_16x16x32_bf16 v[24:27], v[168:171], a[92:95], v[24:27]\n"
    "ds_read_b64_tr_b16 v[166:167], v229 offset:33280\n"
    "buffer_load_dwordx4 a[160:163], v252, s[44:47], s77 offen sc1\n"
    "v_mfma_f32_16x16x32_bf16 v[28:31], v[172:175], a[92:95], v[28:31]\n"
    "ds_read_b64_tr_b16 v[168:169], v230 offset:32768\n"
    "v_mfma_f32_16x16x32_bf16 v[12:15], v[172:175], a[88:91], v[12:15]\n"
    "ds_read_b64_tr_b16 v[170:171], v230 offset:33280\n"
    "s_waitcnt lgkmcnt(10)\n"
    "v_mfma_f32_16x16x32_bf16 v[32:35], a[88:91], v[176:179], v[32:35]\n"
    "ds_read_b64_tr_b16 v[172:173], v231 offset:32768\n"
    "v_mfma_f32_16x16x32_bf16 v[48:51], a[92:95], v[176:179], v[48:51]\n"
    "ds_read_b64_tr_b16 v[174:175], v231 offset:33280\n"
    "v_mfma_f32_16x16x32_bf16 v[52:55], a[92:95], v[180:183], v[52:55]\n"
    "ds_read_b64_tr_b16 v[176:177], v228 offset:40960\n"
    "buffer_load_dwordx4 v237, s[24:27], s61 offen offset:1024 lds\n"
    "v_mfma_f32_16x16x32_bf16 v[36:39], a[88:91], v[180:183], v[36:39]\n"
    "ds_read_b64_tr_b16 v[178:179], v228 offset:41472\n"
    "s_waitcnt lgkmcnt(10)\n"
    "v_mfma_f32_16x16x32_bf16 v[40:43], a[88:91], v[184:187], v[40:43]\n"
    "ds_read_b64_tr_b16 v[180:181], v229 offset:40960\n"
    "v_mfma_f32_16x16x32_bf16 v[56:59], a[92:95], v[184:187], v[56:59]\n"
    "ds_read_b64_tr_b16 v[182:183], v229 offset:41472\n"
    "buffer_load_dwordx4 a[164:167], v252, s[44:47], s77 offen offset:1024 sc1\n"
    "v_mfma_f32_16x16x32_bf16 v[60:63], a[92:95], v[188:191], v[60:63]\n"
    "ds_read_b64_tr_b16 v[184:185], v230 offset:40960\n"
    "v_mfma_f32_16x16x32_bf16 v[44:47], a[88:91], v[188:191], v[44:47]\n"
    "ds_read_b64_tr_b16 v[186:187], v230 offset:41472\n"
    "s_waitcnt vmcnt(22) lgkmcnt(10)\n"
    "v_mfma_f32_16x16x32_bf16 v[0:3], v[160:163], a[96:99], v[0:3]\n"
    "ds_read_b64_tr_b16 v[188:189], v231 offset:40960\n"
    "s_getpc_b64 s[92:93]\n"
    ".Lct1:\n"
    "v_mfma_f32_16x16x32_bf16 v[16:19], v[160:163], a[100:103], v[16:19]\n"
    "ds_read_b64_tr_b16 v[190:191], v231 offset:41472\n"
    "s_and_b32 s93, s93, 0xffff\n"
    "v_mfma_f32_16x16x32_bf16 v[20:23], v[164:167], a[100:103], v[20:23]\n"
    "ds_read_b64_tr_b16 v[160:161], v228 offset:36864\n"
    "buffer_load_dwordx4 v238, s[24:27], s61 offen offset:2048 lds\n"
    "v_mfma_f32_16x16x32_bf16 v[4:7], v[164:167], a[96:99], v[4:7]\n"
    "ds_read_b64_tr_b16 v[162:163], v228 offset:37376\n"
    "s_waitcnt lgkmcnt(10)\n"
    "v_mfma_f32_16x16x32_bf16 v[8:11], v[168:171], a[96:99], v[8:11]\n"
    "ds_read_b64_tr_b16 v[164:165], v229 offset:36864\n"
    "s_mov_b32 s94, .Lend-.Lct1\n"
    "v_mfma_f32_16x16x32_bf16 v[24:27], v[168:171], a[100:103], v[24:27]\n"
    "ds_read_b64_tr_b16 v[166:167], v229 offset:37376\n"
    "s_min_u32 s94, s94, 0x3000\n"
    "v_mfma_f32_16x16x32_bf16 v[28:31], v[172:175], a[100:103], v[28:31]\n"
    "ds_read_b64_tr_b16 v[168:169], v230 offset:36864\n"
    "s_mov_b32 s95, 0x20000\n"
    "v_mfma_f32_16x16x32_bf16 v[12:15], v[172:175], a[96:99], v[12:15]\n"
    "ds_read_b64_tr_b16 v[170:171], v230 offset:37376\n"
    "s_waitcnt lgkmcnt(10)\n"
    "v_mfma_f32_16x16x32_bf16 v[32:35], a[96:99], v[176:179], v[32:35]\n"
    "ds_read_b64_tr_b16 v[172:173], v231 offset:36864\n"
    "s_and_b32 s91, s13, 0x7f\n"
    "v_mfma_f32_16x16x32_bf16 v[48:51], a[100:103], v[176:179], v[48:51]\n"
    "ds_read_b64_tr_b16 v[174:175], v231 offset:37376\n"
    "s_lshl_b32 s91, s91, 10\n"
    "v_mfma_f32_16x16x32_bf16 v[52:55], a[100:103], v[180:183], v[52:55]\n"
    "ds_read_b64_tr_b16 v[176:177], v228 offset:45056\n"
    "buffer_load_dwordx4 v239, s[24:27], s61 offen offset:3072 lds\n"
    "v_mfma_f32_16x16x32_bf16 v[36:39], a[96:99], v[180:183], v[36:39]\n"
    "ds_read_b64_tr_b16 v[178:179], v228 offset:45568\n"
    "s_waitcnt lgkmcnt(10)\n"
    "v_mfma_f32_16x16x32_bf16 v[40:43], a[96:99], v[184:187], v[40:43]\n"
    "ds_read_b64_tr_b16 v[180:181], v229 offset:45056\n"
    "buffer_load_dword v206, v240, s[92:95], s91 offen\n"
    "v_mfma_f32_16x16x32_bf16 v[56:59], a[100:103], v[184:187], v[56:59]\n"
    "ds_read_b64_tr_b16 v[182:183], v229 offset:45568\n"
    "buffer_load_dwordx4 a[168:171], v252, s[44:47], s77 offen offset:2048 sc1\n"
    "v_mfma_f32_16x16x32_bf16 v[60:63], a[100:103], v[188:191], v[60:63]\n"
    "ds_read_b64_tr_b16 v[184:185], v230 offset:45056\n"
    "buffer_load_dwordx4 a[172:175], v252, s[44:47], s77 offen offset:3072 sc1\n"
    "v_mfma_f32_16x16x32_bf16 v[44:47], a[96:99], v[188:191], v[44:47]\n"
    "ds_read_b64_tr_b16 v[186:187], v230 offset:45568\n"
    "s_waitcnt vmcnt(25) lgkmcnt(10)\n"
    "v_mfma_f32_16x16x32_bf16 v[0:3], v[160:163], a[104:107], v[0:3]\n"
    "ds_read_b64_tr_b16 v[188:189], v231 offset:45056\n"
    "s_add_u32 m0, s85, 0x4000\n"
    "v_mfma_f32_16x16x32_bf16 v[16:19], v[160:163], a[108:111], v[16:19]\n"
    "ds_read_b64_tr_b16 v[190:191], v231 offset:45568\n"
    "s_add_u32 s98, s61, s99\n"
    "v_mfma_f32_16x16x32_bf16 v[20:23], v[164:167], a[108:111], v[20:23]\n"
    "ds_read_b64_tr_b16 v[160:161], v228 offset:49152\n"
    "buffer_load_dwordx4 v236, s[24:27], s98 offen lds\n"
    "v_mfma_f32_16x16x32_bf16 v[4:7], v[164:167], a[104:107], v[4:7]\n"
    "ds_read_b64_tr_b16 v[162:163], v228 offset:49664\n"
    "s_waitcnt lgkmcnt(10)\n"
    "v_mfma_f32_16x16x32_bf16 v[8:11], v[168:171], a[104:107], v[8:11]\n"
    "ds_read_b64_tr_b16 v[164:165], v229 offset:49152\n"
    "v_mfma_f32_16x16x32_bf16 v[24:27], v[168:171], a[108:111], v[24:27]\n"
    "ds_read_b64_tr_b16 v[166:167], v229 offset:49664\n"
    "buffer_load_dwordx4 a[176:179], v253, s[44:47], s77 offen sc1\n"
    "v_mfma_f32_16x16x32_bf16 v[28:31], v[172:175], a[108:111], v[28:31]\n"
    "ds_read_b64_tr_b16 v[168:169], v230 offset:49152\n"
    "v_mfma_f32_16x16x32_bf16 v[12:15], v[172:175], a[104:107], v[12:15]\n"
    "ds_read_b64_tr_b16 v[170:171], v230 offset:49664\n"
    "s_waitcnt lgkmcnt(10)\n"
    "v_mfma_f32_16x16x32_bf16 v[32:35], a[104:107], v[176:179], v[32:35]\n"
    "ds_read_b64_tr_b16 v[172:173], v231 offset:49152\n"
    "buffer_load_dwordx4 v237, s[24:27], s98 offen offset:1024 lds\n"
    "v_mfma_f32_16x16x32_bf16 v[48:51], a[108:111], v[176:179], v[48:51]\n"
    "ds_read_b64_tr_b16 v[174:175], v231 offset:49664\n"
    "v_mfma_f32_16x16x32_bf16 v[52:55], a[108:111], v[180:183], v[52:55]\n"
    "ds_read_b64_tr_b16 v[176:177], v228 offset:57344\n"
    "v_mfma_f32_16x16x32_bf16 v[36:39], a[104:107], v[180:183], v[36:39]\n"
    "ds_read_b64_tr_b16 v[178:179], v228 offset:57856\n"
    "s_waitcnt lgkmcnt(10)\n"
    "v_mfma_f32_16x16x32_bf16 v[40:43], a[104:107], v[184:187], v[40:43]\n"
    "ds_read_b64_tr_b16 v[180:181], v229 offset:57344\n"
    "v_mfma_f32_16x16x32_bf16 v[56:59], a[108:111], v[184:187], v[56:59]\n"
    "ds_read_b64_tr_b16 v[182:183], v229 offset:57856\n"
    "buffer_load_dwordx4 a[180:183], v253, s[44:47], s77 offen offset:1024 sc1\n"
    "v_mfma_f32_16x16x32_bf16 v[60:63], a[108:111], v[188:191], v[60:63]\n"
    "ds_read_b64_tr_b16 v[184:185], v230 offset:57344\n"
    "buffer_load_dwordx4 v238, s[24:27], s98 offen offset:2048 lds\n"
    "v_mfma_f32_16x16x32_bf16 v[44:47], a[104:107], v[188:191], v[44:47]\n"
    "ds_read_b64_tr_b16 v[186:187], v230 offset:57856\n"
    "s_waitcnt vmcnt(28) lgkmcnt(10)\n"
    "v_mfma_f32_16x16x32_bf16 v[0:3], v[160:163], a[112:115], v[0:3]\n"
    "ds_read_b64_tr_b16 v[188:189], v231 offset:57344\n"
    "v_mfma_f32_16x16x32_bf16 v[16:19], v[160:163], a[116:119], v[16:19]\n"
    "ds_read_b64_tr_b16 v[190:191], v231 offset:57856\n"
    "v_mfma_f32_16x16x32_bf16 v[20:23], v[164:167], a[116:119], v[20:23]\n"
    "ds_read_b64_tr_b16 v[160:161], v228 offset:53248\n"
    "v_mfma_f32_16x16x32_bf16 v[4:7], v[164:167], a[112:115], v[4:7]\n"
    "ds_read_b64_tr_b16 v[162:163], v228 offset:53760\n"
    "s_waitcnt lgkmcnt(10)\n"
    "v_mfma_f32_16x16x32_bf16 v[8:11], v[168:171], a[112:115], v[8:11]\n"
    "ds_read_b64_tr_b16 v[164:165], v229 offset:53248\n"
    "buffer_load_dwordx4 v239, s[24:27], s98 offen offset:3072 lds\n"
    "v_mfma_f32_16x16x32_bf16 v[24:27], v[168:171], a[116:119], v[24:27]\n"
    "ds_read_b64_tr_b16 v[166:167], v229 offset:53760\n"
    "buffer_load_dwordx4 a[184:187], v253, s[44:47], s77 offen offset:2048 sc1\n"
    "v_mfma_f32_16x16x32_bf16 v[28:31], v[172:175], a[116:119], v[28:31]\n"
    "ds_read_b64_tr_b16 v[168:169], v230 offset:53248\n"
    "v_mfma_f32_16x16x32_bf16 v[12:15], v[172:175], a[112:115], v[12:15]\n"
    "ds_read_b64_tr_b16 v[170:171], v230 offset:53760\n"
    "s_waitcnt lgkmcnt(10)\n"
    "v_mfma_f32_16x16x32_bf16 v[32:35], a[112:115], v[176:179], v[32:35]\n"
    "ds_read_b64_tr_b16 v[172:173], v231 offset:53248\n"
    "v_mfma_f32_16x16x32_bf16 v[48:51], a[116:119], v[176:179], v[48:51]\n"
    "ds_read_b64_tr_b16 v[174:175], v231 offset:53760\n"
    "v_mfma_f32_16x16x32_bf16 v[52:55], a[116:119], v[180:183], v[52:55]\n"
    "ds_read_b64_tr_b16 v[176:177], v228 offset:61440\n"
    "v_mfma_f32_16x16x32_bf16 v[36:39], a[112:115], v[180:183], v[36:39]\n"
    "ds_read_b64_tr_b16 v[178:179], v228 offset:61952\n"
    "s_waitcnt lgkmcnt(10)\n"
    "v_mfma_f32_16x16x32_bf16 v[40:43], a[112:115], v[184:187], v[40:43]\n"
    "ds_read_b64_tr_b16 v[180:181], v229 offset:61440\n"
    "v_mfma_f32_16x16x32_bf16 v[56:59], a[116:119], v[184:187], v[56:59]\n"
    "ds_read_b64_tr_b16 v[182:183], v229 offset:61952\n"
    "buffer_load_dwordx4 a[188:191], v253, s[44:47], s77 offen offset:3072 sc1\n"
    "v_mfma_f32_16x16x32_bf16 v[60:63], a[116:119], v[188:191], v[60:63]\n"
    "ds_read_b64_tr_b16 v[184:185], v230 offset:61440\n"
    "v_mfma_f32_16x16x32_bf16 v[44:47], a[112:115], v[188:191], v[44:47]\n"
    "ds_read_b64_tr_b16 v[186:187], v230 offset:61952\n"
    "s_waitcnt vmcnt(29) lgkmcnt(10)\n"
    "v_mfma_f32_16x16x32_bf16 v[0:3], v[160:163], a[120:123], v[0:3]\n"
    "ds_read_b64_tr_b16 v[188:189], v231 offset:61440\n"
    "s_waitcnt vmcnt(19)\n"
    "v_mfma_f32_16x16x32_bf16 v[16:19], v[160:163], a[124:127], v[16:19]\n"
    "ds_read_b64_tr_b16 v[190:191], v231 offset:61952\n"
    "s_barrier\n"
    "v_mfma_f32_16x16x32_bf16 v[20:23], v[164:167], a[124:127], v[20:23]\n"
    "ds_read_b64_tr_b16 v[160:161], v232\n"
    "s_add_u32 m0, s85, 0x8000\n"
    "v_mfma_f32_16x16x32_bf16 v[4:7], v[164:167], a[120:123], v[4:7]\n"
    "ds_read_b64_tr_b16 v[162:163], v232 offset:512\n"
    "s_waitcnt lgkmcnt(10)\n"
    "v_mfma_f32_16x16x32_bf16 v[8:11], v[168:171], a[120:123], v[8:11]\n"
    "ds_read_b64_tr_b16 v[164:165], v233\n"
    "buffer_load_dwordx4 v236, s[24:27], s62 offen lds\n"
    "v_mfma_f32_16x16x32_bf16 v[24:27], v[168:171], a[124:127], v[24:27]\n"
    "ds_read_b64_tr_b16 v[166:167], v233 offset:512\n"
    "buffer_load_dwordx4 a[192:195], v252, s[44:47], s78 offen sc1\n"
    "v_mfma_f32_16x16x32_bf16 v[28:31], v[172:175], a[124:127], v[28:31]\n"
    "ds_read_b64_tr_b16 v[168:169], v234\n"
    "v_mfma_f32_16x16x32_bf16 v[12:15], v[172:175], a[120:123], v[12:15]\n"
    "ds_read_b64_tr_b16 v[170:171], v234 offset:512\n"
    "s_waitcnt lgkmcnt(10)\n"
    "v_mfma_f32_16x16x32_bf16 v[32:35], a[120:123], v[176:179], v[32:35]\n"
    "ds_read_b64_tr_b16 v[172:173], v235\n"
    "v_mfma_f32_16x16x32_bf16 v[48:51], a[124:127], v[176:179], v[48:51]\n"
    "ds_read_b64_tr_b16 v[174:175], v235 offset:512\n"
    "v_mfma_f32_16x16x32_bf16 v[52:55], a[124:127], v[180:183], v[52:55]\n"
    "ds_read_b64_tr_b16 v[176:177], v232 offset:8192\n"
    "buffer_load_dwordx4 v237, s[24:27], s62 offen offset:1024 lds\n"
    "v_mfma_f32_16x16x32_bf16 v[36:39], a[120:123], v[180:183], v[36:39]\n"
    "ds_read_b64_tr_b16 v[178:179], v232 offset:8704\n"
    "s_waitcnt lgkmcnt(10)\n"
    "v_mfma_f32_16x16x32_bf16 v[40:43], a[120:123], v[184:187], v[40:43]\n"
    "ds_read_b64_tr_b16 v[180:181], v233 offset:8192\n"
    "v_mfma_f32_16x16x32_bf16 v[56:59], a[124:127], v[184:187], v[56:59]\n"
    "ds_read_b64_tr_b16 v[182:183], v233 offset:8704\n"
    "buffer_load_dwordx4 a[196:199], v252, s[44:47], s78 offen offset:1024 sc1\n"
    "v_mfma_f32_16x16x32_bf16 v[60:63], a[124:127], v[188:191], v[60:63]\n"
    "ds_read_b64_tr_b16 v[184:185], v234 offset:8192\n"
    "v_mfma_f32_16x16x32_bf16 v[44:47], a[120:123], v[188:191], v[44:47]\n"
    "ds_read_b64_tr_b16 v[186:187], v234 offset:8704\n"
    "s_waitcnt lgkmcnt(10)\n"
    "v_mfma_f32_16x16x32_bf16 v[0:3], v[160:163], a[128:131], v[0:3]\n"
    "ds_read_b64_tr_b16 v[188:189], v235 offset:8192\n"
    "v_mfma_f32_16x16x32_bf16 v[16:19], v[160:163], a[132:135], v[16:19]\n"
    "ds_read_b64_tr_b16 v[190:191], v235 offset:8704\n"
    "v_mfma_f32_16x16x32_bf16 v[20:23], v[164:167], a[132:135], v[20:23]\n"
    "ds_read_b64_tr_b16 v[160:161], v232 offset:4096\n"
    "buffer_load_dwordx4 v238, s[24:27], s62 offen offset:2048 lds\n"
    "v_mfma_f32_16x16x32_bf16 v[4:7], v[164:167], a[128:131], v[4:7]\n"
    "ds_read_b64_tr_b16 v[162:163], v232 offset:4608\n"
    "s_waitcnt lgkmcnt(10)\n"
    "v_mfma_f32_16x16x32_bf16 v[8:11], v[168:171], a[128:131], v[8:11]\n"
    "ds_read_b64_tr_b16 v[164:165], v233 offset:4096\n"
    "v_mfma_f32_16x16x32_bf16 v[24:27], v[168:171], a[132:135], v[24:27]\n"
    "ds_read_b64_tr_b16 v[166:167], v233 offset:4608\n"
    "buffer_load_dwordx4 a[200:203], v252, s[44:47], s78 offen offset:2048 sc1\n"
    "v_mfma_f32_16x16x32_bf16 v[28:31], v[172:175], a[132:135], v[28:31]\n"
    "ds_read_b64_tr_b16 v[168:169], v234 offset:4096\n"
    "v_mfma_f32_16x16x32_bf16 v[12:15], v[172:175], a[128:131], v[12:15]\n"
    "ds_read_b64_tr_b16 v[170:171], v234 offset:4608\n"
    "s_waitcnt lgkmcnt(10)\n"
    "v_mfma_f32_16x16x32_bf16 v[32:35], a[128:131], v[176:179], v[32:35]\n"
    "ds_read_b64_tr_b16 v[172:173], v235 offset:4096\n"
    "v_mfma_f32_16x16x32_bf16 v[48:51], a[132:135], v[176:179], v[48:51]\n"
    "ds_read_b64_tr_b16 v[174:175], v235 offset:4608\n"
    "v_mfma_f32_16x16x32_bf16 v[52:55], a[132:135], v[180:183], v[52:55]\n"
    "ds_read_b64_tr_b16 v[176:177], v232 offset:12288\n"
    "buffer_load_dwordx4 v239, s[24:27], s62 offen offset:3072 lds\n"
    "v_mfma_f32_16x16x32_bf16 v[36:39], a[128:131], v[180:183], v[36:39]\n"
    "ds_read_b64_tr_b16 v[178:179], v232 offset:12800\n"
    "s_waitcnt lgkmcnt(10)\n"
    "v_mfma_f32_16x16x32_bf16 v[40:43], a[128:131], v[184:187], v[40:43]\n"
    "ds_read_b64_tr_b16 v[180:181], v233 offset:12288\n"
    "v_mfma_f32_16x16x32_bf16 v[56:59], a[132:135], v[184:187], v[56:59]\n"
    "ds_read_b64_tr_b16 v[182:183], v233 offset:12800\n"
    "buffer_load_dwordx4 a[204:207], v252, s[44:47], s78 offen offset:3072 sc1\n"
    "v_mfma_f32_16x16x32_bf16 v[60:63], a[132:135], v[188:191], v[60:63]\n"
    "ds_read_b64_tr_b16 v[184:185], v234 offset:12288\n"
    "v_mfma_f32_16x16x32_bf16 v[44:47], a[128:131], v[188:191], v[44:47]\n"
    "ds_read_b64_tr_b16 v[186:187], v234 offset:12800\n"
    "s_waitcnt lgkmcnt(10)\n"
    "v_mfma_f32_16x16x32_bf16 v[0:3], v[160:163], a[136:139], v[0:3]\n"
    "ds_read_b64_tr_b16 v[188:189], v235 offset:12288\n"
    "s_add_u32 m0, s85, 0xc000\n"
    "v_mfma_f32_16x16x32_bf16 v[16:19], v[160:163], a[140:143], v[16:19]\n"
    "ds_read_b64_tr_b16 v[190:191], v235 offset:12800\n"
    "s_add_u32 s98, s62, s99\n"
    "v_mfma_f32_16x16x32_bf16 v[20:23], v[164:167], a[140:143], v[20:23]\n"
    "ds_read_b64_tr_b16 v[160:161], v232 offset:16384\n"
    "buffer_load_dwordx4 v236, s[24:27], s98 offen lds\n"
    "v_mfma_f32_16x16x32_bf16 v[4:7], v[164:167], a[136:139], v[4:7]\n"
    "ds_read_b64_tr_b16 v[162:163], v232 offset:16896\n"
    "s_waitcnt lgkmcnt(10)\n"
    "v_mfma_f32_16x16x32_bf16 v[8:11], v[168:171], a[136:139], v[8:11]\n"
    "ds_read_b64_tr_b16 v[164:165], v233 offset:16384\n"
    "v_mfma_f32_16x16x32_bf16 v[24:27], v[168:171], a[140:143], v[24:27]\n"
    "ds_read_b64_tr_b16 v[166:167], v233 offset:16896\n"
    "buffer_load_dwordx4 a[208:211], v253, s[44:47], s78 offen sc1\n"
    "v_mfma_f32_16x16x32_bf16 v[28:31], v[172:175], a[140:143], v[28:31]\n"
    "ds_read_b64_tr_b16 v[168:169], v234 offset:16384\n"
    "v_mfma_f32_16x16x32_bf16 v[12:15], v[172:175], a[136:139], v[12:15]\n"
    "ds_read_b64_tr_b16 v[170:171], v234 offset:16896\n"
    "s_waitcnt lgkmcnt(10)\n"
    "v_mfma_f32_16x16x32_bf16 v[32:35], a[136:139], v[176:179], v[32:35]\n"
    "ds_read_b64_tr_b16 v[172:173], v235 offset:16384\n"
    "buffer_load_dwordx4 v237, s[24:27], s98 offen offset:1024 lds\n"
    "v_mfma_f32_16x16x32_bf16 v[48:51], a[140:143], v[176:179], v[48:51]\n"
    "ds_read_b64_tr_b16 v[174:175], v235 offset:16896\n"
    "v_mfma_f32_16x16x32_bf16 v[52:55], a[140:143], v[180:183], v[52:55]\n"
    "ds_read_b64_tr_b16 v[176:177], v232 offset:24576\n"
    "v_mfma_f32_16x16x32_bf16 v[36:39], a[136:139], v[180:183], v[36:39]\n"
    "ds_read_b64_tr_b16 v[178:179], v232 offset:25088\n"
    "s_waitcnt lgkmcnt(10)\n"
    "v_mfma_f32_16x16x32_bf16 v[40:43], a[136:139], v[184:187], v[40:43]\n"
    "ds_read_b64_tr_b16 v[180:181], v233 offset:24576\n"
    "v_mfma_f32_16x16x32_bf16 v[56:59], a[140:143], v[184:187], v[56:59]\n"
    "ds_read_b64_tr_b16 v[182:183], v233 offset:25088\n"
    "buffer_load_dwordx4 a[212:215], v253, s[44:47], s78 offen offset:1024 sc1\n"
    "v_mfma_f32_16x16x32_bf16 v[60:63], a[140:143], v[188:191], v[60:63]\n"
    "ds_read_b64_tr_b16 v[184:185], v234 offset:24576\n"
    "buffer_load_dwordx4 v238, s[24:27], s98 offen offset:2048 lds\n"
    "v_mfma_f32_16x16x32_bf16 v[44:47], a[136:139], v[188:191], v[44:47]\n"
    "ds_read_b64_tr_b16 v[186:187], v234 offset:25088\n"
    "s_waitcnt lgkmcnt(10)\n"
    "v_mfma_f32_16x16x32_bf16 v[0:3], v[160:163], a[144:147], v[0:3]\n"
    "ds_read_b64_tr_b16 v[188:189], v235 offset:24576\n"
    "v_mfma_f32_16x16x32_bf16 v[16:19], v[160:163], a[148:151], v[16:19]\n"
    "ds_read_b64_tr_b16 v[190:191], v235 offset:25088\n"
    "v_mfma_f32_16x16x32_bf16 v[20:23], v[164:167], a[148:151], v[20:23]\n"
    "ds_read_b64_tr_b16 v[160:161], v232 offset:20480\n"
    "v_mfma_f32_16x16x32_bf16 v[4:7], v[164:167], a[144:147], v[4:7]\n"
    "ds_read_b64_tr_b16 v[162:163], v232 offset:20992\n"
    "s_waitcnt lgkmcnt(10)\n"
    "v_mfma_f32_16x16x32_bf16 v[8:11], v[168:171], a[144:147], v[8:11]\n"
    "ds_read_b64_tr_b16 v[164:165], v233 offset:20480\n"
    "buffer_load_dwordx4 v239, s[24:27], s98 offen offset:3072 lds\n"
    "v_mfma_f32_16x16x32_bf16 v[24:27], v[168:171], a[148:151], v[24:27]\n"
    "ds_read_b64_tr_b16 v[166:167], v233 offset:20992\n"
    "buffer_load_dwordx4 a[216:219], v253, s[44:47], s78 offen offset:2048 sc1\n"
    "v_mfma_f32_16x16x32_bf16 v[28:31], v[172:175], a[148:151], v[28:31]\n"
    "ds_read_b64_tr_b16 v[168:169], v234 offset:20480\n"
    "v_mfma_f32_16x16x32_bf16 v[12:15], v[172:175], a[144:147], v[12:15]\n"
    "ds_read_b64_tr_b16 v[170:171], v234 offset:20992\n"
    "s_waitcnt lgkmcnt(10)\n"
    "v_mfma_f32_16x16x32_bf16 v[32:35], a[144:147], v[176:179], v[32:35]\n"
    "ds_read_b64_tr_b16 v[172:173], v235 offset:20480\n"
    "v_mfma_f32_16x16x32_bf16 v[48:51], a[148:151], v[176:179], v[48:51]\n"
    "ds_read_b64_tr_b16 v[174:175], v235 offset:20992\n"
    "v_mfma_f32_16x16x32_bf16 v[52:55], a[148:151], v[180:183], v[52:55]\n"
    "ds_read_b64_tr_b16 v[176:177], v232 offset:28672\n"
    "v_mfma_f32_16x16x32_bf16 v[36:39], a[144:147], v[180:183], v[36:39]\n"
    "ds_read_b64_tr_b16 v[178:179], v232 offset:29184\n"
    "s_waitcnt lgkmcnt(10)\n"
    "v_mfma_f32_16x16x32_bf16 v[40:43], a[144:147], v[184:187], v[40:43]\n"
    "ds_read_b64_tr_b16 v[180:181], v233 offset:28672\n"
    "v_mfma_f32_16x16x32_bf16 v[56:59], a[148:151], v[184:187], v[56:59]\n"
    "ds_read_b64_tr_b16 v[182:183], v233 offset:29184\n"
    "buffer_load_dwordx4 a[220:223], v253, s[44:47], s78 offen offset:3072 sc1\n"
    "v_mfma_f32_16x16x32_bf16 v[60:63], a[148:151], v[188:191], v[60:63]\n"
    "ds_read_b64_tr_b16 v[184:185], v234 offset:28672\n"
    "v_mfma_f32_16x16x32_bf16 v[44:47], a[144:147], v[188:191], v[44:47]\n"
    "ds_read_b64_tr_b16 v[186:187], v234 offset:29184\n"
    "s_waitcnt vmcnt(33) lgkmcnt(10)\n"
    "v_mfma_f32_16x16x32_bf16 v[0:3], v[160:163], a[152:155], v[0:3]\n"
    "ds_read_b64_tr_b16 v[188:189], v235 offset:28672\n"
    "s_waitcnt vmcnt(18)\n"
    "v_mfma_f32_16x16x32_bf16 v[16:19], v[160:163], a[156:159], v[16:19]\n"
    "ds_read_b64_tr_b16 v[190:191], v235 offset:29184\n"
    "s_barrier\n"
    "v_mfma_f32_16x16x32_bf16 v[20:23], v[164:167], a[156:159], v[20:23]\n"
    "ds_read_b64_tr_b16 v[160:161], v224\n"
    "s_add_u32 m0, s85, 0x10000\n"
    "v_mfma_f32_16x16x32_bf16 v[4:7], v[164:167], a[152:155], v[4:7]\n"
    "ds_read_b64_tr_b16 v[162:163], v224 offset:512\n"
    "s_waitcnt lgkmcnt(10)\n"
    "v_mfma_f32_16x16x32_bf16 v[8:11], v[168:171], a[152:155], v[8:11]\n"
    "ds_read_b64_tr_b16 v[164:165], v225\n"
    "buffer_load_dwordx4 v236, s[24:27], s63 offen lds\n"
    "v_mfma_f32_16x16x32_bf16 v[24:27], v[168:171], a[156:159], v[24:27]\n"
    "ds_read_b64_tr_b16 v[166:167], v225 offset:512\n"
    "buffer_load_dwordx4 a[224:227], v252, s[44:47], s79 offen sc1\n"
    "v_mfma_f32_16x16x32_bf16 v[28:31], v[172:175], a[156:159], v[28:31]\n"
    "ds_read_b64_tr_b16 v[168:169], v226\n"
    "v_mfma_f32_16x16x32_bf16 v[12:15], v[172:175], a[152:155], v[12:15]\n"
    "ds_read_b64_tr_b16 v[170:171], v226 offset:512\n"
    "s_waitcnt lgkmcnt(10)\n"
    "v_mfma_f32_16x16x32_bf16 v[32:35], a[152:155], v[176:179], v[32:35]\n"
    "ds_read_b64_tr_b16 v[172:173], v227\n"
    "v_mfma_f32_16x16x32_bf16 v[48:51], a[156:159], v[176:179], v[48:51]\n"
    "ds_read_b64_tr_b16 v[174:175], v227 offset:512\n"
    "v_mfma_f32_16x16x32_bf16 v[52:55], a[156:159], v[180:183], v[52:55]\n"
    "ds_read_b64_tr_b16 v[176:177], v224 offset:8192\n"
    "buffer_load_dwordx4 v237, s[24:27], s63 offen offset:1024 lds\n"
    "v_mfma_f32_16x16x32_bf16 v[36:39], a[152:155], v[180:183], v[36:39]\n"
    "ds_read_b64_tr_b16 v[178:179], v224 offset:8704\n"
    "s_waitcnt lgkmcnt(10)\n"
    "v_mfma_f32_16x16x32_bf16 v[40:43], a[152:155], v[184:187], v[40:43]\n"
    "ds_read_b64_tr_b16 v[180:181], v225 offset:8192\n"
    "v_mfma_f32_16x16x32_bf16 v[56:59], a[156:159], v[184:187], v[56:59]\n"
    "ds_read_b64_tr_b16 v[182:183], v225 offset:8704\n"
    "buffer_load_dwordx4 a[228:231], v252, s[44:47], s79 offen offset:1024 sc1\n"
    "v_mfma_f32_16x16x32_bf16 v[60:63], a[156:159], v[188:191], v[60:63]\n"
    "ds_read_b64_tr_b16 v[184:185], v226 offset:8192\n"
    "v_mfma_f32_16x16x32_bf16 v[44:47], a[152:155], v[188:191], v[44:47]\n"
    "ds_read_b64_tr_b16 v[186:187], v226 offset:8704\n"
    "s_waitcnt lgkmcnt(10)\n"
    "v_mfma_f32_16x16x32_bf16 v[0:3], v[160:163], a[160:163], v[0:3]\n"
    "ds_read_b64_tr_b16 v[188:189], v227 offset:8192\n"
    "v_mfma_f32_16x16x32_bf16 v[16:19], v[160:163], a[164:167], v[16:19]\n"
    "ds_read_b64_tr_b16 v[190:191], v227 offset:8704\n"
    "v_mfma_f32_16x16x32_bf16 v[20:23], v[164:167], a[164:167], v[20:23]\n"
    "ds_read_b64_tr_b16 v[160:161], v224 offset:4096\n"
    "buffer_load_dwordx4 v238, s[24:27], s63 offen offset:2048 lds\n"
    "v_mfma_f32_16x16x32_bf16 v[4:7], v[164:167], a[160:163], v[4:7]\n"
    "ds_read_b64_tr_b16 v[162:163], v224 offset:4608\n"
    "s_waitcnt lgkmcnt(10)\n"
    "v_mfma_f32_16x16x32_bf16 v[8:11], v[168:171], a[160:163], v[8:11]\n"
    "ds_read_b64_tr_b16 v[164:165], v225 offset:4096\n"
    "v_mfma_f32_16x16x32_bf16 v[24:27], v[168:171], a[164:167], v[24:27]\n"
    "ds_read_b64_tr_b16 v[166:167], v225 offset:4608\n"
    "buffer_load_dwordx4 a[232:235], v252, s[44:47], s79 offen offset:2048 sc1\n"
    "v_mfma_f32_16x16x32_bf16 v[28:31], v[172:175], a[164:167], v[28:31]\n"
    "ds_read_b64_tr_b16 v[168:169], v226 offset:4096\n"
    "v_mfma_f32_16x16x32_bf16 v[12:15], v[172:175], a[160:163], v[12:15]\n"
    "ds_read_b64_tr_b16 v[170:171], v226 offset:4608\n"
    "s_waitcnt lgkmcnt(10)\n"
    "v_mfma_f32_16x16x32_bf16 v[32:35], a[160:163], v[176:179], v[32:35]\n"
    "ds_read_b64_tr_b16 v[172:173], v227 offset:4096\n"
    "v_mfma_f32_16x16x32_bf16 v[48:51], a[164:167], v[176:179], v[48:51]\n"
    "ds_read_b64_tr_b16 v[174:175], v227 offset:4608\n"
    "v_mfma_f32_16x16x32_bf16 v[52:55], a[164:167], v[180:183], v[52:55]\n"
    "ds_read_b64_tr_b16 v[176:177], v224 offset:12288\n"
    "buffer_load_dwordx4 v239, s[24:27], s63 offen offset:3072 lds\n"
    "v_mfma_f32_16x16x32_bf16 v[36:39], a[160:163], v[180:183], v[36:39]\n"
    "ds_read_b64_tr_b16 v[178:179], v224 offset:12800\n"
    "s_waitcnt lgkmcnt(10)\n"
    "v_mfma_f32_16x16x32_bf16 v[40:43], a[160:163], v[184:187], v[40:43]\n"
    "ds_read_b64_tr_b16 v[180:181], v225 offset:12288\n"
    "v_mfma_f32_16x16x32_bf16 v[56:59], a[164:167], v[184:187], v[56:59]\n"
    "ds_read_b64_tr_b16 v[182:183], v225 offset:12800\n"
    "buffer_load_dwordx4 a[236:239], v252, s[44:47], s79 offen offset:3072 sc1\n"
    "v_mfma_f32_16x16x32_bf16 v[60:63], a[164:167], v[188:191], v[60:63]\n"
    "ds_read_b64_tr_b16 v[184:185], v226 offset:12288\n"
    "v_mfma_f32_16x16x32_bf16 v[44:47], a[160:163], v[188:191], v[44:47]\n"
    "ds_read_b64_tr_b16 v[186:187], v226 offset:12800\n"
    "s_waitcnt lgkmcnt(10)\n"
    "v_mfma_f32_16x16x32_bf16 v[0:3], v[160:163], a[168:171], v[0:3]\n"
    "ds_read_b64_tr_b16 v[188:189], v227 offset:12288\n"
    "s_add_u32 m0, s85, 0x14000\n"
    "v_mfma_f32_16x16x32_bf16 v[16:19], v[160:163], a[172:175], v[16:19]\n"
    "ds_read_b64_tr_b16 v[190:191], v227 offset:12800\n"
    "s_add_u32 s98, s63, s99\n"
    "v_mfma_f32_16x16x32_bf16 v[20:23], v[164:167], a[172:175], v[20:23]\n"
    "ds_read_b64_tr_b16 v[160:161], v224 offset:16384\n"
    "buffer_load_dwordx4 v236, s[24:27], s98 offen lds\n"
    "v_mfma_f32_16x16x32_bf16 v[4:7], v[164:167], a[168:171], v[4:7]\n"
    "ds_read_b64_tr_b16 v[162:163], v224 offset:16896\n"
    "s_waitcnt lgkmcnt(10)\n"
    "v_mfma_f32_16x16x32_bf16 v[8:11], v[168:171], a[168:171], v[8:11]\n"
    "ds_read_b64_tr_b16 v[164:165], v225 offset:16384\n"
    "v_mfma_f32_16x16x32_bf16 v[24:27], v[168:171], a[172:175], v[24:27]\n"
    "ds_read_b64_tr_b16 v[166:167], v225 offset:16896\n"
    "buffer_load_dwordx4 a[240:243], v253, s[44:47], s79 offen sc1\n"
    "v_mfma_f32_16x16x32_bf16 v[28:31], v[172:175], a[172:175], v[28:31]\n"
    "ds_read_b64_tr_b16 v[168:169], v226 offset:16384\n"
    "v_mfma_f32_16x16x32_bf16 v[12:15], v[172:175], a[168:171], v[12:15]\n"
    "ds_read_b64_tr_b16 v[170:171], v226 offset:16896\n"
    "s_waitcnt lgkmcnt(10)\n"
    "v_mfma_f32_16x16x32_bf16 v[32:35], a[168:171], v[176:179], v[32:35]\n"
    "ds_read_b64_tr_b16 v[172:173], v227 offset:16384\n"
    "buffer_load_dwordx4 v237, s[24:27], s98 offen offset:1024 lds\n"
    "v_mfma_f32_16x16x32_bf16 v[48:51], a[172:175], v[176:179], v[48:51]\n"
    "ds_read_b64_tr_b16 v[174:175], v227 offset:16896\n"
    "v_mfma_f32_16x16x32_bf16 v[52:55], a[172:175], v[180:183], v[52:55]\n"
    "ds_read_b64_tr_b16 v[176:177], v224 offset:24576\n"
    "v_mfma_f32_16x16x32_bf16 v[36:39], a[168:171], v[180:183], v[36:39]\n"
    "ds_read_b64_tr_b16 v[178:179], v224 offset:25088\n"
    "s_waitcnt lgkmcnt(10)\n"
    "v_mfma_f32_16x16x32_bf16 v[40:43], a[168:171], v[184:187], v[40:43]\n"
    "ds_read_b64_tr_b16 v[180:181], v225 offset:24576\n"
    "v_mfma_f32_16x16x32_bf16 v[56:59], a[172:175], v[184:187], v[56:59]\n"
    "ds_read_b64_tr_b16 v[182:183], v225 offset:25088\n"
    "buffer_load_dwordx4 a[244:247], v253, s[44:47], s79 offen offset:1024 sc1\n"
    "v_mfma_f32_16x16x32_bf16 v[60:63], a[172:175], v[188:191], v[60:63]\n"
    "ds_read_b64_tr_b16 v[184:185], v226 offset:24576\n"
    "buffer_load_dwordx4 v238, s[24:27], s98 offen offset:2048 lds\n"
    "v_mfma_f32_16x16x32_bf16 v[44:47], a[168:171], v[188:191], v[44:47]\n"
    "ds_read_b64_tr_b16 v[186:187], v226 offset:25088\n"
    "s_waitcnt lgkmcnt(10)\n"
    "v_mfma_f32_16x16x32_bf16 v[0:3], v[160:163], a[176:179], v[0:3]\n"
    "ds_read_b64_tr_b16 v[188:189], v227 offset:24576\n"
    "v_mfma_f32_16x16x32_bf16 v[16:19], v[160:163], a[180:183], v[16:19]\n"
    "ds_read_b64_tr_b16 v[190:191], v227 offset:25088\n"
    "v_mfma_f32_16x16x32_bf16 v[20:23], v[164:167], a[180:183], v[20:23]\n"
    "ds_read_b64_tr_b16 v[160:161], v224 offset:20480\n"
    "v_mfma_f32_16x16x32_bf16 v[4:7], v[164:167], a[176:179], v[4:7]\n"
    "ds_read_b64_tr_b16 v[162:163], v224 offset:20992\n"
    "s_waitcnt lgkmcnt(10)\n"
    "v_mfma_f32_16x16x32_bf16 v[8:11], v[168:171], a[176:179], v[8:11]\n"
    "ds_read_b64_tr_b16 v[164:165], v225 offset:20480\n"
    "buffer_load_dwordx4 v239, s[24:27], s98 offen offset:3072 lds\n"
    "v_mfma_f32_16x16x32_bf16 v[24:27], v[168:171], a[180:183], v[24:27]\n"
    "ds_read_b64_tr_b16 v[166:167], v225 offset:20992\n"
    "buffer_load_dwordx4 a[248:251], v253, s[44:47], s79 offen offset:2048 sc1\n"
    "v_mfma_f32_16x16x32_bf16 v[28:31], v[172:175], a[180:183], v[28:31]\n"
    "ds_read_b64_tr_b16 v[168:169], v226 offset:20480\n"
    "v_mfma_f32_16x16x32_bf16 v[12:15], v[172:175], a[176:179], v[12:15]\n"
    "ds_read_b64_tr_b16 v[170:171], v226 offset:20992\n"
    "s_waitcnt lgkmcnt(10)\n"
    "v_mfma_f32_16x16x32_bf16 v[32:35], a[176:179], v[176:179], v[32:35]\n"
    "ds_read_b64_tr_b16 v[172:173], v227 offset:20480\n"
    "v_mfma_f32_16x16x32_bf16 v[48:51], a[180:183], v[176:179], v[48:51]\n"
    "ds_read_b64_tr_b16 v[174:175], v227 offset:20992\n"
    "v_mfma_f32_16x16x32_bf16 v[52:55], a[180:183], v[180:183], v[52:55]\n"
    "ds_read_b64_tr_b16 v[176:177], v224 offset:28672\n"
    "v_mfma_f32_16x16x32_bf16 v[36:39], a[176:179], v[180:183], v[36:39]\n"
    "ds_read_b64_tr_b16 v[178:179], v224 offset:29184\n"
    "s_waitcnt lgkmcnt(10)\n"
    "v_mfma_f32_16x16x32_bf16 v[40:43], a[176:179], v[184:187], v[40:43]\n"
    "ds_read_b64_tr_b16 v[180:181], v225 offset:28672\n"
    "v_mfma_f32_16x16x32_bf16 v[56:59], a[180:183], v[184:187], v[56:59]\n"
    "ds_read_b64_tr_b16 v[182:183], v225 offset:29184\n"
    "buffer_load_dwordx4 a[252:255], v253, s[44:47], s79 offen offset:3072 sc1\n"
    "v_mfma_f32_16x16x32_bf16 v[60:63], a[180:183], v[188:191], v[60:63]\n"
    "ds_read_b64_tr_b16 v[184:185], v226 offset:28672\n"
    "v_mfma_f32_16x16x32_bf16 v[44:47], a[176:179], v[188:191], v[44:47]\n"
    "ds_read_b64_tr_b16 v[186:187], v226 offset:29184\n"
    "s_waitcnt vmcnt(32) lgkmcnt(10)\n"
    "v_mfma_f32_16x16x32_bf16 v[0:3], v[160:163], a[184:187], v[0:3]\n"
    "ds_read_b64_tr_b16 v[188:189], v227 offset:28672\n"
    "s_waitcnt vmcnt(18)\n"
    "v_mfma_f32_16x16x32_bf16 v[16:19], v[160:163], a[188:191], v[16:19]\n"
    "ds_read_b64_tr_b16 v[190:191], v227 offset:29184\n"
    "s_barrier\n"
    "v_mfma_f32_16x16x32_bf16 v[20:23], v[164:167], a[188:191], v[20:23]\n"
    "ds_read_b64_tr_b16 v[160:161], v224 offset:32768\n"
    "s_add_u32 m0, s85, 0x18000\n"
    "v_mfma_f32_16x16x32_bf16 v[4:7], v[164:167], a[184:187], v[4:7]\n"
    "ds_read_b64_tr_b16 v[162:163], v224 offset:33280\n"
    "s_waitcnt lgkmcnt(10)\n"
    "v_mfma_f32_16x16x32_bf16 v[8:11], v[168:171], a[184:187], v[8:11]\n"
    "ds_read_b64_tr_b16 v[164:165], v225 offset:32768\n"
    "s_add_u32 s98, s56, s19\n"
    "v_mfma_f32_16x16x32_bf16 v[24:27], v[168:171], a[188:191], v[24:27]\n"
    "ds_read_b64_tr_b16 v[166:167], v225 offset:33280\n"
    "buffer_load_dwordx4 v236, s[24:27], s98 offen lds\n"
    "v_mfma_f32_16x16x32_bf16 v[28:31], v[172:175], a[188:191], v[28:31]\n"
    "ds_read_b64_tr_b16 v[168:169], v226 offset:32768\n"
    "v_mfma_f32_16x16x32_bf16 v[12:15], v[172:175], a[184:187], v[12:15]\n"
    "ds_read_b64_tr_b16 v[170:171], v226 offset:33280\n"
    "s_waitcnt lgkmcnt(10)\n"
    "v_mfma_f32_16x16x32_bf16 v[32:35], a[184:187], v[176:179], v[32:35]\n"
    "ds_read_b64_tr_b16 v[172:173], v227 offset:32768\n"
    "v_mfma_f32_16x16x32_bf16 v[48:51], a[188:191], v[176:179], v[48:51]\n"
    "ds_read_b64_tr_b16 v[174:175], v227 offset:33280\n"
    "v_mfma_f32_16x16x32_bf16 v[52:55], a[188:191], v[180:183], v[52:55]\n"
    "ds_read_b64_tr_b16 v[176:177], v224 offset:40960\n"
    "buffer_load_dwordx4 v237, s[24:27], s98 offen offset:1024 lds\n"
    "v_mfma_f32_16x16x32_bf16 v[36:39], a[184:187], v[180:183], v[36:39]\n"
    "ds_read_b64_tr_b16 v[178:179], v224 offset:41472\n"
    "s_waitcnt lgkmcnt(10)\n"
    "v_mfma_f32_16x16x32_bf16 v[40:43], a[184:187], v[184:187], v[40:43]\n"
    "ds_read_b64_tr_b16 v[180:181], v225 offset:40960\n"
    "v_mfma_f32_16x16x32_bf16 v[56:59], a[188:191], v[184:187], v[56:59]\n"
    "ds_read_b64_tr_b16 v[182:183], v225 offset:41472\n"
    "v_mfma_f32_16x16x32_bf16 v[60:63], a[188:191], v[188:191], v[60:63]\n"
    "ds_read_b64_tr_b16 v[184:185], v226 offset:40960\n"
    "v_mfma_f32_16x16x32_bf16 v[44:47], a[184:187], v[188:191], v[44:47]\n"
    "ds_read_b64_tr_b16 v[186:187], v226 offset:41472\n"
    "s_waitcnt lgkmcnt(10)\n"
    "v_mfma_f32_16x16x32_bf16 v[0:3], v[160:163], a[192:195], v[0:3]\n"
    "ds_read_b64_tr_b16 v[188:189], v227 offset:40960\n"
    "v_mfma_f32_16x16x32_bf16 v[16:19], v[160:163], a[196:199], v[16:19]\n"
    "ds_read_b64_tr_b16 v[190:191], v227 offset:41472\n"
    "v_mfma_f32_16x16x32_bf16 v[20:23], v[164:167], a[196:199], v[20:23]\n"
    "ds_read_b64_tr_b16 v[160:161], v224 offset:36864\n"
    "buffer_load_dwordx4 v238, s[24:27], s98 offen offset:2048 lds\n"
    "v_mfma_f32_16x16x32_bf16 v[4:7], v[164:167], a[192:195], v[4:7]\n"
    "ds_read_b64_tr_b16 v[162:163], v224 offset:37376\n"
    "s_waitcnt lgkmcnt(10)\n"
    "v_mfma_f32_16x16x32_bf16 v[8:11], v[168:171], a[192:195], v[8:11]\n"
    "ds_read_b64_tr_b16 v[164:165], v225 offset:36864\n"
    "v_mfma_f32_16x16x32_bf16 v[24:27], v[168:171], a[196:199], v[24:27]\n"
    "ds_read_b64_tr_b16 v[166:167], v225 offset:37376\n"
    "v_mfma_f32_16x16x32_bf16 v[28:31], v[172:175], a[196:199], v[28:31]\n"
    "ds_read_b64_tr_b16 v[168:169], v226 offset:36864\n"
    "v_mfma_f32_16x16x32_bf16 v[12:15], v[172:175], a[192:195], v[12:15]\n"
    "ds_read_b64_tr_b16 v[170:171], v226 offset:37376\n"
    "s_waitcnt lgkmcnt(10)\n"
    "v_mfma_f32_16x16x32_bf16 v[32:35], a[192:195], v[176:179], v[32:35]\n"
    "ds_read_b64_tr_b16 v[172:173], v227 offset:36864\n"
    "v_mbcnt_lo_u32_b32 v251, -1, 0\n"
    "v_mfma_f32_16x16x32_bf16 v[48:51], a[196:199], v[176:179], v[48:51]\n"
    "ds_read_b64_tr_b16 v[174:175], v227 offset:37376\n"
    "v_mbcnt_hi_u32_b32 v251, -1, v251\n"
    "v_mfma_f32_16x16x32_bf16 v[52:55], a[196:199], v[180:183], v[52:55]\n"
    "ds_read_b64_tr_b16 v[176:177], v224 offset:45056\n"
    "buffer_load_dwordx4 v239, s[24:27], s98 offen offset:3072 lds\n"
    "v_mfma_f32_16x16x32_bf16 v[36:39], a[192:195], v[180:183], v[36:39]\n"
    "ds_read_b64_tr_b16 v[178:179], v224 offset:45568\n"
    "s_waitcnt lgkmcnt(10)\n"
    "v_mfma_f32_16x16x32_bf16 v[40:43], a[192:195], v[184:187], v[40:43]\n"
    "ds_read_b64_tr_b16 v[180:181], v225 offset:45056\n"
    "v_lshrrev_b32 v249, 4, v251\n"
    "v_mfma_f32_16x16x32_bf16 v[56:59], a[196:199], v[184:187], v[56:59]\n"
    "ds_read_b64_tr_b16 v[182:183], v225 offset:45568\n"
    "v_bfe_u32 v250, v251, 2, 2\n"
    "v_mfma_f32_16x16x32_bf16 v[60:63], a[196:199], v[188:191], v[60:63]\n"
    "ds_read_b64_tr_b16 v[184:185], v226 offset:45056\n"
    "v_lshl_add_u32 v250, v249, 3, v250\n"
    "v_mfma_f32_16x16x32_bf16 v[44:47], a[192:195], v[188:191], v[44:47]\n"
    "ds_read_b64_tr_b16 v[186:187], v226 offset:45568\n"
    "s_waitcnt lgkmcnt(10)\n"
    "v_mfma_f32_16x16x32_bf16 v[0:3], v[160:163], a[200:203], v[0:3]\n"
    "ds_read_b64_tr_b16 v[188:189], v227 offset:45056\n"
    "s_add_u32 m0, s85, 0\n"
    "v_mfma_f32_16x16x32_bf16 v[16:19], v[160:163], a[204:207], v[16:19]\n"
    "ds_read_b64_tr_b16 v[190:191], v227 offset:45568\n"
    "s_add_u32 s98, s57, s19\n"
    "v_mfma_f32_16x16x32_bf16 v[20:23], v[164:167], a[204:207], v[20:23]\n"
    "ds_read_b64_tr_b16 v[160:161], v224 offset:49152\n"
    "buffer_load_dwordx4 v236, s[24:27], s98 offen lds\n"
    "v_mfma_f32_16x16x32_bf16 v[4:7], v[164:167], a[200:203], v[4:7]\n"
    "ds_read_b64_tr_b16 v[162:163], v224 offset:49664\n"
    "s_waitcnt lgkmcnt(10)\n"
    "v_mfma_f32_16x16x32_bf16 v[8:11], v[168:171], a[200:203], v[8:11]\n"
    "ds_read_b64_tr_b16 v[164:165], v225 offset:49152\n"
    "v_lshlrev_b32 v250, 6, v250\n"
    "v_mfma_f32_16x16x32_bf16 v[24:27], v[168:171], a[204:207], v[24:27]\n"
    "ds_read_b64_tr_b16 v[166:167], v225 offset:49664\n"
    "v_and_b32 v249, 1, v249\n"
    "v_mfma_f32_16x16x32_bf16 v[28:31], v[172:175], a[204:207], v[28:31]\n"
    "ds_read_b64_tr_b16 v[168:169], v226 offset:49152\n"
    "v_lshl_add_u32 v250, v249, 5, v250\n"
    "v_mfma_f32_16x16x32_bf16 v[12:15], v[172:175], a[200:203], v[12:15]\n"
    "ds_read_b64_tr_b16 v[170:171], v226 offset:49664\n"
    "s_waitcnt lgkmcnt(10)\n"
    "v_mfma_f32_16x16x32_bf16 v[32:35], a[200:203], v[176:179], v[32:35]\n"
    "ds_read_b64_tr_b16 v[172:173], v227 offset:49152\n"
    "buffer_load_dwordx4 v237, s[24:27], s98 offen offset:1024 lds\n"
    "v_mfma_f32_16x16x32_bf16 v[48:51], a[204:207], v[176:179], v[48:51]\n"
    "ds_read_b64_tr_b16 v[174:175], v227 offset:49664\n"
    "v_and_b32 v249, 3, v251\n"
    "v_mfma_f32_16x16x32_bf16 v[52:55], a[204:207], v[180:183], v[52:55]\n"
    "ds_read_b64_tr_b16 v[176:177], v224 offset:57344\n"
    "v_lshl_add_u32 v232, v249, 3, v250\n"
    "v_mfma_f32_16x16x32_bf16 v[36:39], a[200:203], v[180:183], v[36:39]\n"
    "ds_read_b64_tr_b16 v[178:179], v224 offset:57856\n"
    "s_waitcnt lgkmcnt(10)\n"
    "v_mfma_f32_16x16x32_bf16 v[40:43], a[200:203], v[184:187], v[40:43]\n"
    "ds_read_b64_tr_b16 v[180:181], v225 offset:57344\n"
    "v_xor_b32 v233, 32, v232\n"
    "v_mfma_f32_16x16x32_bf16 v[56:59], a[204:207], v[184:187], v[56:59]\n"
    "ds_read_b64_tr_b16 v[182:183], v225 offset:57856\n"
    "v_add_u32 v234, 0x10000, v232\n"
    "v_mfma_f32_16x16x32_bf16 v[60:63], a[204:207], v[188:191], v[60:63]\n"
    "ds_read_b64_tr_b16 v[184:185], v226 offset:57344\n"
    "buffer_load_dwordx4 v238, s[24:27], s98 offen offset:2048 lds\n"
    "v_mfma_f32_16x16x32_bf16 v[44:47], a[200:203], v[188:191], v[44:47]\n"
    "ds_read_b64_tr_b16 v[186:187], v226 offset:57856\n"
    "s_waitcnt lgkmcnt(10)\n"
    "v_mfma_f32_16x16x32_bf16 v[0:3], v[160:163], a[208:211], v[0:3]\n"
    "ds_read_b64_tr_b16 v[188:189], v227 offset:57344\n"
    "v_add_u32 v235, 0x10000, v233\n"
    "v_mfma_f32_16x16x32_bf16 v[16:19], v[160:163], a[212:215], v[16:19]\n"
    "ds_read_b64_tr_b16 v[190:191], v227 offset:57856\n"
    "v_mfma_f32_16x16x32_bf16 v[20:23], v[164:167], a[212:215], v[20:23]\n"
    "ds_read_b64_tr_b16 v[160:161], v224 offset:53248\n"
    "v_mfma_f32_16x16x32_bf16 v[4:7], v[164:167], a[208:211], v[4:7]\n"
    "ds_read_b64_tr_b16 v[162:163], v224 offset:53760\n"
    "s_waitcnt lgkmcnt(10)\n"
    "v_mfma_f32_16x16x32_bf16 v[8:11], v[168:171], a[208:211], v[8:11]\n"
    "ds_read_b64_tr_b16 v[164:165], v225 offset:53248\n"
    "buffer_load_dwordx4 v239, s[24:27], s98 offen offset:3072 lds\n"
    "v_mfma_f32_16x16x32_bf16 v[24:27], v[168:171], a[212:215], v[24:27]\n"
    "ds_read_b64_tr_b16 v[166:167], v225 offset:53760\n"
    "v_mfma_f32_16x16x32_bf16 v[28:31], v[172:175], a[212:215], v[28:31]\n"
    "ds_read_b64_tr_b16 v[168:169], v226 offset:53248\n"
    "v_mfma_f32_16x16x32_bf16 v[12:15], v[172:175], a[208:211], v[12:15]\n"
    "ds_read_b64_tr_b16 v[170:171], v226 offset:53760\n"
    "s_waitcnt lgkmcnt(10)\n"
    "v_mfma_f32_16x16x32_bf16 v[32:35], a[208:211], v[176:179], v[32:35]\n"
    "ds_read_b64_tr_b16 v[172:173], v227 offset:53248\n"
    "v_mfma_f32_16x16x32_bf16 v[48:51], a[212:215], v[176:179], v[48:51]\n"
    "ds_read_b64_tr_b16 v[174:175], v227 offset:53760\n"
    "v_mfma_f32_16x16x32_bf16 v[52:55], a[212:215], v[180:183], v[52:55]\n"
    "ds_read_b64_tr_b16 v[176:177], v224 offset:61440\n"
    "v_mfma_f32_16x16x32_bf16 v[36:39], a[208:211], v[180:183], v[36:39]\n"
    "ds_read_b64_tr_b16 v[178:179], v224 offset:61952\n"
    "s_waitcnt lgkmcnt(10)\n"
    "v_mfma_f32_16x16x32_bf16 v[40:43], a[208:211], v[184:187], v[40:43]\n"
    "ds_read_b64_tr_b16 v[180:181], v225 offset:61440\n"
    "v_mfma_f32_16x16x32_bf16 v[56:59], a[212:215], v[184:187], v[56:59]\n"
    "ds_read_b64_tr_b16 v[182:183], v225 offset:61952\n"
    "v_mfma_f32_16x16x32_bf16 v[60:63], a[212:215], v[188:191], v[60:63]\n"
    "ds_read_b64_tr_b16 v[184:185], v226 offset:61440\n"
    "v_mfma_f32_16x16x32_bf16 v[44:47], a[208:211], v[188:191], v[44:47]\n"
    "ds_read_b64_tr_b16 v[186:187], v226 offset:61952\n"
    "s_waitcnt vmcnt(24) lgkmcnt(10)\n"
    "v_mfma_f32_16x16x32_bf16 v[0:3], v[160:163], a[216:219], v[0:3]\n"
    "ds_read_b64_tr_b16 v[188:189], v227 offset:61440\n"
    "s_waitcnt vmcnt(10)\n"
    "v_mfma_f32_16x16x32_bf16 v[16:19], v[160:163], a[220:223], v[16:19]\n"
    "ds_read_b64_tr_b16 v[190:191], v227 offset:61952\n"
    "s_barrier\n"
    "v_mfma_f32_16x16x32_bf16 v[20:23], v[164:167], a[220:223], v[20:23]\n"
    "ds_read_b64_tr_b16 v[160:161], v228\n"
    "s_add_u32 m0, s85, 0x4000\n"
    "v_mfma_f32_16x16x32_bf16 v[4:7], v[164:167], a[216:219], v[4:7]\n"
    "ds_read_b64_tr_b16 v[162:163], v228 offset:512\n"
    "s_waitcnt lgkmcnt(10)\n"
    "v_mfma_f32_16x16x32_bf16 v[8:11], v[168:171], a[216:219], v[8:11]\n"
    "ds_read_b64_tr_b16 v[164:165], v229\n"
    "s_add_u32 s98, s58, s19\n"
    "v_mfma_f32_16x16x32_bf16 v[24:27], v[168:171], a[220:223], v[24:27]\n"
    "ds_read_b64_tr_b16 v[166:167], v229 offset:512\n"
    "buffer_load_dwordx4 v236, s[24:27], s98 offen lds\n"
    "v_mfma_f32_16x16x32_bf16 v[28:31], v[172:175], a[220:223], v[28:31]\n"
    "ds_read_b64_tr_b16 v[168:169], v230\n"
    "v_mfma_f32_16x16x32_bf16 v[12:15], v[172:175], a[216:219], v[12:15]\n"
    "ds_read_b64_tr_b16 v[170:171], v230 offset:512\n"
    "s_waitcnt lgkmcnt(10)\n"
    "v_mfma_f32_16x16x32_bf16 v[32:35], a[216:219], v[176:179], v[32:35]\n"
    "ds_read_b64_tr_b16 v[172:173], v231\n"
    "v_mfma_f32_16x16x32_bf16 v[48:51], a[220:223], v[176:179], v[48:51]\n"
    "ds_read_b64_tr_b16 v[174:175], v231 offset:512\n"
    "v_mfma_f32_16x16x32_bf16 v[52:55], a[220:223], v[180:183], v[52:55]\n"
    "ds_read_b64_tr_b16 v[176:177], v228 offset:8192\n"
    "buffer_load_dwordx4 v237, s[24:27], s98 offen offset:1024 lds\n"
    "v_mfma_f32_16x16x32_bf16 v[36:39], a[216:219], v[180:183], v[36:39]\n"
    "ds_read_b64_tr_b16 v[178:179], v228 offset:8704\n"
    "s_waitcnt lgkmcnt(10)\n"
    "v_mfma_f32_16x16x32_bf16 v[40:43], a[216:219], v[184:187], v[40:43]\n"
    "ds_read_b64_tr_b16 v[180:181], v229 offset:8192\n"
    "v_mfma_f32_16x16x32_bf16 v[56:59], a[220:223], v[184:187], v[56:59]\n"
    "ds_read_b64_tr_b16 v[182:183], v229 offset:8704\n"
    "v_mfma_f32_16x16x32_bf16 v[60:63], a[220:223], v[188:191], v[60:63]\n"
    "ds_read_b64_tr_b16 v[184:185], v230 offset:8192\n"
    "v_mfma_f32_16x16x32_bf16 v[44:47], a[216:219], v[188:191], v[44:47]\n"
    "ds_read_b64_tr_b16 v[186:187], v230 offset:8704\n"
    "s_waitcnt lgkmcnt(10)\n"
    "v_mfma_f32_16x16x32_bf16 v[0:3], v[160:163], a[224:227], v[0:3]\n"
    "ds_read_b64_tr_b16 v[188:189], v231 offset:8192\n"
    "v_mbcnt_lo_u32_b32 v251, -1, 0\n"
    "v_mfma_f32_16x16x32_bf16 v[16:19], v[160:163], a[228:231], v[16:19]\n"
    "ds_read_b64_tr_b16 v[190:191], v231 offset:8704\n"
    "v_mbcnt_hi_u32_b32 v251, -1, v251\n"
    "v_mfma_f32_16x16x32_bf16 v[20:23], v[164:167], a[228:231], v[20:23]\n"
    "ds_read_b64_tr_b16 v[160:161], v228 offset:4096\n"
    "buffer_load_dwordx4 v238, s[24:27], s98 offen offset:2048 lds\n"
    "v_mfma_f32_16x16x32_bf16 v[4:7], v[164:167], a[224:227], v[4:7]\n"
    "ds_read_b64_tr_b16 v[162:163], v228 offset:4608\n"
    "s_waitcnt lgkmcnt(10)\n"
    "v_mfma_f32_16x16x32_bf16 v[8:11], v[168:171], a[224:227], v[8:11]\n"
    "ds_read_b64_tr_b16 v[164:165], v229 offset:4096\n"
    "v_lshrrev_b32 v204, 3, v251\n"
    "v_mfma_f32_16x16x32_bf16 v[24:27], v[168:171], a[228:231], v[24:27]\n"
    "ds_read_b64_tr_b16 v[166:167], v229 offset:4608\n"
    "v_lshlrev_b32 v204, 10, v204\n"
    "v_mfma_f32_16x16x32_bf16 v[28:31], v[172:175], a[228:231], v[28:31]\n"
    "ds_read_b64_tr_b16 v[168:169], v230 offset:4096\n"
    "v_mfma_f32_16x16x32_bf16 v[12:15], v[172:175], a[224:227], v[12:15]\n"
    "ds_read_b64_tr_b16 v[170:171], v230 offset:4608\n"
    "s_waitcnt lgkmcnt(10)\n"
    "v_mfma_f32_16x16x32_bf16 v[32:35], a[224:227], v[176:179], v[32:35]\n"
    "ds_read_b64_tr_b16 v[172:173], v231 offset:4096\n"
    "v_mfma_f32_16x16x32_bf16 v[48:51], a[228:231], v[176:179], v[48:51]\n"
    "ds_read_b64_tr_b16 v[174:175], v231 offset:4608\n"
    "v_mfma_f32_16x16x32_bf16 v[52:55], a[228:231], v[180:183], v[52:55]\n"
    "ds_read_b64_tr_b16 v[176:177], v228 offset:12288\n"
    "buffer_load_dwordx4 v239, s[24:27], s98 offen offset:3072 lds\n"
    "v_mfma_f32_16x16x32_bf16 v[36:39], a[224:227], v[180:183], v[36:39]\n"
    "ds_read_b64_tr_b16 v[178:179], v228 offset:12800\n"
    "s_waitcnt lgkmcnt(10)\n"
    "v_mfma_f32_16x16x32_bf16 v[40:43], a[224:227], v[184:187], v[40:43]\n"
    "ds_read_b64_tr_b16 v[180:181], v229 offset:12288\n"
    "v_mfma_f32_16x16x32_bf16 v[56:59], a[228:231], v[184:187], v[56:59]\n"
    "ds_read_b64_tr_b16 v[182:183], v229 offset:12800\n"
    "v_mfma_f32_16x16x32_bf16 v[60:63], a[228:231], v[188:191], v[60:63]\n"
    "ds_read_b64_tr_b16 v[184:185], v230 offset:12288\n"
    "v_mfma_f32_16x16x32_bf16 v[44:47], a[224:227], v[188:191], v[44:47]\n"
    "ds_read_b64_tr_b16 v[186:187], v230 offset:12800\n"
    "s_waitcnt lgkmcnt(10)\n"
    "v_mfma_f32_16x16x32_bf16 v[0:3], v[160:163], a[232:235], v[0:3]\n"
    "ds_read_b64_tr_b16 v[188:189], v231 offset:12288\n"
    "s_add_u32 m0, s85, 0x8000\n"
    "v_mfma_f32_16x16x32_bf16 v[16:19], v[160:163], a[236:239], v[16:19]\n"
    "ds_read_b64_tr_b16 v[190:191], v231 offset:12800\n"
    "s_add_u32 s98, s59, s19\n"
    "v_mfma_f32_16x16x32_bf16 v[20:23], v[164:167], a[236:239], v[20:23]\n"
    "ds_read_b64_tr_b16 v[160:161], v228 offset:16384\n"
    "buffer_load_dwordx4 v236, s[24:27], s98 offen lds\n"
    "v_mfma_f32_16x16x32_bf16 v[4:7], v[164:167], a[232:235], v[4:7]\n"
    "ds_read_b64_tr_b16 v[162:163], v228 offset:16896\n"
    "s_waitcnt lgkmcnt(10)\n"
    "v_mfma_f32_16x16x32_bf16 v[8:11], v[168:171], a[232:235], v[8:11]\n"
    "ds_read_b64_tr_b16 v[164:165], v229 offset:16384\n"
    "v_mfma_f32_16x16x32_bf16 v[24:27], v[168:171], a[236:239], v[24:27]\n"
    "ds_read_b64_tr_b16 v[166:167], v229 offset:16896\n"
    "v_mfma_f32_16x16x32_bf16 v[28:31], v[172:175], a[236:239], v[28:31]\n"
    "ds_read_b64_tr_b16 v[168:169], v230 offset:16384\n"
    "v_mfma_f32_16x16x32_bf16 v[12:15], v[172:175], a[232:235], v[12:15]\n"
    "ds_read_b64_tr_b16 v[170:171], v230 offset:16896\n"
    "s_waitcnt lgkmcnt(10)\n"
    "v_mfma_f32_16x16x32_bf16 v[32:35], a[232:235], v[176:179], v[32:35]\n"
    "ds_read_b64_tr_b16 v[172:173], v231 offset:16384\n"
    "buffer_load_dwordx4 v237, s[24:27], s98 offen offset:1024 lds\n"
    "v_mfma_f32_16x16x32_bf16 v[48:51], a[236:239], v[176:179], v[48:51]\n"
    "ds_read_b64_tr_b16 v[174:175], v231 offset:16896\n"
    "v_mfma_f32_16x16x32_bf16 v[52:55], a[236:239], v[180:183], v[52:55]\n"
    "ds_read_b64_tr_b16 v[176:177], v228 offset:24576\n"
    "v_mfma_f32_16x16x32_bf16 v[36:39], a[232:235], v[180:183], v[36:39]\n"
    "ds_read_b64_tr_b16 v[178:179], v228 offset:25088\n"
    "s_waitcnt lgkmcnt(10)\n"
    "v_mfma_f32_16x16x32_bf16 v[40:43], a[232:235], v[184:187], v[40:43]\n"
    "ds_read_b64_tr_b16 v[180:181], v229 offset:24576\n"
    "v_mfma_f32_16x16x32_bf16 v[56:59], a[236:239], v[184:187], v[56:59]\n"
    "ds_read_b64_tr_b16 v[182:183], v229 offset:25088\n"
    "v_mfma_f32_16x16x32_bf16 v[60:63], a[236:239], v[188:191], v[60:63]\n"
    "ds_read_b64_tr_b16 v[184:185], v230 offset:24576\n"
    "buffer_load_dwordx4 v238, s[24:27], s98 offen offset:2048 lds\n"
    "v_mfma_f32_16x16x32_bf16 v[44:47], a[232:235], v[188:191], v[44:47]\n"
    "ds_read_b64_tr_b16 v[186:187], v230 offset:25088\n"
    "s_waitcnt lgkmcnt(10)\n"
    "v_mfma_f32_16x16x32_bf16 v[0:3], v[160:163], a[240:243], v[0:3]\n"
    "ds_read_b64_tr_b16 v[188:189], v231 offset:24576\n"
    "v_mfma_f32_16x16x32_bf16 v[16:19], v[160:163], a[244:247], v[16:19]\n"
    "ds_read_b64_tr_b16 v[190:191], v231 offset:25088\n"
    "v_mfma_f32_16x16x32_bf16 v[20:23], v[164:167], a[244:247], v[20:23]\n"
    "ds_read_b64_tr_b16 v[160:161], v228 offset:20480\n"
    "v_mfma_f32_16x16x32_bf16 v[4:7], v[164:167], a[240:243], v[4:7]\n"
    "ds_read_b64_tr_b16 v[162:163], v228 offset:20992\n"
    "s_waitcnt lgkmcnt(10)\n"
    "v_mfma_f32_16x16x32_bf16 v[8:11], v[168:171], a[240:243], v[8:11]\n"
    "ds_read_b64_tr_b16 v[164:165], v229 offset:20480\n"
    "buffer_load_dwordx4 v239, s[24:27], s98 offen offset:3072 lds\n"
    "v_mfma_f32_16x16x32_bf16 v[24:27], v[168:171], a[244:247], v[24:27]\n"
    "ds_read_b64_tr_b16 v[166:167], v229 offset:20992\n"
    "v_mfma_f32_16x16x32_bf16 v[28:31], v[172:175], a[244:247], v[28:31]\n"
    "ds_read_b64_tr_b16 v[168:169], v230 offset:20480\n"
    "v_mfma_f32_16x16x32_bf16 v[12:15], v[172:175], a[240:243], v[12:15]\n"
    "ds_read_b64_tr_b16 v[170:171], v230 offset:20992\n"
    "s_waitcnt lgkmcnt(10)\n"
    "v_mfma_f32_16x16x32_bf16 v[32:35], a[240:243], v[176:179], v[32:35]\n"
    "ds_read_b64_tr_b16 v[172:173], v231 offset:20480\n"
    "v_mfma_f32_16x16x32_bf16 v[48:51], a[244:247], v[176:179], v[48:51]\n"
    "ds_read_b64_tr_b16 v[174:175], v231 offset:20992\n"
    "v_mfma_f32_16x16x32_bf16 v[52:55], a[244:247], v[180:183], v[52:55]\n"
    "ds_read_b64_tr_b16 v[176:177], v228 offset:28672\n"
    "v_mfma_f32_16x16x32_bf16 v[36:39], a[240:243], v[180:183], v[36:39]\n"
    "ds_read_b64_tr_b16 v[178:179], v228 offset:29184\n"
    "s_waitcnt lgkmcnt(10)\n"
    "v_mfma_f32_16x16x32_bf16 v[40:43], a[240:243], v[184:187], v[40:43]\n"
    "ds_read_b64_tr_b16 v[180:181], v229 offset:28672\n"
    "v_mfma_f32_16x16x32_bf16 v[56:59], a[244:247], v[184:187], v[56:59]\n"
    "ds_read_b64_tr_b16 v[182:183], v229 offset:29184\n"
    "v_mfma_f32_16x16x32_bf16 v[60:63], a[244:247], v[188:191], v[60:63]\n"
    "ds_read_b64_tr_b16 v[184:185], v230 offset:28672\n"
    "v_mfma_f32_16x16x32_bf16 v[44:47], a[240:243], v[188:191], v[44:47]\n"
    "ds_read_b64_tr_b16 v[186:187], v230 offset:29184\n"
    "s_waitcnt vmcnt(16) lgkmcnt(10)\n"
    "v_mfma_f32_16x16x32_bf16 v[0:3], v[160:163], a[248:251], v[0:3]\n"
    "ds_read_b64_tr_b16 v[188:189], v231 offset:28672\n"
    "s_waitcnt vmcnt(8)\n"
    "v_mfma_f32_16x16x32_bf16 v[16:19], v[160:163], a[252:255], v[16:19]\n"
    "ds_read_b64_tr_b16 v[190:191], v231 offset:29184\n"
    "s_barrier\n"
    "v_mfma_f32_16x16x32_bf16 v[20:23], v[164:167], a[252:255], v[20:23]\n"
    "ds_read_b64_tr_b16 v[160:161], v228 offset:32768\n"
    "s_add_u32 m0, s85, 0xc000\n"
    "v_mfma_f32_16x16x32_bf16 v[4:7], v[164:167], a[248:251], v[4:7]\n"
    "ds_read_b64_tr_b16 v[162:163], v228 offset:33280\n"
    "s_waitcnt lgkmcnt(10)\n"
    "v_mfma_f32_16x16x32_bf16 v[8:11], v[168:171], a[248:251], v[8:11]\n"
    "ds_read_b64_tr_b16 v[164:165], v229 offset:32768\n"
    "s_add_u32 s98, s60, s19\n"
    "v_mfma_f32_16x16x32_bf16 v[24:27], v[168:171], a[252:255], v[24:27]\n"
    "ds_read_b64_tr_b16 v[166:167], v229 offset:33280\n"
    "buffer_load_dwordx4 v236, s[24:27], s98 offen lds\n"
    "v_mfma_f32_16x16x32_bf16 v[28:31], v[172:175], a[252:255], v[28:31]\n"
    "ds_read_b64_tr_b16 v[168:169], v230 offset:32768\n"
    "v_mfma_f32_16x16x32_bf16 v[12:15], v[172:175], a[248:251], v[12:15]\n"
    "ds_read_b64_tr_b16 v[170:171], v230 offset:33280\n"
    "s_waitcnt lgkmcnt(10)\n"
    "v_mfma_f32_16x16x32_bf16 v[32:35], a[248:251], v[176:179], v[32:35]\n"
    "ds_read_b64_tr_b16 v[172:173], v231 offset:32768\n"
    "v_mfma_f32_16x16x32_bf16 v[48:51], a[252:255], v[176:179], v[48:51]\n"
    "ds_read_b64_tr_b16 v[174:175], v231 offset:33280\n"
    "v_mfma_f32_16x16x32_bf16 v[52:55], a[252:255], v[180:183], v[52:55]\n"
    "ds_read_b64_tr_b16 v[176:177], v228 offset:36864\n"
    "buffer_load_dwordx4 v237, s[24:27], s98 offen offset:1024 lds\n"
    "v_mfma_f32_16x16x32_bf16 v[36:39], a[248:251], v[180:183], v[36:39]\n"
    "ds_read_b64_tr_b16 v[178:179], v228 offset:37376\n"
    "s_waitcnt lgkmcnt(10)\n"
    "v_mfma_f32_16x16x32_bf16 v[40:43], a[248:251], v[184:187], v[40:43]\n"
    "ds_read_b64_tr_b16 v[180:181], v229 offset:36864\n"
    "v_mfma_f32_16x16x32_bf16 v[56:59], a[252:255], v[184:187], v[56:59]\n"
    "ds_read_b64_tr_b16 v[182:183], v229 offset:37376\n"
    "v_mfma_f32_16x16x32_bf16 v[60:63], a[252:255], v[188:191], v[60:63]\n"
    "ds_read_b64_tr_b16 v[184:185], v230 offset:36864\n"
    "v_mfma_f32_16x16x32_bf16 v[44:47], a[248:251], v[188:191], v[44:47]\n"
    "ds_read_b64_tr_b16 v[186:187], v230 offset:37376\n"
    "s_waitcnt lgkmcnt(10)\n"
    "v_mfma_f32_16x16x32_bf16 v[64:67], v[160:163], a[0:3], 0\n"
    "ds_read_b64_tr_b16 v[188:189], v231 offset:36864\n"
    "s_getpc_b64 s[92:93]\n"
    ".Lct2:\n"
    "v_mfma_f32_16x16x32_bf16 v[80:83], v[160:163], a[4:7], 0\n"
    "ds_read_b64_tr_b16 v[190:191], v231 offset:37376\n"
    "s_and_b32 s93, s93, 0xffff\n"
    "v_mfma_f32_16x16x32_bf16 v[84:87], v[164:167], a[4:7], 0\n"
    "ds_read_b64_tr_b16 v[160:161], v228 offset:40960\n"
    "buffer_load_dwordx4 v238, s[24:27], s98 offen offset:2048 lds\n"
    "v_mfma_f32_16x16x32_bf16 v[68:71], v[164:167], a[0:3], 0\n"
    "ds_read_b64_tr_b16 v[162:163], v228 offset:41472\n"
    "s_waitcnt lgkmcnt(10)\n"
    "v_mfma_f32_16x16x32_bf16 v[72:75], v[168:171], a[0:3], 0\n"
    "ds_read_b64_tr_b16 v[164:165], v229 offset:40960\n"
    "s_mov_b32 s94, .Lend-.Lct2\n"
    "v_mfma_f32_16x16x32_bf16 v[88:91], v[168:171], a[4:7], 0\n"
    "ds_read_b64_tr_b16 v[166:167], v229 offset:41472\n"
    "s_min_u32 s94, s94, 0x3000\n"
    "v_mfma_f32_16x16x32_bf16 v[92:95], v[172:175], a[4:7], 0\n"
    "ds_read_b64_tr_b16 v[168:169], v230 offset:40960\n"
    "s_mov_b32 s95, 0x20000\n"
    "v_mfma_f32_16x16x32_bf16 v[76:79], v[172:175], a[0:3], 0\n"
    "ds_read_b64_tr_b16 v[170:171], v230 offset:41472\n"
    "s_waitcnt lgkmcnt(10)\n"
    "v_mfma_f32_16x16x32_bf16 v[64:67], v[176:179], a[8:11], v[64:67]\n"
    "ds_read_b64_tr_b16 v[172:173], v231 offset:40960\n"
    "s_and_b32 s91, s13, 0x7f\n"
    "v_mfma_f32_16x16x32_bf16 v[80:83], v[176:179], a[12:15], v[80:83]\n"
    "ds_read_b64_tr_b16 v[174:175], v231 offset:41472\n"
    "s_lshl_b32 s91, s91, 10\n"
    "v_mfma_f32_16x16x32_bf16 v[84:87], v[180:183], a[12:15], v[84:87]\n"
    "ds_read_b64_tr_b16 v[176:177], v228 offset:45056\n"
    "buffer_load_dwordx4 v239, s[24:27], s98 offen offset:3072 lds\n"
    "v_mfma_f32_16x16x32_bf16 v[68:71], v[180:183], a[8:11], v[68:71]\n"
    "ds_read_b64_tr_b16 v[178:179], v228 offset:45568\n"
    "s_waitcnt lgkmcnt(10)\n"
    "v_mfma_f32_16x16x32_bf16 v[72:75], v[184:187], a[8:11], v[72:75]\n"
    "ds_read_b64_tr_b16 v[180:181], v229 offset:45056\n"
    "buffer_load_dword v206, v240, s[92:95], s91 offen\n"
    "v_mfma_f32_16x16x32_bf16 v[88:91], v[184:187], a[12:15], v[88:91]\n"
    "ds_read_b64_tr_b16 v[182:183], v229 offset:45568\n"
    "v_cvt_pk_bf16_f32 v128, v0, v1\n"
    "v_mfma_f32_16x16x32_bf16 v[92:95], v[188:191], a[12:15], v[92:95]\n"
    "ds_read_b64_tr_b16 v[184:185], v230 offset:45056\n"
    "v_cvt_pk_bf16_f32 v129, v2, v3\n"
    "v_mfma_f32_16x16x32_bf16 v[76:79], v[188:191], a[8:11], v[76:79]\n"
    "ds_read_b64_tr_b16 v[186:187], v230 offset:45568\n"
    "s_waitcnt lgkmcnt(10)\n"
    "v_mfma_f32_16x16x32_bf16 v[64:67], v[160:163], a[16:19], v[64:67]\n"
    "ds_read_b64_tr_b16 v[188:189], v231 offset:45056\n"
    "s_add_u32 m0, s85, 0x10000\n"
    "v_mfma_f32_16x16x32_bf16 v[80:83], v[160:163], a[20:23], v[80:83]\n"
    "ds_read_b64_tr_b16 v[190:191], v231 offset:45568\n"
    "s_add_u32 s98, s61, s19\n"
    "v_mfma_f32_16x16x32_bf16 v[84:87], v[164:167], a[20:23], v[84:87]\n"
    "ds_read_b64_tr_b16 v[160:161], v224\n"
    "buffer_load_dwordx4 v236, s[24:27], s98 offen lds\n"
    "v_mfma_f32_16x16x32_bf16 v[68:71], v[164:167], a[16:19], v[68:71]\n"
    "ds_read_b64_tr_b16 v[162:163], v224 offset:512\n"
    "s_waitcnt lgkmcnt(10)\n"
    "v_mfma_f32_16x16x32_bf16 v[72:75], v[168:171], a[16:19], v[72:75]\n"
    "ds_read_b64_tr_b16 v[164:165], v225\n"
    "v_cvt_pk_bf16_f32 v130, v4, v5\n"
    "v_mfma_f32_16x16x32_bf16 v[88:91], v[168:171], a[20:23], v[88:91]\n"
    "ds_read_b64_tr_b16 v[166:167], v225 offset:512\n"
    "v_cvt_pk_bf16_f32 v131, v6, v7\n"
    "v_mfma_f32_16x16x32_bf16 v[92:95], v[172:175], a[20:23], v[92:95]\n"
    "ds_read_b64_tr_b16 v[168:169], v226\n"
    "v_cvt_pk_bf16_f32 v132, v16, v17\n"
    "v_mfma_f32_16x16x32_bf16 v[76:79], v[172:175], a[16:19], v[76:79]\n"
    "ds_read_b64_tr_b16 v[170:171], v226 offset:512\n"
    "s_waitcnt lgkmcnt(10)\n"
    "v_mfma_f32_16x16x32_bf16 v[64:67], v[176:179], a[24:27], v[64:67]\n"
    "ds_read_b64_tr_b16 v[172:173], v227\n"
    "buffer_load_dwordx4 v237, s[24:27], s98 offen offset:1024 lds\n"
    "v_mfma_f32_16x16x32_bf16 v[80:83], v[176:179], a[28:31], v[80:83]\n"
    "ds_read_b64_tr_b16 v[174:175], v227 offset:512\n"
    "s_add_u32 s99, s86, 0\n"
    "v_mfma_f32_16x16x32_bf16 v[84:87], v[180:183], a[28:31], v[84:87]\n"
    "ds_read_b64_tr_b16 v[176:177], v224 offset:4096\n"
    "buffer_store_dwordx4 v[128:131], v240, s[44:47], s99 offen sc1\n"
    "v_mfma_f32_16x16x32_bf16 v[68:71], v[180:183], a[24:27], v[68:71]\n"
    "ds_read_b64_tr_b16 v[178:179], v224 offset:4608\n"
    "s_waitcnt lgkmcnt(10)\n"
    "v_mfma_f32_16x16x32_bf16 v[72:75], v[184:187], a[24:27], v[72:75]\n"
    "ds_read_b64_tr_b16 v[180:181], v225 offset:4096\n"
    "v_cvt_pk_bf16_f32 v133, v18, v19\n"
    "v_mfma_f32_16x16x32_bf16 v[88:91], v[184:187], a[28:31], v[88:91]\n"
    "ds_read_b64_tr_b16 v[182:183], v225 offset:4608\n"
    "v_cvt_pk_bf16_f32 v134, v20, v21\n"
    "v_mfma_f32_16x16x32_bf16 v[92:95], v[188:191], a[28:31], v[92:95]\n"
    "ds_read_b64_tr_b16 v[184:185], v226 offset:4096\n"
    "buffer_load_dwordx4 v238, s[24:27], s98 offen offset:2048 lds\n"
    "v_mfma_f32_16x16x32_bf16 v[76:79], v[188:191], a[24:27], v[76:79]\n"
    "ds_read_b64_tr_b16 v[186:187], v226 offset:4608\n"
    "s_waitcnt lgkmcnt(10)\n"
    "v_mfma_f32_16x16x32_bf16 v[64:67], v[160:163], a[32:35], v[64:67]\n"
    "ds_read_b64_tr_b16 v[188:189], v227 offset:4096\n"
    "v_cvt_pk_bf16_f32 v135, v22, v23\n"
    "v_mfma_f32_16x16x32_bf16 v[80:83], v[160:163], a[36:39], v[80:83]\n"
    "ds_read_b64_tr_b16 v[190:191], v227 offset:4608\n"
    "v_cvt_pk_bf16_f32 v136, v8, v9\n"
    "v_mfma_f32_16x16x32_bf16 v[84:87], v[164:167], a[36:39], v[84:87]\n"
    "ds_read_b64_tr_b16 v[160:161], v224 offset:8192\n"
    "v_cvt_pk_bf16_f32 v137, v10, v11\n"
    "v_mfma_f32_16x16x32_bf16 v[68:71], v[164:167], a[32:35], v[68:71]\n"
    "ds_read_b64_tr_b16 v[162:163], v224 offset:8704\n"
    "s_waitcnt lgkmcnt(10)\n"
    "v_mfma_f32_16x16x32_bf16 v[72:75], v[168:171], a[32:35], v[72:75]\n"
    "ds_read_b64_tr_b16 v[164:165], v225 offset:8192\n"
    "buffer_load_dwordx4 v239, s[24:27], s98 offen offset:3072 lds\n"
    "v_mfma_f32_16x16x32_bf16 v[88:91], v[168:171], a[36:39], v[88:91]\n"
    "ds_read_b64_tr_b16 v[166:167], v225 offset:8704\n"
    "v_cvt_pk_bf16_f32 v138, v12, v13\n"
    "v_mfma_f32_16x16x32_bf16 v[92:95], v[172:175], a[36:39], v[92:95]\n"
    "ds_read_b64_tr_b16 v[168:169], v226 offset:8192\n"
    "v_cvt_pk_bf16_f32 v139, v14, v15\n"
    "v_mfma_f32_16x16x32_bf16 v[76:79], v[172:175], a[32:35], v[76:79]\n"
    "ds_read_b64_tr_b16 v[170:171], v226 offset:8704\n"
    "s_waitcnt lgkmcnt(10)\n"
    "v_mfma_f32_16x16x32_bf16 v[64:67], v[176:179], a[40:43], v[64:67]\n"
    "ds_read_b64_tr_b16 v[172:173], v227 offset:8192\n"
    "v_cvt_pk_bf16_f32 v140, v24, v25\n"
    "v_mfma_f32_16x16x32_bf16 v[80:83], v[176:179], a[44:47], v[80:83]\n"
    "ds_read_b64_tr_b16 v[174:175], v227 offset:8704\n"
    "v_cvt_pk_bf16_f32 v141, v26, v27\n"
    "v_mfma_f32_16x16x32_bf16 v[84:87], v[180:183], a[44:47], v[84:87]\n"
    "ds_read_b64_tr_b16 v[176:177], v224 offset:12288\n"
    "v_cvt_pk_bf16_f32 v142, v28, v29\n"
    "v_mfma_f32_16x16x32_bf16 v[68:71], v[180:183], a[40:43], v[68:71]\n"
    "ds_read_b64_tr_b16 v[178:179], v224 offset:12800\n"
    "s_waitcnt lgkmcnt(10)\n"
    "v_mfma_f32_16x16x32_bf16 v[72:75], v[184:187], a[40:43], v[72:75]\n"
    "ds_read_b64_tr_b16 v[180:181], v225 offset:12288\n"
    "v_cvt_pk_bf16_f32 v143, v30, v31\n"
    "v_mfma_f32_16x16x32_bf16 v[88:91], v[184:187], a[44:47], v[88:91]\n"
    "ds_read_b64_tr_b16 v[182:183], v225 offset:12800\n"
    "v_cvt_pk_bf16_f32 v144, v32, v33\n"
    "v_mfma_f32_16x16x32_bf16 v[92:95], v[188:191], a[44:47], v[92:95]\n"
    "ds_read_b64_tr_b16 v[184:185], v226 offset:12288\n"
    "v_cvt_pk_bf16_f32 v145, v34, v35\n"
    "v_mfma_f32_16x16x32_bf16 v[76:79], v[188:191], a[40:43], v[76:79]\n"
    "ds_read_b64_tr_b16 v[186:187], v226 offset:12800\n"
    "s_waitcnt lgkmcnt(10)\n"
    "v_mfma_f32_16x16x32_bf16 v[64:67], v[160:163], a[48:51], v[64:67]\n"
    "ds_read_b64_tr_b16 v[188:189], v227 offset:12288\n"
    "s_waitcnt vmcnt(10)\n"
    "v_mfma_f32_16x16x32_bf16 v[80:83], v[160:163], a[52:55], v[80:83]\n"
    "ds_read_b64_tr_b16 v[190:191], v227 offset:12800\n"
    "s_barrier\n"
    "v_mfma_f32_16x16x32_bf16 v[84:87], v[164:167], a[52:55], v[84:87]\n"
    "ds_read_b64_tr_b16 v[160:161], v224 offset:16384\n"
    "s_add_u32 m0, s85, 0x14000\n"
    "v_mfma_f32_16x16x32_bf16 v[68:71], v[164:167], a[48:51], v[68:71]\n"
    "ds_read_b64_tr_b16 v[162:163], v224 offset:16896\n"
    "s_waitcnt lgkmcnt(10)\n"
    "v_mfma_f32_16x16x32_bf16 v[72:75], v[168:171], a[48:51], v[72:75]\n"
    "ds_read_b64_tr_b16 v[164:165], v225 offset:16384\n"
    "s_add_u32 s98, s62, s19\n"
    "v_mfma_f32_16x16x32_bf16 v[88:91], v[168:171], a[52:55], v[88:91]\n"
    "ds_read_b64_tr_b16 v[166:167], v225 offset:16896\n"
    "buffer_load_dwordx4 v236, s[24:27], s98 offen lds\n"
    "v_mfma_f32_16x16x32_bf16 v[92:95], v[172:175], a[52:55], v[92:95]\n"
    "ds_read_b64_tr_b16 v[168:169], v226 offset:16384\n"
    "v_cvt_pk_bf16_f32 v146, v48, v49\n"
    "v_mfma_f32_16x16x32_bf16 v[76:79], v[172:175], a[48:51], v[76:79]\n"
    "ds_read_b64_tr_b16 v[170:171], v226 offset:16896\n"
    "s_waitcnt lgkmcnt(10)\n"
    "v_mfma_f32_16x16x32_bf16 v[64:67], v[176:179], a[56:59], v[64:67]\n"
    "ds_read_b64_tr_b16 v[172:173], v227 offset:16384\n"
    "v_cvt_pk_bf16_f32 v147, v50, v51\n"
    "v_mfma_f32_16x16x32_bf16 v[80:83], v[176:179], a[60:63], v[80:83]\n"
    "ds_read_b64_tr_b16 v[174:175], v227 offset:16896\n"
    "v_cvt_pk_bf16_f32 v148, v36, v37\n"
    "v_mfma_f32_16x16x32_bf16 v[84:87], v[180:183], a[60:63], v[84:87]\n"
    "ds_read_b64_tr_b16 v[176:177], v224 offset:20480\n"
    "buffer_load_dwordx4 v237, s[24:27], s98 offen offset:1024 lds\n"
    "v_mfma_f32_16x16x32_bf16 v[68:71], v[180:183], a[56:59], v[68:71]\n"
    "ds_read_b64_tr_b16 v[178:179], v224 offset:20992\n"
    "s_waitcnt lgkmcnt(10)\n"
    "v_mfma_f32_16x16x32_bf16 v[72:75], v[184:187], a[56:59], v[72:75]\n"
    "ds_read_b64_tr_b16 v[180:181], v225 offset:20480\n"
    "v_cvt_pk_bf16_f32 v149, v38, v39\n"
    "v_mfma_f32_16x16x32_bf16 v[88:91], v[184:187], a[60:63], v[88:91]\n"
    "ds_read_b64_tr_b16 v[182:183], v225 offset:20992\n"
    "v_cvt_pk_bf16_f32 v150, v52, v53\n"
    "v_mfma_f32_16x16x32_bf16 v[92:95], v[188:191], a[60:63], v[92:95]\n"
    "ds_read_b64_tr_b16 v[184:185], v226 offset:20480\n"
    "v_cvt_pk_bf16_f32 v151, v54, v55\n"
    "v_mfma_f32_16x16x32_bf16 v[76:79], v[188:191], a[56:59], v[76:79]\n"
    "ds_read_b64_tr_b16 v[186:187], v226 offset:20992\n"
    "s_waitcnt lgkmcnt(10)\n"
    "v_mfma_f32_16x16x32_bf16 v[64:67], v[160:163], a[64:67], v[64:67]\n"
    "ds_read_b64_tr_b16 v[188:189], v227 offset:20480\n"
    "v_cvt_pk_bf16_f32 v152, v40, v41\n"
    "v_mfma_f32_16x16x32_bf16 v[80:83], v[160:163], a[68:71], v[80:83]\n"
    "ds_read_b64_tr_b16 v[190:191], v227 offset:20992\n"
    "v_cvt_pk_bf16_f32 v153, v42, v43\n"
    "v_mfma_f32_16x16x32_bf16 v[84:87], v[164:167], a[68:71], v[84:87]\n"
    "ds_read_b64_tr_b16 v[160:161], v224 offset:24576\n"
    "buffer_load_dwordx4 v238, s[24:27], s98 offen offset:2048 lds\n"
    "v_mfma_f32_16x16x32_bf16 v[68:71], v[164:167], a[64:67], v[68:71]\n"
    "ds_read_b64_tr_b16 v[162:163], v224 offset:25088\n"
    "s_waitcnt lgkmcnt(10)\n"
    "v_mfma_f32_16x16x32_bf16 v[72:75], v[168:171], a[64:67], v[72:75]\n"
    "ds_read_b64_tr_b16 v[164:165], v225 offset:24576\n"
    "v_cvt_pk_bf16_f32 v154, v56, v57\n"
    "v_mfma_f32_16x16x32_bf16 v[88:91], v[168:171], a[68:71], v[88:91]\n"
    "ds_read_b64_tr_b16 v[166:167], v225 offset:25088\n"
    "v_cvt_pk_bf16_f32 v155, v58, v59\n"
    "v_mfma_f32_16x16x32_bf16 v[92:95], v[172:175], a[68:71], v[92:95]\n"
    "ds_read_b64_tr_b16 v[168:169], v226 offset:24576\n"
    "v_cvt_pk_bf16_f32 v156, v44, v45\n"
    "v_mfma_f32_16x16x32_bf16 v[76:79], v[172:175], a[64:67], v[76:79]\n"
    "ds_read_b64_tr_b16 v[170:171], v226 offset:25088\n"
    "s_waitcnt lgkmcnt(10)\n"
    "v_mfma_f32_16x16x32_bf16 v[64:67], v[176:179], a[72:75], v[64:67]\n"
    "ds_read_b64_tr_b16 v[172:173], v227 offset:24576\n"
    "buffer_store_dwordx4 v[132:135], v240, s[44:47], s99 offen offset:1024 sc1\n"
    "v_mfma_f32_16x16x32_bf16 v[80:83], v[176:179], a[76:79], v[80:83]\n"
    "ds_read_b64_tr_b16 v[174:175], v227 offset:25088\n"
    "v_cvt_pk_bf16_f32 v157, v46, v47\n"
    "v_mfma_f32_16x16x32_bf16 v[84:87], v[180:183], a[76:79], v[84:87]\n"
    "ds_read_b64_tr_b16 v[176:177], v224 offset:28672\n"
    "buffer_load_dwordx4 v239, s[24:27], s98 offen offset:3072 lds\n"
    "v_mfma_f32_16x16x32_bf16 v[68:71], v[180:183], a[72:75], v[68:71]\n"
    "ds_read_b64_tr_b16 v[178:179], v224 offset:29184\n"
    "s_waitcnt lgkmcnt(10)\n"
    "v_mfma_f32_16x16x32_bf16 v[72:75], v[184:187], a[72:75], v[72:75]\n"
    "ds_read_b64_tr_b16 v[180:181], v225 offset:28672\n"
    "v_cvt_pk_bf16_f32 v158, v60, v61\n"
    "v_mfma_f32_16x16x32_bf16 v[88:91], v[184:187], a[76:79], v[88:91]\n"
    "ds_read_b64_tr_b16 v[182:183], v225 offset:29184\n"
    "v_cvt_pk_bf16_f32 v159, v62, v63\n"
    "v_mfma_f32_16x16x32_bf16 v[92:95], v[188:191], a[76:79], v[92:95]\n"
    "ds_read_b64_tr_b16 v[184:185], v226 offset:28672\n"
    "v_mfma_f32_16x16x32_bf16 v[76:79], v[188:191], a[72:75], v[76:79]\n"
    "ds_read_b64_tr_b16 v[186:187], v226 offset:29184\n"
    "s_waitcnt lgkmcnt(10)\n"
    "v_mfma_f32_16x16x32_bf16 v[64:67], v[160:163], a[80:83], v[64:67]\n"
    "ds_read_b64_tr_b16 v[188:189], v227 offset:28672\n"
    "s_add_u32 m0, s85, 0x18000\n"
    "v_mfma_f32_16x16x32_bf16 v[80:83], v[160:163], a[84:87], v[80:83]\n"
    "ds_read_b64_tr_b16 v[190:191], v227 offset:29184\n"
    "s_add_u32 s98, s63, s19\n"
    "v_mfma_f32_16x16x32_bf16 v[84:87], v[164:167], a[84:87], v[84:87]\n"
    "ds_read_b64_tr_b16 v[160:161], v224 offset:32768\n"
    "buffer_load_dwordx4 v236, s[24:27], s98 offen lds\n"
    "v_mfma_f32_16x16x32_bf16 v[68:71], v[164:167], a[80:83], v[68:71]\n"
    "ds_read_b64_tr_b16 v[162:163], v224 offset:33280\n"
    "s_waitcnt lgkmcnt(10)\n"
    "v_mfma_f32_16x16x32_bf16 v[72:75], v[168:171], a[80:83], v[72:75]\n"
    "ds_read_b64_tr_b16 v[164:165], v225 offset:32768\n"
    "v_mfma_f32_16x16x32_bf16 v[88:91], v[168:171], a[84:87], v[88:91]\n"
    "ds_read_b64_tr_b16 v[166:167], v225 offset:33280\n"
    "v_mfma_f32_16x16x32_bf16 v[92:95], v[172:175], a[84:87], v[92:95]\n"
    "ds_read_b64_tr_b16 v[168:169], v226 offset:32768\n"
    "v_mfma_f32_16x16x32_bf16 v[76:79], v[172:175], a[80:83], v[76:79]\n"
    "ds_read_b64_tr_b16 v[170:171], v226 offset:33280\n"
    "s_waitcnt lgkmcnt(10)\n"
    "v_mfma_f32_16x16x32_bf16 v[64:67], v[176:179], a[88:91], v[64:67]\n"
    "ds_read_b64_tr_b16 v[172:173], v227 offset:32768\n"
    "buffer_load_dwordx4 v237, s[24:27], s98 offen offset:1024 lds\n"
    "v_mfma_f32_16x16x32_bf16 v[80:83], v[176:179], a[92:95], v[80:83]\n"
    "ds_read_b64_tr_b16 v[174:175], v227 offset:33280\n"
    "v_mfma_f32_16x16x32_bf16 v[84:87], v[180:183], a[92:95], v[84:87]\n"
    "ds_read_b64_tr_b16 v[176:177], v224 offset:36864\n"
    "v_mfma_f32_16x16x32_bf16 v[68:71], v[180:183], a[88:91], v[68:71]\n"
    "ds_read_b64_tr_b16 v[178:179], v224 offset:37376\n"
    "s_waitcnt lgkmcnt(10)\n"
    "v_mfma_f32_16x16x32_bf16 v[72:75], v[184:187], a[88:91], v[72:75]\n"
    "ds_read_b64_tr_b16 v[180:181], v225 offset:36864\n"
    "v_mfma_f32_16x16x32_bf16 v[88:91], v[184:187], a[92:95], v[88:91]\n"
    "ds_read_b64_tr_b16 v[182:183], v225 offset:37376\n"
    "v_mfma_f32_16x16x32_bf16 v[92:95], v[188:191], a[92:95], v[92:95]\n"
    "ds_read_b64_tr_b16 v[184:185], v226 offset:36864\n"
    "buffer_load_dwordx4 v238, s[24:27], s98 offen offset:2048 lds\n"
    "v_mfma_f32_16x16x32_bf16 v[76:79], v[188:191], a[88:91], v[76:79]\n"
    "ds_read_b64_tr_b16 v[186:187], v226 offset:37376\n"
    "s_waitcnt lgkmcnt(10)\n"
    "v_mfma_f32_16x16x32_bf16 v[64:67], v[160:163], a[96:99], v[64:67]\n"
    "ds_read_b64_tr_b16 v[188:189], v227 offset:36864\n"
    "v_mfma_f32_16x16x32_bf16 v[80:83], v[160:163], a[100:103], v[80:83]\n"
    "ds_read_b64_tr_b16 v[190:191], v227 offset:37376\n"
    "v_mfma_f32_16x16x32_bf16 v[84:87], v[164:167], a[100:103], v[84:87]\n"
    "ds_read_b64_tr_b16 v[160:161], v224 offset:40960\n"
    "v_mfma_f32_16x16x32_bf16 v[68:71], v[164:167], a[96:99], v[68:71]\n"
    "ds_read_b64_tr_b16 v[162:163], v224 offset:41472\n"
    "s_waitcnt lgkmcnt(10)\n"
    "v_mfma_f32_16x16x32_bf16 v[72:75], v[168:171], a[96:99], v[72:75]\n"
    "ds_read_b64_tr_b16 v[164:165], v225 offset:40960\n"
    "buffer_load_dwordx4 v239, s[24:27], s98 offen offset:3072 lds\n"
    "v_mfma_f32_16x16x32_bf16 v[88:91], v[168:171], a[100:103], v[88:91]\n"
    "ds_read_b64_tr_b16 v[166:167], v225 offset:41472\n"
    "v_mfma_f32_16x16x32_bf16 v[92:95], v[172:175], a[100:103], v[92:95]\n"
    "ds_read_b64_tr_b16 v[168:169], v226 offset:40960\n"
    "v_add_u32 v236, v236, v204\n"
    "v_mfma_f32_16x16x32_bf16 v[76:79], v[172:175], a[96:99], v[76:79]\n"
    "ds_read_b64_tr_b16 v[170:171], v226 offset:41472\n"
    "s_waitcnt lgkmcnt(10)\n"
    "v_mfma_f32_16x16x32_bf16 v[64:67], v[176:179], a[104:107], v[64:67]\n"
    "ds_read_b64_tr_b16 v[172:173], v227 offset:40960\n"
    "v_add_u32 v237, v237, v204\n"
    "v_mfma_f32_16x16x32_bf16 v[80:83], v[176:179], a[108:111], v[80:83]\n"
    "ds_read_b64_tr_b16 v[174:175], v227 offset:41472\n"
    "v_add_u32 v237, 0x2000, v237\n"
    "v_mfma_f32_16x16x32_bf16 v[84:87], v[180:183], a[108:111], v[84:87]\n"
    "ds_read_b64_tr_b16 v[176:177], v224 offset:45056\n"
    "v_add_u32 v238, v238, v204\n"
    "v_mfma_f32_16x16x32_bf16 v[68:71], v[180:183], a[104:107], v[68:71]\n"
    "ds_read_b64_tr_b16 v[178:179], v224 offset:45568\n"
    "s_waitcnt lgkmcnt(10)\n"
    "v_mfma_f32_16x16x32_bf16 v[72:75], v[184:187], a[104:107], v[72:75]\n"
    "ds_read_b64_tr_b16 v[180:181], v225 offset:45056\n"
    "v_add_u32 v238, 0x4000, v238\n"
    "v_mfma_f32_16x16x32_bf16 v[88:91], v[184:187], a[108:111], v[88:91]\n"
    "ds_read_b64_tr_b16 v[182:183], v225 offset:45568\n"
    "v_add_u32 v239, v239, v204\n"
    "v_mfma_f32_16x16x32_bf16 v[92:95], v[188:191], a[108:111], v[92:95]\n"
    "ds_read_b64_tr_b16 v[184:185], v226 offset:45056\n"
    "v_add_u32 v239, 0x6000, v239\n"
    "v_mfma_f32_16x16x32_bf16 v[76:79], v[188:191], a[104:107], v[76:79]\n"
    "ds_read_b64_tr_b16 v[186:187], v226 offset:45568\n"
    "s_waitcnt lgkmcnt(10)\n"
    "v_mfma_f32_16x16x32_bf16 v[64:67], v[160:163], a[112:115], v[64:67]\n"
    "ds_read_b64_tr_b16 v[188:189], v227 offset:45056\n"
    "s_waitcnt vmcnt(9)\n"
    "v_mfma_f32_16x16x32_bf16 v[80:83], v[160:163], a[116:119], v[80:83]\n"
    "ds_read_b64_tr_b16 v[190:191], v227 offset:45568\n"
    "s_barrier\n"
    "v_mfma_f32_16x16x32_bf16 v[84:87], v[164:167], a[116:119], v[84:87]\n"
    "ds_read_b64_tr_b16 v[160:161], v224 offset:49152\n"
    "s_add_u32 m0, s85, 0\n"
    "v_mfma_f32_16x16x32_bf16 v[68:71], v[164:167], a[112:115], v[68:71]\n"
    "ds_read_b64_tr_b16 v[162:163], v224 offset:49664\n"
    "s_waitcnt lgkmcnt(10)\n"
    "v_mfma_f32_16x16x32_bf16 v[72:75], v[168:171], a[112:115], v[72:75]\n"
    "ds_read_b64_tr_b16 v[164:165], v225 offset:49152\n"
    "buffer_load_dwordx4 v236, s[28:31], s64 offen lds\n"
    "v_mfma_f32_16x16x32_bf16 v[88:91], v[168:171], a[116:119], v[88:91]\n"
    "ds_read_b64_tr_b16 v[166:167], v225 offset:49664\n"
    "v_mfma_f32_16x16x32_bf16 v[92:95], v[172:175], a[116:119], v[92:95]\n"
    "ds_read_b64_tr_b16 v[168:169], v226 offset:49152\n"
    "v_mfma_f32_16x16x32_bf16 v[76:79], v[172:175], a[112:115], v[76:79]\n"
    "ds_read_b64_tr_b16 v[170:171], v226 offset:49664\n"
    "s_waitcnt lgkmcnt(10)\n"
    "v_mfma_f32_16x16x32_bf16 v[64:67], v[176:179], a[120:123], v[64:67]\n"
    "ds_read_b64_tr_b16 v[172:173], v227 offset:49152\n"
    "s_add_u32 s99, s86, 0x20000\n"
    "v_mfma_f32_16x16x32_bf16 v[80:83], v[176:179], a[124:127], v[80:83]\n"
    "ds_read_b64_tr_b16 v[174:175], v227 offset:49664\n"
    "buffer_store_dwordx4 v[136:139], v240, s[44:47], s99 offen sc1\n"
    "v_mfma_f32_16x16x32_bf16 v[84:87], v[180:183], a[124:127], v[84:87]\n"
    "ds_read_b64_tr_b16 v[176:177], v224 offset:53248\n"
    "buffer_load_dwordx4 v237, s[28:31], s64 offen offset:1024 lds\n"
    "v_mfma_f32_16x16x32_bf16 v[68:71], v[180:183], a[120:123], v[68:71]\n"
    "ds_read_b64_tr_b16 v[178:179], v224 offset:53760\n"
    "s_waitcnt lgkmcnt(10)\n"
    "v_mfma_f32_16x16x32_bf16 v[72:75], v[184:187], a[120:123], v[72:75]\n"
    "ds_read_b64_tr_b16 v[180:181], v225 offset:53248\n"
    "v_mfma_f32_16x16x32_bf16 v[88:91], v[184:187], a[124:127], v[88:91]\n"
    "ds_read_b64_tr_b16 v[182:183], v225 offset:53760\n"
    "v_mfma_f32_16x16x32_bf16 v[92:95], v[188:191], a[124:127], v[92:95]\n"
    "ds_read_b64_tr_b16 v[184:185], v226 offset:53248\n"
    "v_mfma_f32_16x16x32_bf16 v[76:79], v[188:191], a[120:123], v[76:79]\n"
    "ds_read_b64_tr_b16 v[186:187], v226 offset:53760\n"
    "s_waitcnt lgkmcnt(10)\n"
    "v_mfma_f32_16x16x32_bf16 v[64:67], v[160:163], a[128:131], v[64:67]\n"
    "ds_read_b64_tr_b16 v[188:189], v227 offset:53248\n"
    "s_lshl_b32 s75, s50, 11\n"
    "v_mfma_f32_16x16x32_bf16 v[80:83], v[160:163], a[132:135], v[80:83]\n"
    "ds_read_b64_tr_b16 v[190:191], v227 offset:53760\n"
    "s_mov_b32 s74, s86\n"
    "v_mfma_f32_16x16x32_bf16 v[84:87], v[164:167], a[132:135], v[84:87]\n"
    "ds_read_b64_tr_b16 v[160:161], v224 offset:57344\n"
    "buffer_load_dwordx4 v238, s[28:31], s64 offen offset:2048 lds\n"
    "v_mfma_f32_16x16x32_bf16 v[68:71], v[164:167], a[128:131], v[68:71]\n"
    "ds_read_b64_tr_b16 v[162:163], v224 offset:57856\n"
    "s_waitcnt lgkmcnt(10)\n"
    "v_mfma_f32_16x16x32_bf16 v[72:75], v[168:171], a[128:131], v[72:75]\n"
    "ds_read_b64_tr_b16 v[164:165], v225 offset:57344\n"
    "s_lshl_b32 s89, s49, 18\n"
    "v_mfma_f32_16x16x32_bf16 v[88:91], v[168:171], a[132:135], v[88:91]\n"
    "ds_read_b64_tr_b16 v[166:167], v225 offset:57856\n"
    "s_add_u32 s89, s89, 0x3ffc00\n"
    "v_mfma_f32_16x16x32_bf16 v[92:95], v[172:175], a[132:135], v[92:95]\n"
    "ds_read_b64_tr_b16 v[168:169], v226 offset:57344\n"
    "v_and_b32 v249, 0x300, v240\n"
    "v_mfma_f32_16x16x32_bf16 v[76:79], v[172:175], a[128:131], v[76:79]\n"
    "ds_read_b64_tr_b16 v[170:171], v226 offset:57856\n"
    "s_waitcnt lgkmcnt(10)\n"
    "v_mfma_f32_16x16x32_bf16 v[64:67], v[176:179], a[136:139], v[64:67]\n"
    "ds_read_b64_tr_b16 v[172:173], v227 offset:57344\n"
    "v_add_u32 v250, 0x400, v249\n"
    "v_mfma_f32_16x16x32_bf16 v[80:83], v[176:179], a[140:143], v[80:83]\n"
    "ds_read_b64_tr_b16 v[174:175], v227 offset:57856\n"
    "s_sub_u32 s90, s86, 0x4000\n"
    "v_mfma_f32_16x16x32_bf16 v[84:87], v[180:183], a[140:143], v[84:87]\n"
    "ds_read_b64_tr_b16 v[176:177], v224 offset:61440\n"
    "buffer_load_dwordx4 v239, s[28:31], s64 offen offset:3072 lds\n"
    "v_mfma_f32_16x16x32_bf16 v[68:71], v[180:183], a[136:139], v[68:71]\n"
    "ds_read_b64_tr_b16 v[178:179], v224 offset:61952\n"
    "s_waitcnt lgkmcnt(10)\n"
    "v_mfma_f32_16x16x32_bf16 v[72:75], v[184:187], a[136:139], v[72:75]\n"
    "ds_read_b64_tr_b16 v[180:181], v225 offset:61440\n"
    "s_cmp_ge_u32 s48, 2\n"
    "s_cselect_b32 s72, s90, s89\n"
    "s_cselect_b32 s76, s90, 0x80000000\n"
    "s_cselect_b64 vcc, -1, 0\n"
    "v_mfma_f32_16x16x32_bf16 v[88:91], v[184:187], a[140:143], v[88:91]\n"
    "ds_read_b64_tr_b16 v[182:183], v225 offset:61952\n"
    "v_cndmask_b32 v252, v250, v240, vcc\n"
    "v_mfma_f32_16x16x32_bf16 v[92:95], v[188:191], a[140:143], v[92:95]\n"
    "ds_read_b64_tr_b16 v[184:185], v226 offset:61440\n"
    "v_cndmask_b32 v253, v249, v240, vcc\n"
    "v_mfma_f32_16x16x32_bf16 v[76:79], v[188:191], a[136:139], v[76:79]\n"
    "ds_read_b64_tr_b16 v[186:187], v226 offset:61952\n"
    "s_waitcnt lgkmcnt(10)\n"
    "v_mfma_f32_16x16x32_bf16 v[64:67], v[160:163], a[144:147], v[64:67]\n"
    "ds_read_b64_tr_b16 v[188:189], v227 offset:61440\n"
    "s_add_u32 m0, s85, 0x4000\n"
    "v_mfma_f32_16x16x32_bf16 v[80:83], v[160:163], a[148:151], v[80:83]\n"
    "ds_read_b64_tr_b16 v[190:191], v227 offset:61952\n"
    "s_sub_u32 s90, s86, 0x2000\n"
    "v_mfma_f32_16x16x32_bf16 v[84:87], v[164:167], a[148:151], v[84:87]\n"
    "ds_read_b64_tr_b16 v[160:161], v228\n"
    "buffer_load_dwordx4 v236, s[28:31], s65 offen lds\n"
    "v_mfma_f32_16x16x32_bf16 v[68:71], v[164:167], a[144:147], v[68:71]\n"
    "ds_read_b64_tr_b16 v[162:163], v228 offset:512\n"
    "s_waitcnt lgkmcnt(10)\n"
    "v_mfma_f32_16x16x32_bf16 v[72:75], v[168:171], a[144:147], v[72:75]\n"
    "ds_read_b64_tr_b16 v[164:165], v229\n"
    "s_cmp_ge_u32 s48, 1\n"
    "s_cselect_b32 s73, s90, s89\n"
    "s_cselect_b32 s77, s90, 0x80000000\n"
    "s_cselect_b64 vcc, -1, 0\n"
    "v_mfma_f32_16x16x32_bf16 v[88:91], v[168:171], a[148:151], v[88:91]\n"
    "ds_read_b64_tr_b16 v[166:167], v229 offset:512\n"
    "v_cndmask_b32 v207, v250, v240, vcc\n"
    "v_mfma_f32_16x16x32_bf16 v[92:95], v[172:175], a[148:151], v[92:95]\n"
    "ds_read_b64_tr_b16 v[168:169], v230\n"
    "v_cndmask_b32 v242, v249, v240, vcc\n"
    "v_mfma_f32_16x16x32_bf16 v[76:79], v[172:175], a[144:147], v[76:79]\n"
    "ds_read_b64_tr_b16 v[170:171], v230 offset:512\n"
    "s_waitcnt lgkmcnt(10)\n"
    "v_mfma_f32_16x16x32_bf16 v[64:67], v[176:179], a[152:155], v[64:67]\n"
    "ds_read_b64_tr_b16 v[172:173], v231\n"
    "buffer_load_dwordx4 v237, s[28:31], s65 offen offset:1024 lds\n"
    "v_mfma_f32_16x16x32_bf16 v[80:83], v[176:179], a[156:159], v[80:83]\n"
    "ds_read_b64_tr_b16 v[174:175], v231 offset:512\n"
    "v_mfma_f32_16x16x32_bf16 v[84:87], v[180:183], a[156:159], v[84:87]\n"
    "ds_read_b64_tr_b16 v[176:177], v228 offset:4096\n"
    "v_mfma_f32_16x16x32_bf16 v[68:71], v[180:183], a[152:155], v[68:71]\n"
    "ds_read_b64_tr_b16 v[178:179], v228 offset:4608\n"
    "s_waitcnt lgkmcnt(10)\n"
    "v_mfma_f32_16x16x32_bf16 v[72:75], v[184:187], a[152:155], v[72:75]\n"
    "ds_read_b64_tr_b16 v[180:181], v229 offset:4096\n"
    "v_mfma_f32_16x16x32_bf16 v[88:91], v[184:187], a[156:159], v[88:91]\n"
    "ds_read_b64_tr_b16 v[182:183], v229 offset:4608\n"
    "v_mfma_f32_16x16x32_bf16 v[92:95], v[188:191], a[156:159], v[92:95]\n"
    "ds_read_b64_tr_b16 v[184:185], v230 offset:4096\n"
    "buffer_load_dwordx4 v238, s[28:31], s65 offen offset:2048 lds\n"
    "v_mfma_f32_16x16x32_bf16 v[76:79], v[188:191], a[152:155], v[76:79]\n"
    "ds_read_b64_tr_b16 v[186:187], v230 offset:4608\n"
    "s_waitcnt lgkmcnt(10)\n"
    "v_mfma_f32_16x16x32_bf16 v[64:67], v[160:163], a[160:163], v[64:67]\n"
    "ds_read_b64_tr_b16 v[188:189], v231 offset:4096\n"
    "v_mfma_f32_16x16x32_bf16 v[80:83], v[160:163], a[164:167], v[80:83]\n"
    "ds_read_b64_tr_b16 v[190:191], v231 offset:4608\n"
    "v_mfma_f32_16x16x32_bf16 v[84:87], v[164:167], a[164:167], v[84:87]\n"
    "ds_read_b64_tr_b16 v[160:161], v228 offset:8192\n"
    "v_mfma_f32_16x16x32_bf16 v[68:71], v[164:167], a[160:163], v[68:71]\n"
    "ds_read_b64_tr_b16 v[162:163], v228 offset:8704\n"
    "s_waitcnt lgkmcnt(10)\n"
    "v_mfma_f32_16x16x32_bf16 v[72:75], v[168:171], a[160:163], v[72:75]\n"
    "ds_read_b64_tr_b16 v[164:165], v229 offset:8192\n"
    "buffer_load_dwordx4 v239, s[28:31], s65 offen offset:3072 lds\n"
    "v_mfma_f32_16x16x32_bf16 v[88:91], v[168:171], a[164:167], v[88:91]\n"
    "ds_read_b64_tr_b16 v[166:167], v229 offset:8704\n"
    "v_mfma_f32_16x16x32_bf16 v[92:95], v[172:175], a[164:167], v[92:95]\n"
    "ds_read_b64_tr_b16 v[168:169], v230 offset:8192\n"
    "v_mfma_f32_16x16x32_bf16 v[76:79], v[172:175], a[160:163], v[76:79]\n"
    "ds_read_b64_tr_b16 v[170:171], v230 offset:8704\n"
    "s_waitcnt lgkmcnt(10)\n"
    "v_mfma_f32_16x16x32_bf16 v[64:67], v[176:179], a[168:171], v[64:67]\n"
    "ds_read_b64_tr_b16 v[172:173], v231 offset:8192\n"
    "buffer_store_dwordx4 v[140:143], v240, s[44:47], s99 offen offset:1024 sc1\n"
    "v_mfma_f32_16x16x32_bf16 v[80:83], v[176:179], a[172:175], v[80:83]\n"
    "ds_read_b64_tr_b16 v[174:175], v231 offset:8704\n"
    "v_mfma_f32_16x16x32_bf16 v[84:87], v[180:183], a[172:175], v[84:87]\n"
    "ds_read_b64_tr_b16 v[176:177], v228 offset:12288\n"
    "v_mfma_f32_16x16x32_bf16 v[68:71], v[180:183], a[168:171], v[68:71]\n"
    "ds_read_b64_tr_b16 v[178:179], v228 offset:12800\n"
    "s_waitcnt lgkmcnt(10)\n"
    "v_mfma_f32_16x16x32_bf16 v[72:75], v[184:187], a[168:171], v[72:75]\n"
    "ds_read_b64_tr_b16 v[180:181], v229 offset:12288\n"
    "v_mfma_f32_16x16x32_bf16 v[88:91], v[184:187], a[172:175], v[88:91]\n"
    "ds_read_b64_tr_b16 v[182:183], v229 offset:12800\n"
    "v_mfma_f32_16x16x32_bf16 v[92:95], v[188:191], a[172:175], v[92:95]\n"
    "ds_read_b64_tr_b16 v[184:185], v230 offset:12288\n"
    "v_mfma_f32_16x16x32_bf16 v[76:79], v[188:191], a[168:171], v[76:79]\n"
    "ds_read_b64_tr_b16 v[186:187], v230 offset:12800\n"
    "s_waitcnt lgkmcnt(10)\n"
    "v_mfma_f32_16x16x32_bf16 v[64:67], v[160:163], a[176:179], v[64:67]\n"
    "ds_read_b64_tr_b16 v[188:189], v231 offset:12288\n"
    "s_waitcnt vmcnt(10)\n"
    "v_mfma_f32_16x16x32_bf16 v[80:83], v[160:163], a[180:183], v[80:83]\n"
    "ds_read_b64_tr_b16 v[190:191], v231 offset:12800\n"
    "s_barrier\n"
    "v_mfma_f32_16x16x32_bf16 v[84:87], v[164:167], a[180:183], v[84:87]\n"
    "ds_read_b64_tr_b16 v[160:161], v228 offset:16384\n"
    "s_add_u32 m0, s85, 0x8000\n"
    "v_mfma_f32_16x16x32_bf16 v[68:71], v[164:167], a[176:179], v[68:71]\n"
    "ds_read_b64_tr_b16 v[162:163], v228 offset:16896\n"
    "s_waitcnt lgkmcnt(10)\n"
    "v_mfma_f32_16x16x32_bf16 v[72:75], v[168:171], a[176:179], v[72:75]\n"
    "ds_read_b64_tr_b16 v[164:165], v229 offset:16384\n"
    "buffer_load_dwordx4 v236, s[28:31], s66 offen lds\n"
    "v_mfma_f32_16x16x32_bf16 v[88:91], v[168:171], a[180:183], v[88:91]\n"
    "ds_read_b64_tr_b16 v[166:167], v229 offset:16896\n"
    "v_mfma_f32_16x16x32_bf16 v[92:95], v[172:175], a[180:183], v[92:95]\n"
    "ds_read_b64_tr_b16 v[168:169], v230 offset:16384\n"
    "v_mfma_f32_16x16x32_bf16 v[76:79], v[172:175], a[176:179], v[76:79]\n"
    "ds_read_b64_tr_b16 v[170:171], v230 offset:16896\n"
    "s_waitcnt lgkmcnt(10)\n"
    "v_mfma_f32_16x16x32_bf16 v[64:67], v[176:179], a[184:187], v[64:67]\n"
    "ds_read_b64_tr_b16 v[172:173], v231 offset:16384\n"
    "v_mfma_f32_16x16x32_bf16 v[80:83], v[176:179], a[188:191], v[80:83]\n"
    "ds_read_b64_tr_b16 v[174:175], v231 offset:16896\n"
    "v_mfma_f32_16x16x32_bf16 v[84:87], v[180:183], a[188:191], v[84:87]\n"
    "ds_read_b64_tr_b16 v[176:177], v228 offset:20480\n"
    "buffer_load_dwordx4 v237, s[28:31], s66 offen offset:1024 lds\n"
    "v_mfma_f32_16x16x32_bf16 v[68:71], v[180:183], a[184:187], v[68:71]\n"
    "ds_read_b64_tr_b16 v[178:179], v228 offset:20992\n"
    "s_waitcnt lgkmcnt(10)\n"
    "v_mfma_f32_16x16x32_bf16 v[72:75], v[184:187], a[184:187], v[72:75]\n"
    "ds_read_b64_tr_b16 v[180:181], v229 offset:20480\n"
    "v_mfma_f32_16x16x32_bf16 v[88:91], v[184:187], a[188:191], v[88:91]\n"
    "ds_read_b64_tr_b16 v[182:183], v229 offset:20992\n"
    "v_mfma_f32_16x16x32_bf16 v[92:95], v[188:191], a[188:191], v[92:95]\n"
    "ds_read_b64_tr_b16 v[184:185], v230 offset:20480\n"
    "v_mfma_f32_16x16x32_bf16 v[76:79], v[188:191], a[184:187], v[76:79]\n"
    "ds_read_b64_tr_b16 v[186:187], v230 offset:20992\n"
    "s_waitcnt lgkmcnt(10)\n"
    "v_mfma_f32_16x16x32_bf16 v[64:67], v[160:163], a[192:195], v[64:67]\n"
    "ds_read_b64_tr_b16 v[188:189], v231 offset:20480\n"
    "v_mfma_f32_16x16x32_bf16 v[80:83], v[160:163], a[196:199], v[80:83]\n"
    "ds_read_b64_tr_b16 v[190:191], v231 offset:20992\n"
    "v_mfma_f32_16x16x32_bf16 v[84:87], v[164:167], a[196:199], v[84:87]\n"
    "ds_read_b64_tr_b16 v[160:161], v228 offset:24576\n"
    "buffer_load_dwordx4 v238, s[28:31], s66 offen offset:2048 lds\n"
    "v_mfma_f32_16x16x32_bf16 v[68:71], v[164:167], a[192:195], v[68:71]\n"
    "ds_read_b64_tr_b16 v[162:163], v228 offset:25088\n"
    "s_waitcnt lgkmcnt(10)\n"
    "v_mfma_f32_16x16x32_bf16 v[72:75], v[168:171], a[192:195], v[72:75]\n"
    "ds_read_b64_tr_b16 v[164:165], v229 offset:24576\n"
    "v_mfma_f32_16x16x32_bf16 v[88:91], v[168:171], a[196:199], v[88:91]\n"
    "ds_read_b64_tr_b16 v[166:167], v229 offset:25088\n"
    "v_mfma_f32_16x16x32_bf16 v[92:95], v[172:175], a[196:199], v[92:95]\n"
    "ds_read_b64_tr_b16 v[168:169], v230 offset:24576\n"
    "v_mfma_f32_16x16x32_bf16 v[76:79], v[172:175], a[192:195], v[76:79]\n"
    "ds_read_b64_tr_b16 v[170:171], v230 offset:25088\n"
    "s_waitcnt lgkmcnt(10)\n"
    "v_mfma_f32_16x16x32_bf16 v[64:67], v[176:179], a[200:203], v[64:67]\n"
    "ds_read_b64_tr_b16 v[172:173], v231 offset:24576\n"
    "v_mfma_f32_16x16x32_bf16 v[80:83], v[176:179], a[204:207], v[80:83]\n"
    "ds_read_b64_tr_b16 v[174:175], v231 offset:25088\n"
    "v_mfma_f32_16x16x32_bf16 v[84:87], v[180:183], a[204:207], v[84:87]\n"
    "ds_read_b64_tr_b16 v[176:177], v228 offset:28672\n"
    "buffer_load_dwordx4 v239, s[28:31], s66 offen offset:3072 lds\n"
    "v_mfma_f32_16x16x32_bf16 v[68:71], v[180:183], a[200:203], v[68:71]\n"
    "ds_read_b64_tr_b16 v[178:179], v228 offset:29184\n"
    "s_waitcnt lgkmcnt(10)\n"
    "v_mfma_f32_16x16x32_bf16 v[72:75], v[184:187], a[200:203], v[72:75]\n"
    "ds_read_b64_tr_b16 v[180:181], v229 offset:28672\n"
    "v_mfma_f32_16x16x32_bf16 v[88:91], v[184:187], a[204:207], v[88:91]\n"
    "ds_read_b64_tr_b16 v[182:183], v229 offset:29184\n"
    "v_mfma_f32_16x16x32_bf16 v[92:95], v[188:191], a[204:207], v[92:95]\n"
    "ds_read_b64_tr_b16 v[184:185], v230 offset:28672\n"
    "v_mfma_f32_16x16x32_bf16 v[76:79], v[188:191], a[200:203], v[76:79]\n"
    "ds_read_b64_tr_b16 v[186:187], v230 offset:29184\n"
    "s_waitcnt lgkmcnt(10)\n"
    "v_mfma_f32_16x16x32_bf16 v[64:67], v[160:163], a[208:211], v[64:67]\n"
    "ds_read_b64_tr_b16 v[188:189], v231 offset:28672\n"
    "s_add_u32 m0, s85, 0xc000\n"
    "v_mfma_f32_16x16x32_bf16 v[80:83], v[160:163], a[212:215], v[80:83]\n"
    "ds_read_b64_tr_b16 v[190:191], v231 offset:29184\n"
    "v_mfma_f32_16x16x32_bf16 v[84:87], v[164:167], a[212:215], v[84:87]\n"
    "ds_read_b64_tr_b16 v[160:161], v228 offset:32768\n"
    "buffer_load_dwordx4 v236, s[28:31], s67 offen lds\n"
    "v_mfma_f32_16x16x32_bf16 v[68:71], v[164:167], a[208:211], v[68:71]\n"
    "ds_read_b64_tr_b16 v[162:163], v228 offset:33280\n"
    "s_waitcnt lgkmcnt(10)\n"
    "v_mfma_f32_16x16x32_bf16 v[72:75], v[168:171], a[208:211], v[72:75]\n"
    "ds_read_b64_tr_b16 v[164:165], v229 offset:32768\n"
    "v_mfma_f32_16x16x32_bf16 v[88:91], v[168:171], a[212:215], v[88:91]\n"
    "ds_read_b64_tr_b16 v[166:167], v229 offset:33280\n"
    "v_mfma_f32_16x16x32_bf16 v[92:95], v[172:175], a[212:215], v[92:95]\n"
    "ds_read_b64_tr_b16 v[168:169], v230 offset:32768\n"
    "v_mfma_f32_16x16x32_bf16 v[76:79], v[172:175], a[208:211], v[76:79]\n"
    "ds_read_b64_tr_b16 v[170:171], v230 offset:33280\n"
    "s_waitcnt lgkmcnt(10)\n"
    "v_mfma_f32_16x16x32_bf16 v[64:67], v[176:179], a[216:219], v[64:67]\n"
    "ds_read_b64_tr_b16 v[172:173], v231 offset:32768\n"
    "buffer_load_dwordx4 v237, s[28:31], s67 offen offset:1024 lds\n"
    "v_mfma_f32_16x16x32_bf16 v[80:83], v[176:179], a[220:223], v[80:83]\n"
    "ds_read_b64_tr_b16 v[174:175], v231 offset:33280\n"
    "s_add_u32 s99, s87, 0\n"
    "v_mfma_f32_16x16x32_bf16 v[84:87], v[180:183], a[220:223], v[84:87]\n"
    "ds_read_b64_tr_b16 v[176:177], v228 offset:36864\n"
    "buffer_store_dwordx4 v[144:147], v240, s[44:47], s99 offen sc1\n"
    "v_mfma_f32_16x16x32_bf16 v[68:71], v[180:183], a[216:219], v[68:71]\n"
    "ds_read_b64_tr_b16 v[178:179], v228 offset:37376\n"
    "s_waitcnt lgkmcnt(10)\n"
    "v_mfma_f32_16x16x32_bf16 v[72:75], v[184:187], a[216:219], v[72:75]\n"
    "ds_read_b64_tr_b16 v[180:181], v229 offset:36864\n"
    "v_mfma_f32_16x16x32_bf16 v[88:91], v[184:187], a[220:223], v[88:91]\n"
    "ds_read_b64_tr_b16 v[182:183], v229 offset:37376\n"
    "v_mfma_f32_16x16x32_bf16 v[92:95], v[188:191], a[220:223], v[92:95]\n"
    "ds_read_b64_tr_b16 v[184:185], v230 offset:36864\n"
    "buffer_load_dwordx4 v238, s[28:31], s67 offen offset:2048 lds\n"
    "v_mfma_f32_16x16x32_bf16 v[76:79], v[188:191], a[216:219], v[76:79]\n"
    "ds_read_b64_tr_b16 v[186:187], v230 offset:37376\n"
    "s_waitcnt lgkmcnt(10)\n"
    "v_mfma_f32_16x16x32_bf16 v[64:67], v[160:163], a[224:227], v[64:67]\n"
    "ds_read_b64_tr_b16 v[188:189], v231 offset:36864\n"
    "v_mfma_f32_16x16x32_bf16 v[80:83], v[160:163], a[228:231], v[80:83]\n"
    "ds_read_b64_tr_b16 v[190:191], v231 offset:37376\n"
    "v_mfma_f32_16x16x32_bf16 v[84:87], v[164:167], a[228:231], v[84:87]\n"
    "ds_read_b64_tr_b16 v[160:161], v228 offset:40960\n"
    "v_mfma_f32_16x16x32_bf16 v[68:71], v[164:167], a[224:227], v[68:71]\n"
    "ds_read_b64_tr_b16 v[162:163], v228 offset:41472\n"
    "s_waitcnt lgkmcnt(10)\n"
    "v_mfma_f32_16x16x32_bf16 v[72:75], v[168:171], a[224:227], v[72:75]\n"
    "ds_read_b64_tr_b16 v[164:165], v229 offset:40960\n"
    "buffer_load_dwordx4 v239, s[28:31], s67 offen offset:3072 lds\n"
    "v_mfma_f32_16x16x32_bf16 v[88:91], v[168:171], a[228:231], v[88:91]\n"
    "ds_read_b64_tr_b16 v[166:167], v229 offset:41472\n"
    "v_mfma_f32_16x16x32_bf16 v[92:95], v[172:175], a[228:231], v[92:95]\n"
    "ds_read_b64_tr_b16 v[168:169], v230 offset:40960\n"
    "v_mfma_f32_16x16x32_bf16 v[76:79], v[172:175], a[224:227], v[76:79]\n"
    "ds_read_b64_tr_b16 v[170:171], v230 offset:41472\n"
    "s_waitcnt lgkmcnt(10)\n"
    "v_mfma_f32_16x16x32_bf16 v[64:67], v[176:179], a[232:235], v[64:67]\n"
    "ds_read_b64_tr_b16 v[172:173], v231 offset:40960\n"
    "v_mfma_f32_16x16x32_bf16 v[80:83], v[176:179], a[236:239], v[80:83]\n"
    "ds_read_b64_tr_b16 v[174:175], v231 offset:41472\n"
    "v_mfma_f32_16x16x32_bf16 v[84:87], v[180:183], a[236:239], v[84:87]\n"
    "ds_read_b64_tr_b16 v[176:177], v228 offset:45056\n"
    "v_mfma_f32_16x16x32_bf16 v[68:71], v[180:183], a[232:235], v[68:71]\n"
    "ds_read_b64_tr_b16 v[178:179], v228 offset:45568\n"
    "s_waitcnt lgkmcnt(10)\n"
    "v_mfma_f32_16x16x32_bf16 v[72:75], v[184:187], a[232:235], v[72:75]\n"
    "ds_read_b64_tr_b16 v[180:181], v229 offset:45056\n"
    "v_mfma_f32_16x16x32_bf16 v[88:91], v[184:187], a[236:239], v[88:91]\n"
    "ds_read_b64_tr_b16 v[182:183], v229 offset:45568\n"
    "v_mfma_f32_16x16x32_bf16 v[92:95], v[188:191], a[236:239], v[92:95]\n"
    "ds_read_b64_tr_b16 v[184:185], v230 offset:45056\n"
    "v_mfma_f32_16x16x32_bf16 v[76:79], v[188:191], a[232:235], v[76:79]\n"
    "ds_read_b64_tr_b16 v[186:187], v230 offset:45568\n"
    "s_waitcnt lgkmcnt(10)\n"
    "v_mfma_f32_16x16x32_bf16 v[64:67], v[160:163], a[240:243], v[64:67]\n"
    "ds_read_b64_tr_b16 v[188:189], v231 offset:45056\n"
    "s_waitcnt vmcnt(10)\n"
    "v_mfma_f32_16x16x32_bf16 v[80:83], v[160:163], a[244:247], v[80:83]\n"
    "ds_read_b64_tr_b16 v[190:191], v231 offset:45568\n"
    "s_barrier\n"
    "v_mfma_f32_16x16x32_bf16 v[84:87], v[164:167], a[244:247], v[84:87]\n"
    "ds_read_b64_tr_b16 v[160:161], v224\n"
    "s_add_u32 m0, s85, 0x10000\n"
    "v_mfma_f32_16x16x32_bf16 v[68:71], v[164:167], a[240:243], v[68:71]\n"
    "ds_read_b64_tr_b16 v[162:163], v224 offset:512\n"
    "s_waitcnt lgkmcnt(10)\n"
    "v_mfma_f32_16x16x32_bf16 v[72:75], v[168:171], a[240:243], v[72:75]\n"
    "ds_read_b64_tr_b16 v[164:165], v225\n"
    "buffer_load_dwordx4 v236, s[28:31], s68 offen lds\n"
    "v_mfma_f32_16x16x32_bf16 v[88:91], v[168:171], a[244:247], v[88:91]\n"
    "ds_read_b64_tr_b16 v[166:167], v225 offset:512\n"
    "v_mfma_f32_16x16x32_bf16 v[92:95], v[172:175], a[244:247], v[92:95]\n"
    "ds_read_b64_tr_b16 v[168:169], v226\n"
    "v_mfma_f32_16x16x32_bf16 v[76:79], v[172:175], a[240:243], v[76:79]\n"
    "ds_read_b64_tr_b16 v[170:171], v226 offset:512\n"
    "s_waitcnt lgkmcnt(10)\n"
    "v_mfma_f32_16x16x32_bf16 v[64:67], v[176:179], a[248:251], v[64:67]\n"
    "ds_read_b64_tr_b16 v[172:173], v227\n"
    "v_mfma_f32_16x16x32_bf16 v[80:83], v[176:179], a[252:255], v[80:83]\n"
    "ds_read_b64_tr_b16 v[174:175], v227 offset:512\n"
    "v_mfma_f32_16x16x32_bf16 v[84:87], v[180:183], a[252:255], v[84:87]\n"
    "ds_read_b64_tr_b16 v[176:177], v224 offset:4096\n"
    "buffer_load_dwordx4 v237, s[28:31], s68 offen offset:1024 lds\n"
    "v_mfma_f32_16x16x32_bf16 v[68:71], v[180:183], a[248:251], v[68:71]\n"
    "ds_read_b64_tr_b16 v[178:179], v224 offset:4608\n"
    "s_waitcnt lgkmcnt(10)\n"
    "v_mfma_f32_16x16x32_bf16 v[72:75], v[184:187], a[248:251], v[72:75]\n"
    "ds_read_b64_tr_b16 v[180:181], v225 offset:4096\n"
    "v_mfma_f32_16x16x32_bf16 v[88:91], v[184:187], a[252:255], v[88:91]\n"
    "ds_read_b64_tr_b16 v[182:183], v225 offset:4608\n"
    "v_mfma_f32_16x16x32_bf16 v[92:95], v[188:191], a[252:255], v[92:95]\n"
    "ds_read_b64_tr_b16 v[184:185], v226 offset:4096\n"
    "v_mfma_f32_16x16x32_bf16 v[76:79], v[188:191], a[248:251], v[76:79]\n"
    "ds_read_b64_tr_b16 v[186:187], v226 offset:4608\n"
    "s_waitcnt lgkmcnt(10)\n"
    "v_mfma_f32_16x16x32_bf16 v[96:99], v[160:163], a[0:3], 0\n"
    "ds_read_b64_tr_b16 v[188:189], v227 offset:4096\n"
    "v_mfma_f32_16x16x32_bf16 v[112:115], v[160:163], a[4:7], 0\n"
    "ds_read_b64_tr_b16 v[190:191], v227 offset:4608\n"
    "v_mfma_f32_16x16x32_bf16 v[116:119], v[164:167], a[4:7], 0\n"
    "ds_read_b64_tr_b16 v[160:161], v224 offset:8192\n"
    "buffer_load_dwordx4 v238, s[28:31], s68 offen offset:2048 lds\n"
    "v_mfma_f32_16x16x32_bf16 v[100:103], v[164:167], a[0:3], 0\n"
    "ds_read_b64_tr_b16 v[162:163], v224 offset:8704\n"
    "s_waitcnt lgkmcnt(10)\n"
    "v_mfma_f32_16x16x32_bf16 v[104:107], v[168:171], a[0:3], 0\n"
    "ds_read_b64_tr_b16 v[164:165], v225 offset:8192\n"
    "v_mul_f32 v64, s17, v64\n"
    "v_mfma_f32_16x16x32_bf16 v[120:123], v[168:171], a[4:7], 0\n"
    "ds_read_b64_tr_b16 v[166:167], v225 offset:8704\n"
    "v_mul_f32 v65, s17, v65\n"
    "v_mfma_f32_16x16x32_bf16 v[124:127], v[172:175], a[4:7], 0\n"
    "ds_read_b64_tr_b16 v[168:169], v226 offset:8192\n"
    "v_mul_f32 v66, s17, v66\n"
    "v_mfma_f32_16x16x32_bf16 v[108:111], v[172:175], a[0:3], 0\n"
    "ds_read_b64_tr_b16 v[170:171], v226 offset:8704\n"
    "s_waitcnt lgkmcnt(10)\n"
    "v_mfma_f32_16x16x32_bf16 v[96:99], v[176:179], a[8:11], v[96:99]\n"
    "ds_read_b64_tr_b16 v[172:173], v227 offset:8192\n"
    "buffer_store_dwordx4 v[148:151], v240, s[44:47], s99 offen offset:1024 sc1\n"
    "v_mfma_f32_16x16x32_bf16 v[112:115], v[176:179], a[12:15], v[112:115]\n"
    "ds_read_b64_tr_b16 v[174:175], v227 offset:8704\n"
    "v_mul_f32 v67, s17, v67\n"
    "v_mfma_f32_16x16x32_bf16 v[116:119], v[180:183], a[12:15], v[116:119]\n"
    "ds_read_b64_tr_b16 v[176:177], v224 offset:12288\n"
    "buffer_load_dwordx4 v239, s[28:31], s68 offen offset:3072 lds\n"
    "v_mfma_f32_16x16x32_bf16 v[100:103], v[180:183], a[8:11], v[100:103]\n"
    "ds_read_b64_tr_b16 v[178:179], v224 offset:12800\n"
    "s_waitcnt lgkmcnt(10)\n"
    "v_mfma_f32_16x16x32_bf16 v[104:107], v[184:187], a[8:11], v[104:107]\n"
    "ds_read_b64_tr_b16 v[180:181], v225 offset:12288\n"
    "v_mul_f32 v68, s17, v68\n"
    "v_mfma_f32_16x16x32_bf16 v[120:123], v[184:187], a[12:15], v[120:123]\n"
    "ds_read_b64_tr_b16 v[182:183], v225 offset:12800\n"
    "v_mul_f32 v69, s17, v69\n"
    "v_mfma_f32_16x16x32_bf16 v[124:127], v[188:191], a[12:15], v[124:127]\n"
    "ds_read_b64_tr_b16 v[184:185], v226 offset:12288\n"
    "v_mul_f32 v70, s17, v70\n"
    "v_mfma_f32_16x16x32_bf16 v[108:111], v[188:191], a[8:11], v[108:111]\n"
    "ds_read_b64_tr_b16 v[186:187], v226 offset:12800\n"
    "s_waitcnt lgkmcnt(10)\n"
    "v_mfma_f32_16x16x32_bf16 v[96:99], v[160:163], a[16:19], v[96:99]\n"
    "ds_read_b64_tr_b16 v[188:189], v227 offset:12288\n"
    "s_add_u32 m0, s85, 0x14000\n"
    "v_mfma_f32_16x16x32_bf16 v[112:115], v[160:163], a[20:23], v[112:115]\n"
    "ds_read_b64_tr_b16 v[190:191], v227 offset:12800\n"
    "v_mul_f32 v71, s17, v71\n"
    "v_mfma_f32_16x16x32_bf16 v[116:119], v[164:167], a[20:23], v[116:119]\n"
    "ds_read_b64_tr_b16 v[160:161], v224 offset:16384\n"
    "buffer_load_dwordx4 v236, s[28:31], s69 offen lds\n"
    "v_mfma_f32_16x16x32_bf16 v[100:103], v[164:167], a[16:19], v[100:103]\n"
    "ds_read_b64_tr_b16 v[162:163], v224 offset:16896\n"
    "s_waitcnt lgkmcnt(10)\n"
    "v_mfma_f32_16x16x32_bf16 v[104:107], v[168:171], a[16:19], v[104:107]\n"
    "ds_read_b64_tr_b16 v[164:165], v225 offset:16384\n"
    "v_cvt_pk_bf16_f32 v208, v64, v65\n"
    "v_mfma_f32_16x16x32_bf16 v[120:123], v[168:171], a[20:23], v[120:123]\n"
    "ds_read_b64_tr_b16 v[166:167], v225 offset:16896\n"
    "v_cvt_pk_bf16_f32 v209, v66, v67\n"
    "v_mfma_f32_16x16x32_bf16 v[124:127], v[172:175], a[20:23], v[124:127]\n"
    "ds_read_b64_tr_b16 v[168:169], v226 offset:16384\n"
    "v_cvt_pk_bf16_f32 v210, v68, v69\n"
    "v_mfma_f32_16x16x32_bf16 v[108:111], v[172:175], a[16:19], v[108:111]\n"
    "ds_read_b64_tr_b16 v[170:171], v226 offset:16896\n"
    "s_waitcnt lgkmcnt(10)\n"
    "v_mfma_f32_16x16x32_bf16 v[96:99], v[176:179], a[24:27], v[96:99]\n"
    "ds_read_b64_tr_b16 v[172:173], v227 offset:16384\n"
    "buffer_load_dwordx4 v237, s[28:31], s69 offen offset:1024 lds\n"
    "v_mfma_f32_16x16x32_bf16 v[112:115], v[176:179], a[28:31], v[112:115]\n"
    "ds_read_b64_tr_b16 v[174:175], v227 offset:16896\n"
    "v_cvt_pk_bf16_f32 v211, v70, v71\n"
    "v_mfma_f32_16x16x32_bf16 v[116:119], v[180:183], a[28:31], v[116:119]\n"
    "ds_read_b64_tr_b16 v[176:177], v224 offset:20480\n"
    "v_mul_f32 v80, s17, v80\n"
    "v_mfma_f32_16x16x32_bf16 v[100:103], v[180:183], a[24:27], v[100:103]\n"
    "ds_read_b64_tr_b16 v[178:179], v224 offset:20992\n"
    "s_waitcnt lgkmcnt(10)\n"
    "v_mfma_f32_16x16x32_bf16 v[104:107], v[184:187], a[24:27], v[104:107]\n"
    "ds_read_b64_tr_b16 v[180:181], v225 offset:20480\n"
    "v_mul_f32 v81, s17, v81\n"
    "v_mfma_f32_16x16x32_bf16 v[120:123], v[184:187], a[28:31], v[120:123]\n"
    "ds_read_b64_tr_b16 v[182:183], v225 offset:20992\n"
    "v_mul_f32 v82, s17, v82\n"
    "v_mfma_f32_16x16x32_bf16 v[124:127], v[188:191], a[28:31], v[124:127]\n"
    "ds_read_b64_tr_b16 v[184:185], v226 offset:20480\n"
    "buffer_load_dwordx4 v238, s[28:31], s69 offen offset:2048 lds\n"
    "v_mfma_f32_16x16x32_bf16 v[108:111], v[188:191], a[24:27], v[108:111]\n"
    "ds_read_b64_tr_b16 v[186:187], v226 offset:20992\n"
    "s_waitcnt lgkmcnt(10)\n"
    "v_mfma_f32_16x16x32_bf16 v[96:99], v[160:163], a[32:35], v[96:99]\n"
    "ds_read_b64_tr_b16 v[188:189], v227 offset:20480\n"
    "s_getpc_b64 s[92:93]\n"
    ".Lct3:\n"
    "v_mfma_f32_16x16x32_bf16 v[112:115], v[160:163], a[36:39], v[112:115]\n"
    "ds_read_b64_tr_b16 v[190:191], v227 offset:20992\n"
    "s_and_b32 s93, s93, 0xffff\n"
    "v_mfma_f32_16x16x32_bf16 v[116:119], v[164:167], a[36:39], v[116:119]\n"
    "ds_read_b64_tr_b16 v[160:161], v224 offset:24576\n"
    "s_mov_b32 s94, .Lend-.Lct3\n"
    "v_mfma_f32_16x16x32_bf16 v[100:103], v[164:167], a[32:35], v[100:103]\n"
    "ds_read_b64_tr_b16 v[162:163], v224 offset:25088\n"
    "s_waitcnt lgkmcnt(10)\n"
    "v_mfma_f32_16x16x32_bf16 v[104:107], v[168:171], a[32:35], v[104:107]\n"
    "ds_read_b64_tr_b16 v[164:165], v225 offset:24576\n"
    "buffer_load_dwordx4 v239, s[28:31], s69 offen offset:3072 lds\n"
    "v_mfma_f32_16x16x32_bf16 v[120:123], v[168:171], a[36:39], v[120:123]\n"
    "ds_read_b64_tr_b16 v[166:167], v225 offset:25088\n"
    "s_min_u32 s94, s94, 0x3000\n"
    "v_mfma_f32_16x16x32_bf16 v[124:127], v[172:175], a[36:39], v[124:127]\n"
    "ds_read_b64_tr_b16 v[168:169], v226 offset:24576\n"
    "s_mov_b32 s95, 0x20000\n"
    "v_mfma_f32_16x16x32_bf16 v[108:111], v[172:175], a[32:35], v[108:111]\n"
    "ds_read_b64_tr_b16 v[170:171], v226 offset:25088\n"
    "s_waitcnt lgkmcnt(10)\n"
    "v_mfma_f32_16x16x32_bf16 v[96:99], v[176:179], a[40:43], v[96:99]\n"
    "ds_read_b64_tr_b16 v[172:173], v227 offset:24576\n"
    "s_and_b32 s91, s13, 0x7f\n"
    "v_mfma_f32_16x16x32_bf16 v[112:115], v[176:179], a[44:47], v[112:115]\n"
    "ds_read_b64_tr_b16 v[174:175], v227 offset:25088\n"
    "s_lshl_b32 s91, s91, 10\n"
    "v_mfma_f32_16x16x32_bf16 v[116:119], v[180:183], a[44:47], v[116:119]\n"
    "ds_read_b64_tr_b16 v[176:177], v224 offset:28672\n"
    "buffer_load_dword v206, v240, s[92:95], s91 offen\n"
    "v_mfma_f32_16x16x32_bf16 v[100:103], v[180:183], a[40:43], v[100:103]\n"
    "ds_read_b64_tr_b16 v[178:179], v224 offset:29184\n"
    "s_waitcnt lgkmcnt(10)\n"
    "v_mfma_f32_16x16x32_bf16 v[104:107], v[184:187], a[40:43], v[104:107]\n"
    "ds_read_b64_tr_b16 v[180:181], v225 offset:28672\n"
    "v_mul_f32 v83, s17, v83\n"
    "v_mfma_f32_16x16x32_bf16 v[120:123], v[184:187], a[44:47], v[120:123]\n"
    "ds_read_b64_tr_b16 v[182:183], v225 offset:29184\n"
    "v_mul_f32 v84, s17, v84\n"
    "v_mfma_f32_16x16x32_bf16 v[124:127], v[188:191], a[44:47], v[124:127]\n"
    "ds_read_b64_tr_b16 v[184:185], v226 offset:28672\n"
    "v_mul_f32 v85, s17, v85\n"
    "v_mfma_f32_16x16x32_bf16 v[108:111], v[188:191], a[40:43], v[108:111]\n"
    "ds_read_b64_tr_b16 v[186:187], v226 offset:29184\n"
    "s_waitcnt lgkmcnt(10)\n"
    "v_mfma_f32_16x16x32_bf16 v[96:99], v[160:163], a[48:51], v[96:99]\n"
    "ds_read_b64_tr_b16 v[188:189], v227 offset:28672\n"
    "s_waitcnt vmcnt(10)\n"
    "v_mfma_f32_16x16x32_bf16 v[112:115], v[160:163], a[52:55], v[112:115]\n"
    "ds_read_b64_tr_b16 v[190:191], v227 offset:29184\n"
    "s_barrier\n"
    "v_mfma_f32_16x16x32_bf16 v[116:119], v[164:167], a[52:55], v[116:119]\n"
    "ds_read_b64_tr_b16 v[160:161], v224 offset:32768\n"
    "s_add_u32 m0, s85, 0x18000\n"
    "v_mfma_f32_16x16x32_bf16 v[100:103], v[164:167], a[48:51], v[100:103]\n"
    "ds_read_b64_tr_b16 v[162:163], v224 offset:33280\n"
    "s_waitcnt lgkmcnt(10)\n"
    "v_mfma_f32_16x16x32_bf16 v[104:107], v[168:171], a[48:51], v[104:107]\n"
    "ds_read_b64_tr_b16 v[164:165], v225 offset:32768\n"
    "buffer_load_dwordx4 v236, s[28:31], s70 offen lds\n"
    "v_mfma_f32_16x16x32_bf16 v[120:123], v[168:171], a[52:55], v[120:123]\n"
    "ds_read_b64_tr_b16 v[166:167], v225 offset:33280\n"
    "v_mul_f32 v86, s17, v86\n"
    "v_mfma_f32_16x16x32_bf16 v[124:127], v[172:175], a[52:55], v[124:127]\n"
    "ds_read_b64_tr_b16 v[168:169], v226 offset:32768\n"
    "v_mul_f32 v87, s17, v87\n"
    "v_mfma_f32_16x16x32_bf16 v[108:111], v[172:175], a[48:51], v[108:111]\n"
    "ds_read_b64_tr_b16 v[170:171], v226 offset:33280\n"
    "s_waitcnt lgkmcnt(10)\n"
    "v_mfma_f32_16x16x32_bf16 v[96:99], v[176:179], a[56:59], v[96:99]\n"
    "ds_read_b64_tr_b16 v[172:173], v227 offset:32768\n"
    "s_add_u32 s99, s87, 0x20000\n"
    "v_mfma_f32_16x16x32_bf16 v[112:115], v[176:179], a[60:63], v[112:115]\n"
    "ds_read_b64_tr_b16 v[174:175], v227 offset:33280\n"
    "buffer_store_dwordx4 v[152:155], v240, s[44:47], s99 offen sc1\n"
    "v_mfma_f32_16x16x32_bf16 v[116:119], v[180:183], a[60:63], v[116:119]\n"
    "ds_read_b64_tr_b16 v[176:177], v224 offset:36864\n"
    "buffer_load_dwordx4 v237, s[28:31], s70 offen offset:1024 lds\n"
    "v_mfma_f32_16x16x32_bf16 v[100:103], v[180:183], a[56:59], v[100:103]\n"
    "ds_read_b64_tr_b16 v[178:179], v224 offset:37376\n"
    "s_waitcnt lgkmcnt(10)\n"
    "v_mfma_f32_16x16x32_bf16 v[104:107], v[184:187], a[56:59], v[104:107]\n"
    "ds_read_b64_tr_b16 v[180:181], v225 offset:36864\n"
    "v_cvt_pk_bf16_f32 v212, v80, v81\n"
    "v_mfma_f32_16x16x32_bf16 v[120:123], v[184:187], a[60:63], v[120:123]\n"
    "ds_read_b64_tr_b16 v[182:183], v225 offset:37376\n"
    "v_cvt_pk_bf16_f32 v213, v82, v83\n"
    "v_mfma_f32_16x16x32_bf16 v[124:127], v[188:191], a[60:63], v[124:127]\n"
    "ds_read_b64_tr_b16 v[184:185], v226 offset:36864\n"
    "v_cvt_pk_bf16_f32 v214, v84, v85\n"
    "v_mfma_f32_16x16x32_bf16 v[108:111], v[188:191], a[56:59], v[108:111]\n"
    "ds_read_b64_tr_b16 v[186:187], v226 offset:37376\n"
    "s_waitcnt lgkmcnt(10)\n"
    "v_mfma_f32_16x16x32_bf16 v[96:99], v[160:163], a[64:67], v[96:99]\n"
    "ds_read_b64_tr_b16 v[188:189], v227 offset:36864\n"
    "v_cvt_pk_bf16_f32 v215, v86, v87\n"
    "v_mfma_f32_16x16x32_bf16 v[112:115], v[160:163], a[68:71], v[112:115]\n"
    "ds_read_b64_tr_b16 v[190:191], v227 offset:37376\n"
    "v_mul_f32 v72, s17, v72\n"
    "v_mfma_f32_16x16x32_bf16 v[116:119], v[164:167], a[68:71], v[116:119]\n"
    "ds_read_b64_tr_b16 v[160:161], v224 offset:40960\n"
    "buffer_load_dwordx4 v238, s[28:31], s70 offen offset:2048 lds\n"
    "v_mfma_f32_16x16x32_bf16 v[100:103], v[164:167], a[64:67], v[100:103]\n"
    "ds_read_b64_tr_b16 v[162:163], v224 offset:41472\n"
    "s_waitcnt lgkmcnt(10)\n"
    "v_mfma_f32_16x16x32_bf16 v[104:107], v[168:171], a[64:67], v[104:107]\n"
    "ds_read_b64_tr_b16 v[164:165], v225 offset:40960\n"
    "v_mul_f32 v73, s17, v73\n"
    "v_mfma_f32_16x16x32_bf16 v[120:123], v[168:171], a[68:71], v[120:123]\n"
    "ds_read_b64_tr_b16 v[166:167], v225 offset:41472\n"
    "v_mul_f32 v74, s17, v74\n"
    "v_mfma_f32_16x16x32_bf16 v[124:127], v[172:175], a[68:71], v[124:127]\n"
    "ds_read_b64_tr_b16 v[168:169], v226 offset:40960\n"
    "v_mul_f32 v75, s17, v75\n"
    "v_mfma_f32_16x16x32_bf16 v[108:111], v[172:175], a[64:67], v[108:111]\n"
    "ds_read_b64_tr_b16 v[170:171], v226 offset:41472\n"
    "s_waitcnt lgkmcnt(10)\n"
    "v_mfma_f32_16x16x32_bf16 v[96:99], v[176:179], a[72:75], v[96:99]\n"
    "ds_read_b64_tr_b16 v[172:173], v227 offset:40960\n"
    "v_mul_f32 v76, s17, v76\n"
    "v_mfma_f32_16x16x32_bf16 v[112:115], v[176:179], a[76:79], v[112:115]\n"
    "ds_read_b64_tr_b16 v[174:175], v227 offset:41472\n"
    "v_mul_f32 v77, s17, v77\n"
    "v_mfma_f32_16x16x32_bf16 v[116:119], v[180:183], a[76:79], v[116:119]\n"
    "ds_read_b64_tr_b16 v[176:177], v224 offset:45056\n"
    "buffer_load_dwordx4 v239, s[28:31], s70 offen offset:3072 lds\n"
    "v_mfma_f32_16x16x32_bf16 v[100:103], v[180:183], a[72:75], v[100:103]\n"
    "ds_read_b64_tr_b16 v[178:179], v224 offset:45568\n"
    "s_waitcnt lgkmcnt(10)\n"
    "v_mfma_f32_16x16x32_bf16 v[104:107], v[184:187], a[72:75], v[104:107]\n"
    "ds_read_b64_tr_b16 v[180:181], v225 offset:45056\n"
    "v_mul_f32 v78, s17, v78\n"
    "v_mfma_f32_16x16x32_bf16 v[120:123], v[184:187], a[76:79], v[120:123]\n"
    "ds_read_b64_tr_b16 v[182:183], v225 offset:45568\n"
    "v_mul_f32 v79, s17, v79\n"
    "v_mfma_f32_16x16x32_bf16 v[124:127], v[188:191], a[76:79], v[124:127]\n"
    "ds_read_b64_tr_b16 v[184:185], v226 offset:45056\n"
    "v_cvt_pk_bf16_f32 v216, v72, v73\n"
    "v_mfma_f32_16x16x32_bf16 v[108:111], v[188:191], a[72:75], v[108:111]\n"
    "ds_read_b64_tr_b16 v[186:187], v226 offset:45568\n"
    "s_waitcnt lgkmcnt(10)\n"
    "v_mfma_f32_16x16x32_bf16 v[96:99], v[160:163], a[80:83], v[96:99]\n"
    "ds_read_b64_tr_b16 v[188:189], v227 offset:45056\n"
    "s_add_u32 m0, s85, 0\n"
    "v_mfma_f32_16x16x32_bf16 v[112:115], v[160:163], a[84:87], v[112:115]\n"
    "ds_read_b64_tr_b16 v[190:191], v227 offset:45568\n"
    "v_cvt_pk_bf16_f32 v217, v74, v75\n"
    "v_mfma_f32_16x16x32_bf16 v[116:119], v[164:167], a[84:87], v[116:119]\n"
    "ds_read_b64_tr_b16 v[160:161], v224 offset:49152\n"
    "buffer_load_dwordx4 v236, s[28:31], s71 offen lds\n"
    "v_mfma_f32_16x16x32_bf16 v[100:103], v[164:167], a[80:83], v[100:103]\n"
    "ds_read_b64_tr_b16 v[162:163], v224 offset:49664\n"
    "s_waitcnt lgkmcnt(10)\n"
    "v_mfma_f32_16x16x32_bf16 v[104:107], v[168:171], a[80:83], v[104:107]\n"
    "ds_read_b64_tr_b16 v[164:165], v225 offset:49152\n"
    "v_cvt_pk_bf16_f32 v218, v76, v77\n"
    "v_mfma_f32_16x16x32_bf16 v[120:123], v[168:171], a[84:87], v[120:123]\n"
    "ds_read_b64_tr_b16 v[166:167], v225 offset:49664\n"
    "v_cvt_pk_bf16_f32 v219, v78, v79\n"
    "v_mfma_f32_16x16x32_bf16 v[124:127], v[172:175], a[84:87], v[124:127]\n"
    "ds_read_b64_tr_b16 v[168:169], v226 offset:49152\n"
    "v_mul_f32 v88, s17, v88\n"
    "v_mfma_f32_16x16x32_bf16 v[108:111], v[172:175], a[80:83], v[108:111]\n"
    "ds_read_b64_tr_b16 v[170:171], v226 offset:49664\n"
    "s_waitcnt lgkmcnt(10)\n"
    "v_mfma_f32_16x16x32_bf16 v[96:99], v[176:179], a[88:91], v[96:99]\n"
    "ds_read_b64_tr_b16 v[172:173], v227 offset:49152\n"
    "buffer_load_dwordx4 v237, s[28:31], s71 offen offset:1024 lds\n"
    "v_mfma_f32_16x16x32_bf16 v[112:115], v[176:179], a[92:95], v[112:115]\n"
    "ds_read_b64_tr_b16 v[174:175], v227 offset:49664\n"
    "v_mul_f32 v89, s17, v89\n"
    "v_mfma_f32_16x16x32_bf16 v[116:119], v[180:183], a[92:95], v[116:119]\n"
    "ds_read_b64_tr_b16 v[176:177], v224 offset:53248\n"
    "v_mul_f32 v90, s17, v90\n"
    "v_mfma_f32_16x16x32_bf16 v[100:103], v[180:183], a[88:91], v[100:103]\n"
    "ds_read_b64_tr_b16 v[178:179], v224 offset:53760\n"
    "s_waitcnt lgkmcnt(10)\n"
    "v_mfma_f32_16x16x32_bf16 v[104:107], v[184:187], a[88:91], v[104:107]\n"
    "ds_read_b64_tr_b16 v[180:181], v225 offset:53248\n"
    "v_mul_f32 v91, s17, v91\n"
    "v_mfma_f32_16x16x32_bf16 v[120:123], v[184:187], a[92:95], v[120:123]\n"
    "ds_read_b64_tr_b16 v[182:183], v225 offset:53760\n"
    "v_mul_f32 v92, s17, v92\n"
    "v_mfma_f32_16x16x32_bf16 v[124:127], v[188:191], a[92:95], v[124:127]\n"
    "ds_read_b64_tr_b16 v[184:185], v226 offset:53248\n"
    "buffer_load_dwordx4 v238, s[28:31], s71 offen offset:2048 lds\n"
    "v_mfma_f32_16x16x32_bf16 v[108:111], v[188:191], a[88:91], v[108:111]\n"
    "ds_read_b64_tr_b16 v[186:187], v226 offset:53760\n"
    "s_waitcnt lgkmcnt(10)\n"
    "v_mfma_f32_16x16x32_bf16 v[96:99], v[160:163], a[96:99], v[96:99]\n"
    "ds_read_b64_tr_b16 v[188:189], v227 offset:53248\n"
    "v_mul_f32 v93, s17, v93\n"
    "v_mfma_f32_16x16x32_bf16 v[112:115], v[160:163], a[100:103], v[112:115]\n"
    "ds_read_b64_tr_b16 v[190:191], v227 offset:53760\n"
    "v_mul_f32 v94, s17, v94\n"
    "v_mfma_f32_16x16x32_bf16 v[116:119], v[164:167], a[100:103], v[116:119]\n"
    "ds_read_b64_tr_b16 v[160:161], v224 offset:57344\n"
    "v_mul_f32 v95, s17, v95\n"
    "v_mfma_f32_16x16x32_bf16 v[100:103], v[164:167], a[96:99], v[100:103]\n"
    "ds_read_b64_tr_b16 v[162:163], v224 offset:57856\n"
    "s_waitcnt lgkmcnt(10)\n"
    "v_mfma_f32_16x16x32_bf16 v[104:107], v[168:171], a[96:99], v[104:107]\n"
    "ds_read_b64_tr_b16 v[164:165], v225 offset:57344\n"
    "buffer_load_dwordx4 v239, s[28:31], s71 offen offset:3072 lds\n"
    "v_mfma_f32_16x16x32_bf16 v[120:123], v[168:171], a[100:103], v[120:123]\n"
    "ds_read_b64_tr_b16 v[166:167], v225 offset:57856\n"
    "v_cvt_pk_bf16_f32 v220, v88, v89\n"
    "v_mfma_f32_16x16x32_bf16 v[124:127], v[172:175], a[100:103], v[124:127]\n"
    "ds_read_b64_tr_b16 v[168:169], v226 offset:57344\n"
    "v_cvt_pk_bf16_f32 v221, v90, v91\n"
    "v_mfma_f32_16x16x32_bf16 v[108:111], v[172:175], a[96:99], v[108:111]\n"
    "ds_read_b64_tr_b16 v[170:171], v226 offset:57856\n"
    "s_waitcnt lgkmcnt(10)\n"
    "v_mfma_f32_16x16x32_bf16 v[96:99], v[176:179], a[104:107], v[96:99]\n"
    "ds_read_b64_tr_b16 v[172:173], v227 offset:57344\n"
    "buffer_store_dwordx4 v[156:159], v240, s[44:47], s99 offen offset:1024 sc1\n"
    "v_mfma_f32_16x16x32_bf16 v[112:115], v[176:179], a[108:111], v[112:115]\n"
    "ds_read_b64_tr_b16 v[174:175], v227 offset:57856\n"
    "v_cvt_pk_bf16_f32 v222, v92, v93\n"
    "v_mfma_f32_16x16x32_bf16 v[116:119], v[180:183], a[108:111], v[116:119]\n"
    "ds_read_b64_tr_b16 v[176:177], v224 offset:61440\n"
    "v_cvt_pk_bf16_f32 v223, v94, v95\n"
    "v_mfma_f32_16x16x32_bf16 v[100:103], v[180:183], a[104:107], v[100:103]\n"
    "ds_read_b64_tr_b16 v[178:179], v224 offset:61952\n"
    "s_waitcnt lgkmcnt(10)\n"
    "v_mfma_f32_16x16x32_bf16 v[104:107], v[184:187], a[104:107], v[104:107]\n"
    "ds_read_b64_tr_b16 v[180:181], v225 offset:61440\n"
    "v_mfma_f32_16x16x32_bf16 v[120:123], v[184:187], a[108:111], v[120:123]\n"
    "ds_read_b64_tr_b16 v[182:183], v225 offset:61952\n"
    "v_mfma_f32_16x16x32_bf16 v[124:127], v[188:191], a[108:111], v[124:127]\n"
    "ds_read_b64_tr_b16 v[184:185], v226 offset:61440\n"
    "v_mfma_f32_16x16x32_bf16 v[108:111], v[188:191], a[104:107], v[108:111]\n"
    "ds_read_b64_tr_b16 v[186:187], v226 offset:61952\n"
    "s_waitcnt lgkmcnt(10)\n"
    "v_mfma_f32_16x16x32_bf16 v[96:99], v[160:163], a[112:115], v[96:99]\n"
    "ds_read_b64_tr_b16 v[188:189], v227 offset:61440\n"
    "s_waitcnt vmcnt(11)\n"
    "v_mfma_f32_16x16x32_bf16 v[112:115], v[160:163], a[116:119], v[112:115]\n"
    "ds_read_b64_tr_b16 v[190:191], v227 offset:61952\n"
    "s_barrier\n"
    "v_mfma_f32_16x16x32_bf16 v[116:119], v[164:167], a[116:119], v[116:119]\n"
    "ds_read_b64_tr_b16 v[160:161], v228\n"
    "s_add_u32 m0, s85, 0x4000\n"
    "v_mfma_f32_16x16x32_bf16 v[100:103], v[164:167], a[112:115], v[100:103]\n"
    "ds_read_b64_tr_b16 v[162:163], v228 offset:512\n"
    "s_waitcnt lgkmcnt(10)\n"
    "v_mfma_f32_16x16x32_bf16 v[104:107], v[168:171], a[112:115], v[104:107]\n"
    "ds_read_b64_tr_b16 v[164:165], v229\n"
    "s_add_u32 s98, s64, 0x80\n"
    "v_mfma_f32_16x16x32_bf16 v[120:123], v[168:171], a[116:119], v[120:123]\n"
    "ds_read_b64_tr_b16 v[166:167], v229 offset:512\n"
    "buffer_load_dwordx4 v236, s[28:31], s98 offen lds\n"
    "v_mfma_f32_16x16x32_bf16 v[124:127], v[172:175], a[116:119], v[124:127]\n"
    "ds_read_b64_tr_b16 v[168:169], v230\n"
    "v_mbcnt_lo_u32_b32 v251, -1, 0\n"
    "v_mfma_f32_16x16x32_bf16 v[108:111], v[172:175], a[112:115], v[108:111]\n"
    "ds_read_b64_tr_b16 v[170:171], v230 offset:512\n"
    "s_waitcnt lgkmcnt(10)\n"
    "v_mfma_f32_16x16x32_bf16 v[96:99], v[176:179], a[120:123], v[96:99]\n"
    "ds_read_b64_tr_b16 v[172:173], v231\n"
    "v_mbcnt_hi_u32_b32 v251, -1, v251\n"
    "v_mfma_f32_16x16x32_bf16 v[112:115], v[176:179], a[124:127], v[112:115]\n"
    "ds_read_b64_tr_b16 v[174:175], v231 offset:512\n"
    "s_add_u32 s89, s51, 0\n"
    "v_mfma_f32_16x16x32_bf16 v[116:119], v[180:183], a[124:127], v[116:119]\n"
    "ds_read_b64_tr_b16 v[176:177], v228 offset:4096\n"
    "buffer_load_dwordx4 v237, s[28:31], s98 offen offset:1024 lds\n"
    "v_mfma_f32_16x16x32_bf16 v[100:103], v[180:183], a[120:123], v[100:103]\n"
    "ds_read_b64_tr_b16 v[178:179], v228 offset:4608\n"
    "s_waitcnt lgkmcnt(10)\n"
    "v_mfma_f32_16x16x32_bf16 v[104:107], v[184:187], a[120:123], v[104:107]\n"
    "ds_read_b64_tr_b16 v[180:181], v229 offset:4096\n"
    "s_and_b32 s89, s89, 3\n"
    "v_mfma_f32_16x16x32_bf16 v[120:123], v[184:187], a[124:127], v[120:123]\n"
    "ds_read_b64_tr_b16 v[182:183], v229 offset:4608\n"
    "s_lshl_b32 s89, s89, 10\n"
    "v_mfma_f32_16x16x32_bf16 v[124:127], v[188:191], a[124:127], v[124:127]\n"
    "ds_read_b64_tr_b16 v[184:185], v230 offset:4096\n"
    "s_add_u32 s89, s89, s12\n"
    "v_mfma_f32_16x16x32_bf16 v[108:111], v[188:191], a[120:123], v[108:111]\n"
    "ds_read_b64_tr_b16 v[186:187], v230 offset:4608\n"
    "s_waitcnt lgkmcnt(10)\n"
    "v_mfma_f32_16x16x32_bf16 v[96:99], v[160:163], a[128:131], v[96:99]\n"
    "ds_read_b64_tr_b16 v[188:189], v231 offset:4096\n"
    "v_add_u32 v192, s89, v240\n"
    "v_mfma_f32_16x16x32_bf16 v[112:115], v[160:163], a[132:135], v[112:115]\n"
    "ds_read_b64_tr_b16 v[190:191], v231 offset:4608\n"
    "v_add_u32 v196, s89, v241\n"
    "v_mfma_f32_16x16x32_bf16 v[116:119], v[164:167], a[132:135], v[116:119]\n"
    "ds_read_b64_tr_b16 v[160:161], v228 offset:8192\n"
    "buffer_load_dwordx4 v238, s[28:31], s98 offen offset:2048 lds\n"
    "v_mfma_f32_16x16x32_bf16 v[100:103], v[164:167], a[128:131], v[100:103]\n"
    "ds_read_b64_tr_b16 v[162:163], v228 offset:8704\n"
    "s_waitcnt lgkmcnt(10)\n"
    "v_mfma_f32_16x16x32_bf16 v[104:107], v[168:171], a[128:131], v[104:107]\n"
    "ds_read_b64_tr_b16 v[164:165], v229 offset:8192\n"
    "s_add_u32 s89, s51, 1\n"
    "v_mfma_f32_16x16x32_bf16 v[120:123], v[168:171], a[132:135], v[120:123]\n"
    "ds_read_b64_tr_b16 v[166:167], v229 offset:8704\n"
    "s_and_b32 s89, s89, 3\n"
    "v_mfma_f32_16x16x32_bf16 v[124:127], v[172:175], a[132:135], v[124:127]\n"
    "ds_read_b64_tr_b16 v[168:169], v230 offset:8192\n"
    "s_lshl_b32 s89, s89, 10\n"
    "v_mfma_f32_16x16x32_bf16 v[108:111], v[172:175], a[128:131], v[108:111]\n"
    "ds_read_b64_tr_b16 v[170:171], v230 offset:8704\n"
    "s_waitcnt lgkmcnt(10)\n"
    "v_mfma_f32_16x16x32_bf16 v[96:99], v[176:179], a[136:139], v[96:99]\n"
    "ds_read_b64_tr_b16 v[172:173], v231 offset:8192\n"
    "s_add_u32 s89, s89, s12\n"
    "v_mfma_f32_16x16x32_bf16 v[112:115], v[176:179], a[140:143], v[112:115]\n"
    "ds_read_b64_tr_b16 v[174:175], v231 offset:8704\n"
    "v_add_u32 v193, s89, v240\n"
    "v_mfma_f32_16x16x32_bf16 v[116:119], v[180:183], a[140:143], v[116:119]\n"
    "ds_read_b64_tr_b16 v[176:177], v228 offset:12288\n"
    "buffer_load_dwordx4 v239, s[28:31], s98 offen offset:3072 lds\n"
    "v_mfma_f32_16x16x32_bf16 v[100:103], v[180:183], a[136:139], v[100:103]\n"
    "ds_read_b64_tr_b16 v[178:179], v228 offset:12800\n"
    "s_waitcnt lgkmcnt(10)\n"
    "v_mfma_f32_16x16x32_bf16 v[104:107], v[184:187], a[136:139], v[104:107]\n"
    "ds_read_b64_tr_b16 v[180:181], v229 offset:12288\n"
    "v_add_u32 v197, s89, v241\n"
    "v_mfma_f32_16x16x32_bf16 v[120:123], v[184:187], a[140:143], v[120:123]\n"
    "ds_read_b64_tr_b16 v[182:183], v229 offset:12800\n"
    "s_add_u32 s89, s51, 2\n"
    "v_mfma_f32_16x16x32_bf16 v[124:127], v[188:191], a[140:143], v[124:127]\n"
    "ds_read_b64_tr_b16 v[184:185], v230 offset:12288\n"
    "s_and_b32 s89, s89, 3\n"
    "v_mfma_f32_16x16x32_bf16 v[108:111], v[188:191], a[136:139], v[108:111]\n"
    "ds_read_b64_tr_b16 v[186:187], v230 offset:12800\n"
    "s_waitcnt lgkmcnt(10)\n"
    "v_mfma_f32_16x16x32_bf16 v[96:99], v[160:163], a[144:147], v[96:99]\n"
    "ds_read_b64_tr_b16 v[188:189], v231 offset:12288\n"
    "s_add_u32 m0, s85, 0x8000\n"
    "v_mfma_f32_16x16x32_bf16 v[112:115], v[160:163], a[148:151], v[112:115]\n"
    "ds_read_b64_tr_b16 v[190:191], v231 offset:12800\n"
    "s_add_u32 s98, s65, 0x80\n"
    "v_mfma_f32_16x16x32_bf16 v[116:119], v[164:167], a[148:151], v[116:119]\n"
    "ds_read_b64_tr_b16 v[160:161], v228 offset:16384\n"
    "buffer_load_dwordx4 v236, s[28:31], s98 offen lds\n"
    "v_mfma_f32_16x16x32_bf16 v[100:103], v[164:167], a[144:147], v[100:103]\n"
    "ds_read_b64_tr_b16 v[162:163], v228 offset:16896\n"
    "s_waitcnt lgkmcnt(10)\n"
    "v_mfma_f32_16x16x32_bf16 v[104:107], v[168:171], a[144:147], v[104:107]\n"
    "ds_read_b64_tr_b16 v[164:165], v229 offset:16384\n"
    "s_lshl_b32 s89, s89, 10\n"
    "v_mfma_f32_16x16x32_bf16 v[120:123], v[168:171], a[148:151], v[120:123]\n"
    "ds_read_b64_tr_b16 v[166:167], v229 offset:16896\n"
    "s_add_u32 s89, s89, s12\n"
    "v_mfma_f32_16x16x32_bf16 v[124:127], v[172:175], a[148:151], v[124:127]\n"
    "ds_read_b64_tr_b16 v[168:169], v230 offset:16384\n"
    "v_add_u32 v194, s89, v240\n"
    "v_mfma_f32_16x16x32_bf16 v[108:111], v[172:175], a[144:147], v[108:111]\n"
    "ds_read_b64_tr_b16 v[170:171], v230 offset:16896\n"
    "s_waitcnt lgkmcnt(10)\n"
    "v_mfma_f32_16x16x32_bf16 v[96:99], v[176:179], a[152:155], v[96:99]\n"
    "ds_read_b64_tr_b16 v[172:173], v231 offset:16384\n"
    "buffer_load_dwordx4 v237, s[28:31], s98 offen offset:1024 lds\n"
    "v_mfma_f32_16x16x32_bf16 v[112:115], v[176:179], a[156:159], v[112:115]\n"
    "ds_read_b64_tr_b16 v[174:175], v231 offset:16896\n"
    "v_add_u32 v198, s89, v241\n"
    "v_mfma_f32_16x16x32_bf16 v[116:119], v[180:183], a[156:159], v[116:119]\n"
    "ds_read_b64_tr_b16 v[176:177], v228 offset:20480\n"
    "s_add_u32 s89, s51, 3\n"
    "v_mfma_f32_16x16x32_bf16 v[100:103], v[180:183], a[152:155], v[100:103]\n"
    "ds_read_b64_tr_b16 v[178:179], v228 offset:20992\n"
    "s_waitcnt lgkmcnt(10)\n"
    "v_mfma_f32_16x16x32_bf16 v[104:107], v[184:187], a[152:155], v[104:107]\n"
    "ds_read_b64_tr_b16 v[180:181], v229 offset:20480\n"
    "s_and_b32 s89, s89, 3\n"
    "v_mfma_f32_16x16x32_bf16 v[120:123], v[184:187], a[156:159], v[120:123]\n"
    "ds_read_b64_tr_b16 v[182:183], v229 offset:20992\n"
    "s_lshl_b32 s89, s89, 10\n"
    "v_mfma_f32_16x16x32_bf16 v[124:127], v[188:191], a[156:159], v[124:127]\n"
    "ds_read_b64_tr_b16 v[184:185], v230 offset:20480\n"
    "buffer_load_dwordx4 v238, s[28:31], s98 offen offset:2048 lds\n"
    "v_mfma_f32_16x16x32_bf16 v[108:111], v[188:191], a[152:155], v[108:111]\n"
    "ds_read_b64_tr_b16 v[186:187], v230 offset:20992\n"
    "s_waitcnt lgkmcnt(10)\n"
    "v_mfma_f32_16x16x32_bf16 v[96:99], v[160:163], a[160:163], v[96:99]\n"
    "ds_read_b64_tr_b16 v[188:189], v231 offset:20480\n"
    "s_add_u32 s89, s89, s12\n"
    "v_mfma_f32_16x16x32_bf16 v[112:115], v[160:163], a[164:167], v[112:115]\n"
    "ds_read_b64_tr_b16 v[190:191], v231 offset:20992\n"
    "v_add_u32 v195, s89, v240\n"
    "v_mfma_f32_16x16x32_bf16 v[116:119], v[164:167], a[164:167], v[116:119]\n"
    "ds_read_b64_tr_b16 v[160:161], v228 offset:24576\n"
    "v_add_u32 v199, s89, v241\n"
    "v_mfma_f32_16x16x32_bf16 v[100:103], v[164:167], a[160:163], v[100:103]\n"
    "ds_read_b64_tr_b16 v[162:163], v228 offset:25088\n"
    "s_waitcnt lgkmcnt(10)\n"
    "v_mfma_f32_16x16x32_bf16 v[104:107], v[168:171], a[160:163], v[104:107]\n"
    "ds_read_b64_tr_b16 v[164:165], v229 offset:24576\n"
    "buffer_load_dwordx4 v239, s[28:31], s98 offen offset:3072 lds\n"
    "v_mfma_f32_16x16x32_bf16 v[120:123], v[168:171], a[164:167], v[120:123]\n"
    "ds_read_b64_tr_b16 v[166:167], v229 offset:25088\n"
    "v_lshrrev_b32 v249, 4, v251\n"
    "v_mfma_f32_16x16x32_bf16 v[124:127], v[172:175], a[164:167], v[124:127]\n"
    "ds_read_b64_tr_b16 v[168:169], v230 offset:24576\n"
    "v_bfe_u32 v250, v251, 1, 3\n"
    "v_mfma_f32_16x16x32_bf16 v[108:111], v[172:175], a[160:163], v[108:111]\n"
    "ds_read_b64_tr_b16 v[170:171], v230 offset:25088\n"
    "s_waitcnt lgkmcnt(10)\n"
    "v_mfma_f32_16x16x32_bf16 v[96:99], v[176:179], a[168:171], v[96:99]\n"
    "ds_read_b64_tr_b16 v[172:173], v231 offset:24576\n"
    "v_xor_b32 v250, v250, v249\n"
    "v_mfma_f32_16x16x32_bf16 v[112:115], v[176:179], a[172:175], v[112:115]\n"
    "ds_read_b64_tr_b16 v[174:175], v231 offset:25088\n"
    "v_lshlrev_b32 v250, 5, v250\n"
    "v_mfma_f32_16x16x32_bf16 v[116:119], v[180:183], a[172:175], v[116:119]\n"
    "ds_read_b64_tr_b16 v[176:177], v228 offset:28672\n"
    "v_lshl_add_u32 v250, v249, 11, v250\n"
    "v_mfma_f32_16x16x32_bf16 v[100:103], v[180:183], a[168:171], v[100:103]\n"
    "ds_read_b64_tr_b16 v[178:179], v228 offset:29184\n"
    "s_waitcnt lgkmcnt(10)\n"
    "v_mfma_f32_16x16x32_bf16 v[104:107], v[184:187], a[168:171], v[104:107]\n"
    "ds_read_b64_tr_b16 v[180:181], v229 offset:28672\n"
    "v_and_b32 v249, 1, v251\n"
    "v_mfma_f32_16x16x32_bf16 v[120:123], v[184:187], a[172:175], v[120:123]\n"
    "ds_read_b64_tr_b16 v[182:183], v229 offset:29184\n"
    "v_lshl_add_u32 v250, v249, 4, v250\n"
    "v_mfma_f32_16x16x32_bf16 v[124:127], v[188:191], a[172:175], v[124:127]\n"
    "ds_read_b64_tr_b16 v[184:185], v230 offset:28672\n"
    "s_lshl_b32 s89, s50, 15\n"
    "v_mfma_f32_16x16x32_bf16 v[108:111], v[188:191], a[168:171], v[108:111]\n"
    "ds_read_b64_tr_b16 v[186:187], v230 offset:29184\n"
    "s_waitcnt lgkmcnt(10)\n"
    "v_mfma_f32_16x16x32_bf16 v[96:99], v[160:163], a[176:179], v[96:99]\n"
    "ds_read_b64_tr_b16 v[188:189], v231 offset:28672\n"
    "s_waitcnt vmcnt(9)\n"
    "v_mfma_f32_16x16x32_bf16 v[112:115], v[160:163], a[180:183], v[112:115]\n"
    "ds_read_b64_tr_b16 v[190:191], v231 offset:29184\n"
    "s_barrier\n"
    "v_mfma_f32_16x16x32_bf16 v[116:119], v[164:167], a[180:183], v[116:119]\n"
    "ds_read_b64_tr_b16 v[160:161], v228 offset:32768\n"
    "s_add_u32 m0, s85, 0xc000\n"
    "v_mfma_f32_16x16x32_bf16 v[100:103], v[164:167], a[176:179], v[100:103]\n"
    "ds_read_b64_tr_b16 v[162:163], v228 offset:33280\n"
    "s_waitcnt lgkmcnt(10)\n"
    "v_mfma_f32_16x16x32_bf16 v[104:107], v[168:171], a[176:179], v[104:107]\n"
    "ds_read_b64_tr_b16 v[164:165], v229 offset:32768\n"
    "s_add_u32 s98, s66, 0x80\n"
    "v_mfma_f32_16x16x32_bf16 v[120:123], v[168:171], a[180:183], v[120:123]\n"
    "ds_read_b64_tr_b16 v[166:167], v229 offset:33280\n"
    "buffer_load_dwordx4 v236, s[28:31], s98 offen lds\n"
    "v_mfma_f32_16x16x32_bf16 v[124:127], v[172:175], a[180:183], v[124:127]\n"
    "ds_read_b64_tr_b16 v[168:169], v230 offset:32768\n"
    "s_lshl_b32 s90, s53, 8\n"
    "v_mfma_f32_16x16x32_bf16 v[108:111], v[172:175], a[176:179], v[108:111]\n"
    "ds_read_b64_tr_b16 v[170:171], v230 offset:33280\n"
    "s_waitcnt lgkmcnt(10)\n"
    "v_mfma_f32_16x16x32_bf16 v[96:99], v[176:179], a[184:187], v[96:99]\n"
    "ds_read_b64_tr_b16 v[172:173], v231 offset:32768\n"
    "s_add_u32 s89, s89, s90\n"
    "v_mfma_f32_16x16x32_bf16 v[112:115], v[176:179], a[188:191], v[112:115]\n"
    "ds_read_b64_tr_b16 v[174:175], v231 offset:33280\n"
    "s_mul_i32 s90, s51, 0x1c0000\n"
    "v_mfma_f32_16x16x32_bf16 v[116:119], v[180:183], a[188:191], v[116:119]\n"
    "ds_read_b64_tr_b16 v[176:177], v228 offset:36864\n"
    "buffer_load_dwordx4 v237, s[28:31], s98 offen offset:1024 lds\n"
    "v_mfma_f32_16x16x32_bf16 v[100:103], v[180:183], a[184:187], v[100:103]\n"
    "ds_read_b64_tr_b16 v[178:179], v228 offset:37376\n"
    "s_waitcnt lgkmcnt(10)\n"
    "v_mfma_f32_16x16x32_bf16 v[104:107], v[184:187], a[184:187], v[104:107]\n"
    "ds_read_b64_tr_b16 v[180:181], v229 offset:36864\n"
    "s_add_u32 s89, s89, s90\n"
    "v_mfma_f32_16x16x32_bf16 v[120:123], v[184:187], a[188:191], v[120:123]\n"
    "ds_read_b64_tr_b16 v[182:183], v229 offset:37376\n"
    "v_add_u32 v250, s89, v250\n"
    "v_mfma_f32_16x16x32_bf16 v[124:127], v[188:191], a[188:191], v[124:127]\n"
    "ds_read_b64_tr_b16 v[184:185], v230 offset:36864\n"
    "v_add_u32 v200, 0, v250\n"
    "v_mfma_f32_16x16x32_bf16 v[108:111], v[188:191], a[184:187], v[108:111]\n"
    "ds_read_b64_tr_b16 v[186:187], v230 offset:37376\n"
    "s_waitcnt lgkmcnt(10)\n"
    "v_mfma_f32_16x16x32_bf16 v[96:99], v[160:163], a[192:195], v[96:99]\n"
    "ds_read_b64_tr_b16 v[188:189], v231 offset:36864\n"
    "v_mov_b32 v0, -1\n"
    "v_mfma_f32_16x16x32_bf16 v[112:115], v[160:163], a[196:199], v[112:115]\n"
    "ds_read_b64_tr_b16 v[190:191], v231 offset:37376\n"
    "v_mov_b32 v1, -1\n"
    "v_mfma_f32_16x16x32_bf16 v[116:119], v[164:167], a[196:199], v[116:119]\n"
    "ds_read_b64_tr_b16 v[160:161], v228 offset:40960\n"
    "buffer_load_dwordx4 v238, s[28:31], s98 offen offset:2048 lds\n"
    "v_mfma_f32_16x16x32_bf16 v[100:103], v[164:167], a[192:195], v[100:103]\n"
    "ds_read_b64_tr_b16 v[162:163], v228 offset:41472\n"
    "s_waitcnt lgkmcnt(10)\n"
    "v_mfma_f32_16x16x32_bf16 v[104:107], v[168:171], a[192:195], v[104:107]\n"
    "ds_read_b64_tr_b16 v[164:165], v229 offset:40960\n"
    "v_mov_b32 v2, -1\n"
    "v_mfma_f32_16x16x32_bf16 v[120:123], v[168:171], a[196:199], v[120:123]\n"
    "ds_read_b64_tr_b16 v[166:167], v229 offset:41472\n"
    "v_mov_b32 v3, -1\n"
    "v_mfma_f32_16x16x32_bf16 v[124:127], v[172:175], a[196:199], v[124:127]\n"
    "ds_read_b64_tr_b16 v[168:169], v230 offset:40960\n"
    "s_add_u32 s99, s15, 0xc000\n"
    "v_mfma_f32_16x16x32_bf16 v[108:111], v[172:175], a[192:195], v[108:111]\n"
    "ds_read_b64_tr_b16 v[170:171], v230 offset:41472\n"
    "s_waitcnt lgkmcnt(10)\n"
    "v_mfma_f32_16x16x32_bf16 v[96:99], v[176:179], a[200:203], v[96:99]\n"
    "ds_read_b64_tr_b16 v[172:173], v231 offset:40960\n"
    "buffer_store_dwordx4 v[0:3], v240, s[44:47], s99 offen offset:2048 sc1\n"
    "v_mfma_f32_16x16x32_bf16 v[112:115], v[176:179], a[204:207], v[112:115]\n"
    "ds_read_b64_tr_b16 v[174:175], v231 offset:41472\n"
    "buffer_store_dwordx4 v[0:3], v240, s[44:47], s99 offen offset:3072 sc1\n"
    "v_mfma_f32_16x16x32_bf16 v[116:119], v[180:183], a[204:207], v[116:119]\n"
    "ds_read_b64_tr_b16 v[176:177], v228 offset:45056\n"
    "buffer_load_dwordx4 v239, s[28:31], s98 offen offset:3072 lds\n"
    "v_mfma_f32_16x16x32_bf16 v[100:103], v[180:183], a[200:203], v[100:103]\n"
    "ds_read_b64_tr_b16 v[178:179], v228 offset:45568\n"
    "s_waitcnt lgkmcnt(10)\n"
    "v_mfma_f32_16x16x32_bf16 v[104:107], v[184:187], a[200:203], v[104:107]\n"
    "ds_read_b64_tr_b16 v[180:181], v229 offset:45056\n"
    "v_add_u32 v201, 0x1c00, v250\n"
    "v_mfma_f32_16x16x32_bf16 v[120:123], v[184:187], a[204:207], v[120:123]\n"
    "ds_read_b64_tr_b16 v[182:183], v229 offset:45568\n"
    "v_xor_b32 v201, 0x80, v201\n"
    "v_mfma_f32_16x16x32_bf16 v[124:127], v[188:191], a[204:207], v[124:127]\n"
    "ds_read_b64_tr_b16 v[184:185], v230 offset:45056\n"
    "v_add_u32 v202, 0x3800, v250\n"
    "v_mfma_f32_16x16x32_bf16 v[108:111], v[188:191], a[200:203], v[108:111]\n"
    "ds_read_b64_tr_b16 v[186:187], v230 offset:45568\n"
    "s_waitcnt lgkmcnt(10)\n"
    "v_mfma_f32_16x16x32_bf16 v[96:99], v[160:163], a[208:211], v[96:99]\n"
    "ds_read_b64_tr_b16 v[188:189], v231 offset:45056\n"
    "s_add_u32 m0, s85, 0x10000\n"
    "v_mfma_f32_16x16x32_bf16 v[112:115], v[160:163], a[212:215], v[112:115]\n"
    "ds_read_b64_tr_b16 v[190:191], v231 offset:45568\n"
    "s_add_u32 s98, s67, 0x80\n"
    "v_mfma_f32_16x16x32_bf16 v[116:119], v[164:167], a[212:215], v[116:119]\n"
    "ds_read_b64_tr_b16 v[160:161], v224\n"
    "buffer_load_dwordx4 v236, s[28:31], s98 offen lds\n"
    "v_mfma_f32_16x16x32_bf16 v[100:103], v[164:167], a[208:211], v[100:103]\n"
    "ds_read_b64_tr_b16 v[162:163], v224 offset:512\n"
    "s_waitcnt lgkmcnt(10)\n"
    "v_mfma_f32_16x16x32_bf16 v[104:107], v[168:171], a[208:211], v[104:107]\n"
    "ds_read_b64_tr_b16 v[164:165], v225\n"
    "v_add_u32 v203, 0x5400, v250\n"
    "v_mfma_f32_16x16x32_bf16 v[120:123], v[168:171], a[212:215], v[120:123]\n"
    "ds_read_b64_tr_b16 v[166:167], v225 offset:512\n"
    "v_xor_b32 v203, 0x80, v203\n"
    "v_mfma_f32_16x16x32_bf16 v[124:127], v[172:175], a[212:215], v[124:127]\n"
    "ds_read_b64_tr_b16 v[168:169], v226\n"
    "s_and_b32 s89, s52, 3\n"
    "v_mfma_f32_16x16x32_bf16 v[108:111], v[172:175], a[208:211], v[108:111]\n"
    "ds_read_b64_tr_b16 v[170:171], v226 offset:512\n"
    "s_waitcnt lgkmcnt(10)\n"
    "v_mfma_f32_16x16x32_bf16 v[96:99], v[176:179], a[216:219], v[96:99]\n"
    "ds_read_b64_tr_b16 v[172:173], v227\n"
    "buffer_load_dwordx4 v237, s[28:31], s98 offen offset:1024 lds\n"
    "v_mfma_f32_16x16x32_bf16 v[112:115], v[176:179], a[220:223], v[112:115]\n"
    "ds_read_b64_tr_b16 v[174:175], v227 offset:512\n"
    "s_mul_i32 s89, s89, 8\n"
    "v_mfma_f32_16x16x32_bf16 v[116:119], v[180:183], a[220:223], v[116:119]\n"
    "ds_read_b64_tr_b16 v[176:177], v224 offset:4096\n"
    "s_mul_i32 s90, s53, 0\n"
    "v_mfma_f32_16x16x32_bf16 v[100:103], v[180:183], a[216:219], v[100:103]\n"
    "ds_read_b64_tr_b16 v[178:179], v224 offset:4608\n"
    "s_waitcnt lgkmcnt(10)\n"
    "v_mfma_f32_16x16x32_bf16 v[104:107], v[184:187], a[216:219], v[104:107]\n"
    "ds_read_b64_tr_b16 v[180:181], v225 offset:4096\n"
    "s_add_u32 s89, s89, s90\n"
    "v_mfma_f32_16x16x32_bf16 v[120:123], v[184:187], a[220:223], v[120:123]\n"
    "ds_read_b64_tr_b16 v[182:183], v225 offset:4608\n"
    "s_and_b32 s89, s89, 28\n"
    "v_mfma_f32_16x16x32_bf16 v[124:127], v[188:191], a[220:223], v[124:127]\n"
    "ds_read_b64_tr_b16 v[184:185], v226 offset:4096\n"
    "buffer_load_dwordx4 v238, s[28:31], s98 offen offset:2048 lds\n"
    "v_mfma_f32_16x16x32_bf16 v[108:111], v[188:191], a[216:219], v[108:111]\n"
    "ds_read_b64_tr_b16 v[186:187], v226 offset:4608\n"
    "s_waitcnt lgkmcnt(10)\n"
    "v_mfma_f32_16x16x32_bf16 v[96:99], v[160:163], a[224:227], v[96:99]\n"
    "ds_read_b64_tr_b16 v[188:189], v227 offset:4096\n"
    "s_mov_b32 s90, 0xb841a730\n"
    "v_mfma_f32_16x16x32_bf16 v[112:115], v[160:163], a[228:231], v[112:115]\n"
    "ds_read_b64_tr_b16 v[190:191], v227 offset:4608\n"
    "s_mov_b32 s91, 0xb841a730\n"
    "v_mfma_f32_16x16x32_bf16 v[116:119], v[164:167], a[228:231], v[116:119]\n"
    "ds_read_b64_tr_b16 v[160:161], v224 offset:8192\n"
    "s_lshr_b64 s[90:91], s[90:91], s89\n"
    "v_mfma_f32_16x16x32_bf16 v[100:103], v[164:167], a[224:227], v[100:103]\n"
    "ds_read_b64_tr_b16 v[162:163], v224 offset:8704\n"
    "s_waitcnt lgkmcnt(10)\n"
    "v_mfma_f32_16x16x32_bf16 v[104:107], v[168:171], a[224:227], v[104:107]\n"
    "ds_read_b64_tr_b16 v[164:165], v225 offset:8192\n"
    "buffer_load_dwordx4 v239, s[28:31], s98 offen offset:3072 lds\n"
    "v_mfma_f32_16x16x32_bf16 v[120:123], v[168:171], a[228:231], v[120:123]\n"
    "ds_read_b64_tr_b16 v[166:167], v225 offset:8704\n"
    "s_bfe_u32 s92, s90, 0x40000\n"
    "v_mfma_f32_16x16x32_bf16 v[124:127], v[172:175], a[228:231], v[124:127]\n"
    "ds_read_b64_tr_b16 v[168:169], v226 offset:8192\n"
    "s_lshl_b32 s56, s92, 13\n"
    "v_mfma_f32_16x16x32_bf16 v[108:111], v[172:175], a[224:227], v[108:111]\n"
    "ds_read_b64_tr_b16 v[170:171], v226 offset:8704\n"
    "s_waitcnt lgkmcnt(10)\n"
    "v_mfma_f32_16x16x32_bf16 v[96:99], v[176:179], a[232:235], v[96:99]\n"
    "ds_read_b64_tr_b16 v[172:173], v227 offset:8192\n"
    "s_bfe_u32 s92, s90, 0x40004\n"
    "v_mfma_f32_16x16x32_bf16 v[112:115], v[176:179], a[236:239], v[112:115]\n"
    "ds_read_b64_tr_b16 v[174:175], v227 offset:8704\n"
    "s_lshl_b32 s57, s92, 13\n"
    "v_mfma_f32_16x16x32_bf16 v[116:119], v[180:183], a[236:239], v[116:119]\n"
    "ds_read_b64_tr_b16 v[176:177], v224 offset:12288\n"
    "s_bfe_u32 s92, s90, 0x40008\n"
    "v_mfma_f32_16x16x32_bf16 v[100:103], v[180:183], a[232:235], v[100:103]\n"
    "ds_read_b64_tr_b16 v[178:179], v224 offset:12800\n"
    "s_waitcnt lgkmcnt(10)\n"
    "v_mfma_f32_16x16x32_bf16 v[104:107], v[184:187], a[232:235], v[104:107]\n"
    "ds_read_b64_tr_b16 v[180:181], v225 offset:12288\n"
    "s_lshl_b32 s58, s92, 13\n"
    "v_mfma_f32_16x16x32_bf16 v[120:123], v[184:187], a[236:239], v[120:123]\n"
    "ds_read_b64_tr_b16 v[182:183], v225 offset:12800\n"
    "s_bfe_u32 s92, s90, 0x4000c\n"
    "v_mfma_f32_16x16x32_bf16 v[124:127], v[188:191], a[236:239], v[124:127]\n"
    "ds_read_b64_tr_b16 v[184:185], v226 offset:12288\n"
    "s_lshl_b32 s59, s92, 13\n"
    "v_mfma_f32_16x16x32_bf16 v[108:111], v[188:191], a[232:235], v[108:111]\n"
    "ds_read_b64_tr_b16 v[186:187], v226 offset:12800\n"
    "s_waitcnt lgkmcnt(10)\n"
    "v_mfma_f32_16x16x32_bf16 v[96:99], v[160:163], a[240:243], v[96:99]\n"
    "ds_read_b64_tr_b16 v[188:189], v227 offset:12288\n"
    "s_waitcnt vmcnt(10)\n"
    "v_mfma_f32_16x16x32_bf16 v[112:115], v[160:163], a[244:247], v[112:115]\n"
    "ds_read_b64_tr_b16 v[190:191], v227 offset:12800\n"
    "s_barrier\n"
    "v_mfma_f32_16x16x32_bf16 v[116:119], v[164:167], a[244:247], v[116:119]\n"
    "ds_read_b64_tr_b16 v[160:161], v224 offset:16384\n"
    "buffer_atomic_add v248, v247, s[44:47], 0 offen\n"
    "v_mfma_f32_16x16x32_bf16 v[100:103], v[164:167], a[240:243], v[100:103]\n"
    "ds_read_b64_tr_b16 v[162:163], v224 offset:16896\n"
    "s_waitcnt lgkmcnt(10)\n"
    "v_mfma_f32_16x16x32_bf16 v[104:107], v[168:171], a[240:243], v[104:107]\n"
    "ds_read_b64_tr_b16 v[164:165], v225 offset:16384\n"
    "s_add_u32 m0, s85, 0x14000\n"
    "v_mfma_f32_16x16x32_bf16 v[120:123], v[168:171], a[244:247], v[120:123]\n"
    "ds_read_b64_tr_b16 v[166:167], v225 offset:16896\n"
    "s_add_u32 s98, s68, 0x80\n"
    "v_mfma_f32_16x16x32_bf16 v[124:127], v[172:175], a[244:247], v[124:127]\n"
    "ds_read_b64_tr_b16 v[168:169], v226 offset:16384\n"
    "buffer_load_dwordx4 v236, s[28:31], s98 offen lds\n"
    "v_mfma_f32_16x16x32_bf16 v[108:111], v[172:175], a[240:243], v[108:111]\n"
    "ds_read_b64_tr_b16 v[170:171], v226 offset:16896\n"
    "s_waitcnt lgkmcnt(10)\n"
    "v_mfma_f32_16x16x32_bf16 v[96:99], v[176:179], a[248:251], v[96:99]\n"
    "ds_read_b64_tr_b16 v[172:173], v227 offset:16384\n"
    "s_bfe_u32 s92, s90, 0x40010\n"
    "v_mfma_f32_16x16x32_bf16 v[112:115], v[176:179], a[252:255], v[112:115]\n"
    "ds_read_b64_tr_b16 v[174:175], v227 offset:16896\n"
    "s_lshl_b32 s60, s92, 13\n"
    "v_mfma_f32_16x16x32_bf16 v[116:119], v[180:183], a[252:255], v[116:119]\n"
    "ds_read_b64_tr_b16 v[176:177], v224 offset:20480\n"
    "buffer_load_dwordx4 v237, s[28:31], s98 offen offset:1024 lds\n"
    "v_mfma_f32_16x16x32_bf16 v[100:103], v[180:183], a[248:251], v[100:103]\n"
    "ds_read_b64_tr_b16 v[178:179], v224 offset:20992\n"
    "s_waitcnt lgkmcnt(10)\n"
    "v_mfma_f32_16x16x32_bf16 v[104:107], v[184:187], a[248:251], v[104:107]\n"
    "ds_read_b64_tr_b16 v[180:181], v225 offset:20480\n"
    "s_bfe_u32 s92, s90, 0x40014\n"
    "v_mfma_f32_16x16x32_bf16 v[120:123], v[184:187], a[252:255], v[120:123]\n"
    "ds_read_b64_tr_b16 v[182:183], v225 offset:20992\n"
    "s_lshl_b32 s61, s92, 13\n"
    "v_mfma_f32_16x16x32_bf16 v[124:127], v[188:191], a[252:255], v[124:127]\n"
    "ds_read_b64_tr_b16 v[184:185], v226 offset:20480\n"
    "s_bfe_u32 s92, s90, 0x40018\n"
    "v_mfma_f32_16x16x32_bf16 v[108:111], v[188:191], a[248:251], v[108:111]\n"
    "ds_read_b64_tr_b16 v[186:187], v226 offset:20992\n"
    "s_waitcnt lgkmcnt(10)\n"
    "v_mfma_f32_16x16x32_bf16 v[64:67], v[160:163], a[0:3], 0\n"
    "ds_read_b64_tr_b16 v[188:189], v227 offset:20480\n"
    "s_lshl_b32 s62, s92, 13\n"
    "v_mfma_f32_16x16x32_bf16 v[80:83], v[160:163], a[4:7], 0\n"
    "ds_read_b64_tr_b16 v[190:191], v227 offset:20992\n"
    "s_bfe_u32 s92, s90, 0x4001c\n"
    "v_mfma_f32_16x16x32_bf16 v[84:87], v[164:167], a[4:7], 0\n"
    "ds_read_b64_tr_b16 v[160:161], v224 offset:24576\n"
    "buffer_load_dwordx4 v238, s[28:31], s98 offen offset:2048 lds\n"
    "v_mfma_f32_16x16x32_bf16 v[68:71], v[164:167], a[0:3], 0\n"
    "ds_read_b64_tr_b16 v[162:163], v224 offset:25088\n"
    "s_waitcnt lgkmcnt(10)\n"
    "v_mfma_f32_16x16x32_bf16 v[72:75], v[168:171], a[0:3], 0\n"
    "ds_read_b64_tr_b16 v[164:165], v225 offset:24576\n"
    "s_lshl_b32 s63, s92, 13\n"
    "v_mfma_f32_16x16x32_bf16 v[88:91], v[168:171], a[4:7], 0\n"
    "ds_read_b64_tr_b16 v[166:167], v225 offset:25088\n"
    "v_cvt_pk_bf16_f32 v128, v96, v97\n"
    "v_mfma_f32_16x16x32_bf16 v[92:95], v[172:175], a[4:7], 0\n"
    "ds_read_b64_tr_b16 v[168:169], v226 offset:24576\n"
    "v_cvt_pk_bf16_f32 v129, v98, v99\n"
    "v_mfma_f32_16x16x32_bf16 v[76:79], v[172:175], a[0:3], 0\n"
    "ds_read_b64_tr_b16 v[170:171], v226 offset:25088\n"
    "s_waitcnt lgkmcnt(10)\n"
    "v_mfma_f32_16x16x32_bf16 v[64:67], v[176:179], a[8:11], v[64:67]\n"
    "ds_read_b64_tr_b16 v[172:173], v227 offset:24576\n"
    "v_cvt_pk_bf16_f32 v130, v100, v101\n"
    "v_mfma_f32_16x16x32_bf16 v[80:83], v[176:179], a[12:15], v[80:83]\n"
    "ds_read_b64_tr_b16 v[174:175], v227 offset:25088\n"
    "v_cvt_pk_bf16_f32 v131, v102, v103\n"
    "v_mfma_f32_16x16x32_bf16 v[84:87], v[180:183], a[12:15], v[84:87]\n"
    "ds_read_b64_tr_b16 v[176:177], v224 offset:28672\n"
    "buffer_load_dwordx4 v239, s[28:31], s98 offen offset:3072 lds\n"
    "v_mfma_f32_16x16x32_bf16 v[68:71], v[180:183], a[8:11], v[68:71]\n"
    "ds_read_b64_tr_b16 v[178:179], v224 offset:29184\n"
    "s_waitcnt lgkmcnt(10)\n"
    "v_mfma_f32_16x16x32_bf16 v[72:75], v[184:187], a[8:11], v[72:75]\n"
    "ds_read_b64_tr_b16 v[180:181], v225 offset:28672\n"
    "v_pk_max_i16 v128, v128, 0\n"
    "v_mfma_f32_16x16x32_bf16 v[88:91], v[184:187], a[12:15], v[88:91]\n"
    "ds_read_b64_tr_b16 v[182:183], v225 offset:29184\n"
    "v_pk_max_i16 v129, v129, 0\n"
    "v_mfma_f32_16x16x32_bf16 v[92:95], v[188:191], a[12:15], v[92:95]\n"
    "ds_read_b64_tr_b16 v[184:185], v226 offset:28672\n"
    "v_pk_max_i16 v130, v130, 0\n"
    "v_mfma_f32_16x16x32_bf16 v[76:79], v[188:191], a[8:11], v[76:79]\n"
    "ds_read_b64_tr_b16 v[186:187], v226 offset:29184\n"
    "s_waitcnt lgkmcnt(10)\n"
    "v_mfma_f32_16x16x32_bf16 v[64:67], v[160:163], a[16:19], v[64:67]\n"
    "ds_read_b64_tr_b16 v[188:189], v227 offset:28672\n"
    "s_add_u32 m0, s85, 0x18000\n"
    "v_mfma_f32_16x16x32_bf16 v[80:83], v[160:163], a[20:23], v[80:83]\n"
    "ds_read_b64_tr_b16 v[190:191], v227 offset:29184\n"
    "s_add_u32 s98, s69, 0x80\n"
    "v_mfma_f32_16x16x32_bf16 v[84:87], v[164:167], a[20:23], v[84:87]\n"
    "ds_read_b64_tr_b16 v[160:161], v224 offset:32768\n"
    "buffer_load_dwordx4 v236, s[28:31], s98 offen lds\n"
    "v_mfma_f32_16x16x32_bf16 v[68:71], v[164:167], a[16:19], v[68:71]\n"
    "ds_read_b64_tr_b16 v[162:163], v224 offset:33280\n"
    "s_waitcnt lgkmcnt(10)\n"
    "v_mfma_f32_16x16x32_bf16 v[72:75], v[168:171], a[16:19], v[72:75]\n"
    "ds_read_b64_tr_b16 v[164:165], v225 offset:32768\n"
    "v_pk_max_i16 v131, v131, 0\n"
    "v_mfma_f32_16x16x32_bf16 v[88:91], v[168:171], a[20:23], v[88:91]\n"
    "ds_read_b64_tr_b16 v[166:167], v225 offset:33280\n"
    "v_cvt_pk_bf16_f32 v132, v112, v113\n"
    "v_mfma_f32_16x16x32_bf16 v[92:95], v[172:175], a[20:23], v[92:95]\n"
    "ds_read_b64_tr_b16 v[168:169], v226 offset:32768\n"
    "v_cvt_pk_bf16_f32 v133, v114, v115\n"
    "v_mfma_f32_16x16x32_bf16 v[76:79], v[172:175], a[16:19], v[76:79]\n"
    "ds_read_b64_tr_b16 v[170:171], v226 offset:33280\n"
    "s_waitcnt lgkmcnt(10)\n"
    "v_mfma_f32_16x16x32_bf16 v[64:67], v[176:179], a[24:27], v[64:67]\n"
    "ds_read_b64_tr_b16 v[172:173], v227 offset:32768\n"
    "buffer_load_dwordx4 v237, s[28:31], s98 offen offset:1024 lds\n"
    "v_mfma_f32_16x16x32_bf16 v[80:83], v[176:179], a[28:31], v[80:83]\n"
    "ds_read_b64_tr_b16 v[174:175], v227 offset:33280\n"
    "s_add_u32 s99, s16, 0\n"
    "v_mfma_f32_16x16x32_bf16 v[84:87], v[180:183], a[28:31], v[84:87]\n"
    "ds_read_b64_tr_b16 v[176:177], v224 offset:36864\n"
    "buffer_store_dwordx4 v[128:131], v240, s[44:47], s99 offen sc1\n"
    "v_mfma_f32_16x16x32_bf16 v[68:71], v[180:183], a[24:27], v[68:71]\n"
    "ds_read_b64_tr_b16 v[178:179], v224 offset:37376\n"
    "s_waitcnt lgkmcnt(10)\n"
    "v_mfma_f32_16x16x32_bf16 v[72:75], v[184:187], a[24:27], v[72:75]\n"
    "ds_read_b64_tr_b16 v[180:181], v225 offset:36864\n"
    "v_cvt_pk_bf16_f32 v134, v116, v117\n"
    "v_mfma_f32_16x16x32_bf16 v[88:91], v[184:187], a[28:31], v[88:91]\n"
    "ds_read_b64_tr_b16 v[182:183], v225 offset:37376\n"
    "v_cvt_pk_bf16_f32 v135, v118, v119\n"
    "v_mfma_f32_16x16x32_bf16 v[92:95], v[188:191], a[28:31], v[92:95]\n"
    "ds_read_b64_tr_b16 v[184:185], v226 offset:36864\n"
    "buffer_load_dwordx4 v238, s[28:31], s98 offen offset:2048 lds\n"
    "v_mfma_f32_16x16x32_bf16 v[76:79], v[188:191], a[24:27], v[76:79]\n"
    "ds_read_b64_tr_b16 v[186:187], v226 offset:37376\n"
    "s_waitcnt lgkmcnt(10)\n"
    "v_mfma_f32_16x16x32_bf16 v[64:67], v[160:163], a[32:35], v[64:67]\n"
    "ds_read_b64_tr_b16 v[188:189], v227 offset:36864\n"
    "s_getpc_b64 s[92:93]\n"
    ".Lct4:\n"
    "v_mfma_f32_16x16x32_bf16 v[80:83], v[160:163], a[36:39], v[80:83]\n"
    "ds_read_b64_tr_b16 v[190:191], v227 offset:37376\n"
    "s_and_b32 s93, s93, 0xffff\n"
    "v_mfma_f32_16x16x32_bf16 v[84:87], v[164:167], a[36:39], v[84:87]\n"
    "ds_read_b64_tr_b16 v[160:161], v224 offset:40960\n"
    "s_mov_b32 s94, .Lend-.Lct4\n"
    "v_mfma_f32_16x16x32_bf16 v[68:71], v[164:167], a[32:35], v[68:71]\n"
    "ds_read_b64_tr_b16 v[162:163], v224 offset:41472\n"
    "s_waitcnt lgkmcnt(10)\n"
    "v_mfma_f32_16x16x32_bf16 v[72:75], v[168:171], a[32:35], v[72:75]\n"
    "ds_read_b64_tr_b16 v[164:165], v225 offset:40960\n"
    "buffer_load_dwordx4 v239, s[28:31], s98 offen offset:3072 lds\n"
    "v_mfma_f32_16x16x32_bf16 v[88:91], v[168:171], a[36:39], v[88:91]\n"
    "ds_read_b64_tr_b16 v[166:167], v225 offset:41472\n"
    "s_min_u32 s94, s94, 0x3000\n"
    "v_mfma_f32_16x16x32_bf16 v[92:95], v[172:175], a[36:39], v[92:95]\n"
    "ds_read_b64_tr_b16 v[168:169], v226 offset:40960\n"
    "s_mov_b32 s95, 0x20000\n"
    "v_mfma_f32_16x16x32_bf16 v[76:79], v[172:175], a[32:35], v[76:79]\n"
    "ds_read_b64_tr_b16 v[170:171], v226 offset:41472\n"
    "s_waitcnt lgkmcnt(10)\n"
    "v_mfma_f32_16x16x32_bf16 v[64:67], v[176:179], a[40:43], v[64:67]\n"
    "ds_read_b64_tr_b16 v[172:173], v227 offset:40960\n"
    "s_and_b32 s91, s13, 0x7f\n"
    "v_mfma_f32_16x16x32_bf16 v[80:83], v[176:179], a[44:47], v[80:83]\n"
    "ds_read_b64_tr_b16 v[174:175], v227 offset:41472\n"
    "s_lshl_b32 s91, s91, 10\n"
    "v_mfma_f32_16x16x32_bf16 v[84:87], v[180:183], a[44:47], v[84:87]\n"
    "ds_read_b64_tr_b16 v[176:177], v224 offset:45056\n"
    "buffer_load_dword v206, v240, s[92:95], s91 offen\n"
    "v_mfma_f32_16x16x32_bf16 v[68:71], v[180:183], a[40:43], v[68:71]\n"
    "ds_read_b64_tr_b16 v[178:179], v224 offset:45568\n"
    "s_waitcnt lgkmcnt(10)\n"
    "v_mfma_f32_16x16x32_bf16 v[72:75], v[184:187], a[40:43], v[72:75]\n"
    "ds_read_b64_tr_b16 v[180:181], v225 offset:45056\n"
    "v_pk_max_i16 v132, v132, 0\n"
    "v_mfma_f32_16x16x32_bf16 v[88:91], v[184:187], a[44:47], v[88:91]\n"
    "ds_read_b64_tr_b16 v[182:183], v225 offset:45568\n"
    "v_pk_max_i16 v133, v133, 0\n"
    "v_mfma_f32_16x16x32_bf16 v[92:95], v[188:191], a[44:47], v[92:95]\n"
    "ds_read_b64_tr_b16 v[184:185], v226 offset:45056\n"
    "v_pk_max_i16 v134, v134, 0\n"
    "v_mfma_f32_16x16x32_bf16 v[76:79], v[188:191], a[40:43], v[76:79]\n"
    "ds_read_b64_tr_b16 v[186:187], v226 offset:45568\n"
    "s_waitcnt lgkmcnt(10)\n"
    "v_mfma_f32_16x16x32_bf16 v[64:67], v[160:163], a[48:51], v[64:67]\n"
    "ds_read_b64_tr_b16 v[188:189], v227 offset:45056\n"
    "s_waitcnt vmcnt(11)\n"
    "v_mfma_f32_16x16x32_bf16 v[80:83], v[160:163], a[52:55], v[80:83]\n"
    "ds_read_b64_tr_b16 v[190:191], v227 offset:45568\n"
    "s_barrier\n"
    "v_mfma_f32_16x16x32_bf16 v[84:87], v[164:167], a[52:55], v[84:87]\n"
    "ds_read_b64_tr_b16 v[160:161], v224 offset:49152\n"
    "s_add_u32 m0, s85, 0\n"
    "v_mfma_f32_16x16x32_bf16 v[68:71], v[164:167], a[48:51], v[68:71]\n"
    "ds_read_b64_tr_b16 v[162:163], v224 offset:49664\n"
    "s_waitcnt lgkmcnt(10)\n"
    "v_mfma_f32_16x16x32_bf16 v[72:75], v[168:171], a[48:51], v[72:75]\n"
    "ds_read_b64_tr_b16 v[164:165], v225 offset:49152\n"
    "s_add_u32 s98, s70, 0x80\n"
    "v_mfma_f32_16x16x32_bf16 v[88:91], v[168:171], a[52:55], v[88:91]\n"
    "ds_read_b64_tr_b16 v[166:167], v225 offset:49664\n"
    "buffer_load_dwordx4 v236, s[28:31], s98 offen lds\n"
    "v_mfma_f32_16x16x32_bf16 v[92:95], v[172:175], a[52:55], v[92:95]\n"
    "ds_read_b64_tr_b16 v[168:169], v226 offset:49152\n"
    "v_pk_max_i16 v135, v135, 0\n"
    "v_mfma_f32_16x16x32_bf16 v[76:79], v[172:175], a[48:51], v[76:79]\n"
    "ds_read_b64_tr_b16 v[170:171], v226 offset:49664\n"
    "s_waitcnt lgkmcnt(10)\n"
    "v_mfma_f32_16x16x32_bf16 v[64:67], v[176:179], a[56:59], v[64:67]\n"
    "ds_read_b64_tr_b16 v[172:173], v227 offset:49152\n"
    "v_cvt_pk_bf16_f32 v136, v104, v105\n"
    "v_mfma_f32_16x16x32_bf16 v[80:83], v[176:179], a[60:63], v[80:83]\n"
    "ds_read_b64_tr_b16 v[174:175], v227 offset:49664\n"
    "v_cvt_pk_bf16_f32 v137, v106, v107\n"
    "v_mfma_f32_16x16x32_bf16 v[84:87], v[180:183], a[60:63], v[84:87]\n"
    "ds_read_b64_tr_b16 v[176:177], v224 offset:53248\n"
    "buffer_load_dwordx4 v237, s[28:31], s98 offen offset:1024 lds\n"
    "v_mfma_f32_16x16x32_bf16 v[68:71], v[180:183], a[56:59], v[68:71]\n"
    "ds_read_b64_tr_b16 v[178:179], v224 offset:53760\n"
    "s_waitcnt lgkmcnt(10)\n"
    "v_mfma_f32_16x16x32_bf16 v[72:75], v[184:187], a[56:59], v[72:75]\n"
    "ds_read_b64_tr_b16 v[180:181], v225 offset:53248\n"
    "v_cvt_pk_bf16_f32 v138, v108, v109\n"
    "v_mfma_f32_16x16x32_bf16 v[88:91], v[184:187], a[60:63], v[88:91]\n"
    "ds_read_b64_tr_b16 v[182:183], v225 offset:53760\n"
    "v_cvt_pk_bf16_f32 v139, v110, v111\n"
    "v_mfma_f32_16x16x32_bf16 v[92:95], v[188:191], a[60:63], v[92:95]\n"
    "ds_read_b64_tr_b16 v[184:185], v226 offset:53248\n"
    "v_pk_max_i16 v136, v136, 0\n"
    "v_mfma_f32_16x16x32_bf16 v[76:79], v[188:191], a[56:59], v[76:79]\n"
    "ds_read_b64_tr_b16 v[186:187], v226 offset:53760\n"
    "s_waitcnt lgkmcnt(10)\n"
    "v_mfma_f32_16x16x32_bf16 v[64:67], v[160:163], a[64:67], v[64:67]\n"
    "ds_read_b64_tr_b16 v[188:189], v227 offset:53248\n"
    "v_pk_max_i16 v137, v137, 0\n"
    "v_mfma_f32_16x16x32_bf16 v[80:83], v[160:163], a[68:71], v[80:83]\n"
    "ds_read_b64_tr_b16 v[190:191], v227 offset:53760\n"
    "v_pk_max_i16 v138, v138, 0\n"
    "v_mfma_f32_16x16x32_bf16 v[84:87], v[164:167], a[68:71], v[84:87]\n"
    "ds_read_b64_tr_b16 v[160:161], v224 offset:57344\n"
    "buffer_load_dwordx4 v238, s[28:31], s98 offen offset:2048 lds\n"
    "v_mfma_f32_16x16x32_bf16 v[68:71], v[164:167], a[64:67], v[68:71]\n"
    "ds_read_b64_tr_b16 v[162:163], v224 offset:57856\n"
    "s_waitcnt lgkmcnt(10)\n"
    "v_mfma_f32_16x16x32_bf16 v[72:75], v[168:171], a[64:67], v[72:75]\n"
    "ds_read_b64_tr_b16 v[164:165], v225 offset:57344\n"
    "v_pk_max_i16 v139, v139, 0\n"
    "v_mfma_f32_16x16x32_bf16 v[88:91], v[168:171], a[68:71], v[88:91]\n"
    "ds_read_b64_tr_b16 v[166:167], v225 offset:57856\n"
    "v_cvt_pk_bf16_f32 v140, v120, v121\n"
    "v_mfma_f32_16x16x32_bf16 v[92:95], v[172:175], a[68:71], v[92:95]\n"
    "ds_read_b64_tr_b16 v[168:169], v226 offset:57344\n"
    "v_cvt_pk_bf16_f32 v141, v122, v123\n"
    "v_mfma_f32_16x16x32_bf16 v[76:79], v[172:175], a[64:67], v[76:79]\n"
    "ds_read_b64_tr_b16 v[170:171], v226 offset:57856\n"
    "s_waitcnt lgkmcnt(10)\n"
    "v_mfma_f32_16x16x32_bf16 v[64:67], v[176:179], a[72:75], v[64:67]\n"
    "ds_read_b64_tr_b16 v[172:173], v227 offset:57344\n"
    "v_cvt_pk_bf16_f32 v142, v124, v125\n"
    "v_mfma_f32_16x16x32_bf16 v[80:83], v[176:179], a[76:79], v[80:83]\n"
    "ds_read_b64_tr_b16 v[174:175], v227 offset:57856\n"
    "v_cvt_pk_bf16_f32 v143, v126, v127\n"
    "v_mfma_f32_16x16x32_bf16 v[84:87], v[180:183], a[76:79], v[84:87]\n"
    "ds_read_b64_tr_b16 v[176:177], v224 offset:61440\n"
    "buffer_load_dwordx4 v239, s[28:31], s98 offen offset:3072 lds\n"
    "v_mfma_f32_16x16x32_bf16 v[68:71], v[180:183], a[72:75], v[68:71]\n"
    "ds_read_b64_tr_b16 v[178:179], v224 offset:61952\n"
    "s_waitcnt lgkmcnt(10)\n"
    "v_mfma_f32_16x16x32_bf16 v[72:75], v[184:187], a[72:75], v[72:75]\n"
    "ds_read_b64_tr_b16 v[180:181], v225 offset:61440\n"
    "v_pk_max_i16 v140, v140, 0\n"
    "v_mfma_f32_16x16x32_bf16 v[88:91], v[184:187], a[76:79], v[88:91]\n"
    "ds_read_b64_tr_b16 v[182:183], v225 offset:61952\n"
    "v_pk_max_i16 v141, v141, 0\n"
    "v_mfma_f32_16x16x32_bf16 v[92:95], v[188:191], a[76:79], v[92:95]\n"
    "ds_read_b64_tr_b16 v[184:185], v226 offset:61440\n"
    "v_pk_max_i16 v142, v142, 0\n"
    "v_mfma_f32_16x16x32_bf16 v[76:79], v[188:191], a[72:75], v[76:79]\n"
    "ds_read_b64_tr_b16 v[186:187], v226 offset:61952\n"
    "s_waitcnt lgkmcnt(10)\n"
    "v_mfma_f32_16x16x32_bf16 v[64:67], v[160:163], a[80:83], v[64:67]\n"
    "ds_read_b64_tr_b16 v[188:189], v227 offset:61440\n"
    "s_add_u32 m0, s85, 0x4000\n"
    "v_mfma_f32_16x16x32_bf16 v[80:83], v[160:163], a[84:87], v[80:83]\n"
    "ds_read_b64_tr_b16 v[190:191], v227 offset:61952\n"
    "s_add_u32 s98, s71, 0x80\n"
    "v_mfma_f32_16x16x32_bf16 v[84:87], v[164:167], a[84:87], v[84:87]\n"
    "ds_read_b64_tr_b16 v[160:161], v228\n"
    "buffer_load_dwordx4 v236, s[28:31], s98 offen lds\n"
    "v_mfma_f32_16x16x32_bf16 v[68:71], v[164:167], a[80:83], v[68:71]\n"
    "ds_read_b64_tr_b16 v[162:163], v228 offset:512\n"
    "s_waitcnt lgkmcnt(10)\n"
    "v_mfma_f32_16x16x32_bf16 v[72:75], v[168:171], a[80:83], v[72:75]\n"
    "ds_read_b64_tr_b16 v[164:165], v229\n"
    "v_pk_max_i16 v143, v143, 0\n"
    "v_mfma_f32_16x16x32_bf16 v[88:91], v[168:171], a[84:87], v[88:91]\n"
    "ds_read_b64_tr_b16 v[166:167], v229 offset:512\n"
    "v_mfma_f32_16x16x32_bf16 v[92:95], v[172:175], a[84:87], v[92:95]\n"
    "ds_read_b64_tr_b16 v[168:169], v230\n"
    "v_mfma_f32_16x16x32_bf16 v[76:79], v[172:175], a[80:83], v[76:79]\n"
    "ds_read_b64_tr_b16 v[170:171], v230 offset:512\n"
    "s_waitcnt lgkmcnt(10)\n"
    "v_mfma_f32_16x16x32_bf16 v[64:67], v[176:179], a[88:91], v[64:67]\n"
    "ds_read_b64_tr_b16 v[172:173], v231\n"
    "buffer_load_dwordx4 v237, s[28:31], s98 offen offset:1024 lds\n"
    "v_mfma_f32_16x16x32_bf16 v[80:83], v[176:179], a[92:95], v[80:83]\n"
    "ds_read_b64_tr_b16 v[174:175], v231 offset:512\n"
    "buffer_store_dwordx4 v[132:135], v240, s[44:47], s99 offen offset:1024 sc1\n"
    "v_mfma_f32_16x16x32_bf16 v[84:87], v[180:183], a[92:95], v[84:87]\n"
    "ds_read_b64_tr_b16 v[176:177], v228 offset:4096\n"
    "v_mfma_f32_16x16x32_bf16 v[68:71], v[180:183], a[88:91], v[68:71]\n"
    "ds_read_b64_tr_b16 v[178:179], v228 offset:4608\n"
    "s_waitcnt lgkmcnt(10)\n"
    "v_mfma_f32_16x16x32_bf16 v[72:75], v[184:187], a[88:91], v[72:75]\n"
    "ds_read_b64_tr_b16 v[180:181], v229 offset:4096\n"
    "v_mfma_f32_16x16x32_bf16 v[88:91], v[184:187], a[92:95], v[88:91]\n"
    "ds_read_b64_tr_b16 v[182:183], v229 offset:4608\n"
    "v_mfma_f32_16x16x32_bf16 v[92:95], v[188:191], a[92:95], v[92:95]\n"
    "ds_read_b64_tr_b16 v[184:185], v230 offset:4096\n"
    "buffer_load_dwordx4 v238, s[28:31], s98 offen offset:2048 lds\n"
    "v_mfma_f32_16x16x32_bf16 v[76:79], v[188:191], a[88:91], v[76:79]\n"
    "ds_read_b64_tr_b16 v[186:187], v230 offset:4608\n"
    "s_waitcnt lgkmcnt(10)\n"
    "v_mfma_f32_16x16x32_bf16 v[64:67], v[160:163], a[96:99], v[64:67]\n"
    "ds_read_b64_tr_b16 v[188:189], v231 offset:4096\n"
    "buffer_load_dword v246, v245, s[44:47], 0 offen sc1\n"
    "v_mfma_f32_16x16x32_bf16 v[80:83], v[160:163], a[100:103], v[80:83]\n"
    "ds_read_b64_tr_b16 v[190:191], v231 offset:4608\n"
    "v_mfma_f32_16x16x32_bf16 v[84:87], v[164:167], a[100:103], v[84:87]\n"
    "ds_read_b64_tr_b16 v[160:161], v228 offset:8192\n"
    "v_mfma_f32_16x16x32_bf16 v[68:71], v[164:167], a[96:99], v[68:71]\n"
    "ds_read_b64_tr_b16 v[162:163], v228 offset:8704\n"
    "s_waitcnt lgkmcnt(10)\n"
    "v_mfma_f32_16x16x32_bf16 v[72:75], v[168:171], a[96:99], v[72:75]\n"
    "ds_read_b64_tr_b16 v[164:165], v229 offset:8192\n"
    "buffer_load_dwordx4 v239, s[28:31], s98 offen offset:3072 lds\n"
    "v_mfma_f32_16x16x32_bf16 v[88:91], v[168:171], a[100:103], v[88:91]\n"
    "ds_read_b64_tr_b16 v[166:167], v229 offset:8704\n"
    "v_mfma_f32_16x16x32_bf16 v[92:95], v[172:175], a[100:103], v[92:95]\n"
    "ds_read_b64_tr_b16 v[168:169], v230 offset:8192\n"
    "v_mfma_f32_16x16x32_bf16 v[76:79], v[172:175], a[96:99], v[76:79]\n"
    "ds_read_b64_tr_b16 v[170:171], v230 offset:8704\n"
    "s_waitcnt lgkmcnt(10)\n"
    "v_mfma_f32_16x16x32_bf16 v[64:67], v[176:179], a[104:107], v[64:67]\n"
    "ds_read_b64_tr_b16 v[172:173], v231 offset:8192\n"
    "v_mfma_f32_16x16x32_bf16 v[80:83], v[176:179], a[108:111], v[80:83]\n"
    "ds_read_b64_tr_b16 v[174:175], v231 offset:8704\n"
    "v_mfma_f32_16x16x32_bf16 v[84:87], v[180:183], a[108:111], v[84:87]\n"
    "ds_read_b64_tr_b16 v[176:177], v228 offset:12288\n"
    "v_mfma_f32_16x16x32_bf16 v[68:71], v[180:183], a[104:107], v[68:71]\n"
    "ds_read_b64_tr_b16 v[178:179], v228 offset:12800\n"
    "s_waitcnt lgkmcnt(10)\n"
    "v_mfma_f32_16x16x32_bf16 v[72:75], v[184:187], a[104:107], v[72:75]\n"
    "ds_read_b64_tr_b16 v[180:181], v229 offset:12288\n"
    "v_mfma_f32_16x16x32_bf16 v[88:91], v[184:187], a[108:111], v[88:91]\n"
    "ds_read_b64_tr_b16 v[182:183], v229 offset:12800\n"
    "v_mfma_f32_16x16x32_bf16 v[92:95], v[188:191], a[108:111], v[92:95]\n"
    "ds_read_b64_tr_b16 v[184:185], v230 offset:12288\n"
    "v_mfma_f32_16x16x32_bf16 v[76:79], v[188:191], a[104:107], v[76:79]\n"
    "ds_read_b64_tr_b16 v[186:187], v230 offset:12800\n"
    "s_waitcnt lgkmcnt(10)\n"
    "v_mfma_f32_16x16x32_bf16 v[64:67], v[160:163], a[112:115], v[64:67]\n"
    "ds_read_b64_tr_b16 v[188:189], v231 offset:12288\n"
    "s_waitcnt vmcnt(11)\n"
    "v_mfma_f32_16x16x32_bf16 v[80:83], v[160:163], a[116:119], v[80:83]\n"
    "ds_read_b64_tr_b16 v[190:191], v231 offset:12800\n"
    "s_barrier\n"
    "v_mfma_f32_16x16x32_bf16 v[84:87], v[164:167], a[116:119], v[84:87]\n"
    "ds_read_b64_tr_b16 v[160:161], v228 offset:16384\n"
    "s_add_u32 m0, s85, 0x8000\n"
    "v_mfma_f32_16x16x32_bf16 v[68:71], v[164:167], a[112:115], v[68:71]\n"
    "ds_read_b64_tr_b16 v[162:163], v228 offset:16896\n"
    "s_waitcnt lgkmcnt(10)\n"
    "v_mfma_f32_16x16x32_bf16 v[72:75], v[168:171], a[112:115], v[72:75]\n"
    "ds_read_b64_tr_b16 v[164:165], v229 offset:16384\n"
    "s_add_u32 s98, s64, 0x100\n"
    "v_mfma_f32_16x16x32_bf16 v[88:91], v[168:171], a[116:119], v[88:91]\n"
    "ds_read_b64_tr_b16 v[166:167], v229 offset:16896\n"
    "buffer_load_dwordx4 v236, s[28:31], s98 offen lds\n"
    "v_mfma_f32_16x16x32_bf16 v[92:95], v[172:175], a[116:119], v[92:95]\n"
    "ds_read_b64_tr_b16 v[168:169], v230 offset:16384\n"
    "v_mfma_f32_16x16x32_bf16 v[76:79], v[172:175], a[112:115], v[76:79]\n"
    "ds_read_b64_tr_b16 v[170:171], v230 offset:16896\n"
    "s_waitcnt lgkmcnt(10)\n"
    "v_mfma_f32_16x16x32_bf16 v[64:67], v[176:179], a[120:123], v[64:67]\n"
    "ds_read_b64_tr_b16 v[172:173], v231 offset:16384\n"
    "v_mfma_f32_16x16x32_bf16 v[80:83], v[176:179], a[124:127], v[80:83]\n"
    "ds_read_b64_tr_b16 v[174:175], v231 offset:16896\n"
    "v_mfma_f32_16x16x32_bf16 v[84:87], v[180:183], a[124:127], v[84:87]\n"
    "ds_read_b64_tr_b16 v[176:177], v228 offset:20480\n"
    "buffer_load_dwordx4 v237, s[28:31], s98 offen offset:1024 lds\n"
    "v_mfma_f32_16x16x32_bf16 v[68:71], v[180:183], a[120:123], v[68:71]\n"
    "ds_read_b64_tr_b16 v[178:179], v228 offset:20992\n"
    "s_waitcnt lgkmcnt(10)\n"
    "v_mfma_f32_16x16x32_bf16 v[72:75], v[184:187], a[120:123], v[72:75]\n"
    "ds_read_b64_tr_b16 v[180:181], v229 offset:20480\n"
    "v_mfma_f32_16x16x32_bf16 v[88:91], v[184:187], a[124:127], v[88:91]\n"
    "ds_read_b64_tr_b16 v[182:183], v229 offset:20992\n"
    "v_mfma_f32_16x16x32_bf16 v[92:95], v[188:191], a[124:127], v[92:95]\n"
    "ds_read_b64_tr_b16 v[184:185], v230 offset:20480\n"
    "v_mfma_f32_16x16x32_bf16 v[76:79], v[188:191], a[120:123], v[76:79]\n"
    "ds_read_b64_tr_b16 v[186:187], v230 offset:20992\n"
    "s_waitcnt lgkmcnt(10)\n"
    "v_mfma_f32_16x16x32_bf16 v[64:67], v[160:163], a[128:131], v[64:67]\n"
    "ds_read_b64_tr_b16 v[188:189], v231 offset:20480\n"
    "v_mbcnt_lo_u32_b32 v251, -1, 0\n"
    "v_mfma_f32_16x16x32_bf16 v[80:83], v[160:163], a[132:135], v[80:83]\n"
    "ds_read_b64_tr_b16 v[190:191], v231 offset:20992\n"
    "v_mbcnt_hi_u32_b32 v251, -1, v251\n"
    "v_mfma_f32_16x16x32_bf16 v[84:87], v[164:167], a[132:135], v[84:87]\n"
    "ds_read_b64_tr_b16 v[160:161], v228 offset:24576\n"
    "buffer_load_dwordx4 v238, s[28:31], s98 offen offset:2048 lds\n"
    "v_mfma_f32_16x16x32_bf16 v[68:71], v[164:167], a[128:131], v[68:71]\n"
    "ds_read_b64_tr_b16 v[162:163], v228 offset:25088\n"
    "s_waitcnt lgkmcnt(10)\n"
    "v_mfma_f32_16x16x32_bf16 v[72:75], v[168:171], a[128:131], v[72:75]\n"
    "ds_read_b64_tr_b16 v[164:165], v229 offset:24576\n"
    "v_lshrrev_b32 v249, 5, v251\n"
    "v_mfma_f32_16x16x32_bf16 v[88:91], v[168:171], a[132:135], v[88:91]\n"
    "ds_read_b64_tr_b16 v[166:167], v229 offset:25088\n"
    "v_bfe_u32 v250, v251, 1, 1\n"
    "v_mfma_f32_16x16x32_bf16 v[92:95], v[172:175], a[132:135], v[92:95]\n"
    "ds_read_b64_tr_b16 v[168:169], v230 offset:24576\n"
    "v_xor_b32 v249, v249, v250\n"
    "v_mfma_f32_16x16x32_bf16 v[76:79], v[172:175], a[128:131], v[76:79]\n"
    "ds_read_b64_tr_b16 v[170:171], v230 offset:25088\n"
    "s_waitcnt lgkmcnt(10)\n"
    "v_mfma_f32_16x16x32_bf16 v[64:67], v[176:179], a[136:139], v[64:67]\n"
    "ds_read_b64_tr_b16 v[172:173], v231 offset:24576\n"
    "v_lshlrev_b32 v249, 5, v249\n"
    "v_mfma_f32_16x16x32_bf16 v[80:83], v[176:179], a[140:143], v[80:83]\n"
    "ds_read_b64_tr_b16 v[174:175], v231 offset:25088\n"
    "v_and_b32 v250, 1, v251\n"
    "v_mfma_f32_16x16x32_bf16 v[84:87], v[180:183], a[140:143], v[84:87]\n"
    "ds_read_b64_tr_b16 v[176:177], v228 offset:28672\n"
    "buffer_load_dwordx4 v239, s[28:31], s98 offen offset:3072 lds\n"
    "v_mfma_f32_16x16x32_bf16 v[68:71], v[180:183], a[136:139], v[68:71]\n"
    "ds_read_b64_tr_b16 v[178:179], v228 offset:29184\n"
    "s_waitcnt lgkmcnt(10)\n"
    "v_mfma_f32_16x16x32_bf16 v[72:75], v[184:187], a[136:139], v[72:75]\n"
    "ds_read_b64_tr_b16 v[180:181], v229 offset:28672\n"
    "v_lshl_add_u32 v249, v250, 4, v249\n"
    "v_mfma_f32_16x16x32_bf16 v[88:91], v[184:187], a[140:143], v[88:91]\n"
    "ds_read_b64_tr_b16 v[182:183], v229 offset:29184\n"
    "v_lshrrev_b32 v250, 2, v251\n"
    "v_mfma_f32_16x16x32_bf16 v[92:95], v[188:191], a[140:143], v[92:95]\n"
    "ds_read_b64_tr_b16 v[184:185], v230 offset:28672\n"
    "s_mov_b32 s89, 0x1c00\n"
    "v_mfma_f32_16x16x32_bf16 v[76:79], v[188:191], a[136:139], v[76:79]\n"
    "ds_read_b64_tr_b16 v[186:187], v230 offset:29184\n"
    "s_waitcnt lgkmcnt(10)\n"
    "v_mfma_f32_16x16x32_bf16 v[64:67], v[160:163], a[144:147], v[64:67]\n"
    "ds_read_b64_tr_b16 v[188:189], v231 offset:28672\n"
    "s_add_u32 m0, s85, 0xc000\n"
    "v_mfma_f32_16x16x32_bf16 v[80:83], v[160:163], a[148:151], v[80:83]\n"
    "ds_read_b64_tr_b16 v[190:191], v231 offset:29184\n"
    "s_add_u32 s98, s65, 0x100\n"
    "v_mfma_f32_16x16x32_bf16 v[84:87], v[164:167], a[148:151], v[84:87]\n"
    "ds_read_b64_tr_b16 v[160:161], v228 offset:32768\n"
    "buffer_load_dwordx4 v236, s[28:31], s98 offen lds\n"
    "v_mfma_f32_16x16x32_bf16 v[68:71], v[164:167], a[144:147], v[68:71]\n"
    "ds_read_b64_tr_b16 v[162:163], v228 offset:33280\n"
    "s_waitcnt lgkmcnt(10)\n"
    "v_mfma_f32_16x16x32_bf16 v[72:75], v[168:171], a[144:147], v[72:75]\n"
    "ds_read_b64_tr_b16 v[164:165], v229 offset:32768\n"
    "v_mad_u32_u24 v204, v250, s89, v249\n"
    "v_mfma_f32_16x16x32_bf16 v[88:91], v[168:171], a[148:151], v[88:91]\n"
    "ds_read_b64_tr_b16 v[166:167], v229 offset:33280\n"
    "s_and_b32 s89, s49, 1\n"
    "v_mfma_f32_16x16x32_bf16 v[92:95], v[172:175], a[148:151], v[92:95]\n"
    "ds_read_b64_tr_b16 v[168:169], v230 offset:32768\n"
    "s_mul_i32 s89, s89, 0x140\n"
    "v_mfma_f32_16x16x32_bf16 v[76:79], v[172:175], a[144:147], v[76:79]\n"
    "ds_read_b64_tr_b16 v[170:171], v230 offset:33280\n"
    "s_waitcnt lgkmcnt(10)\n"
    "v_mfma_f32_16x16x32_bf16 v[64:67], v[176:179], a[152:155], v[64:67]\n"
    "ds_read_b64_tr_b16 v[172:173], v231 offset:32768\n"
    "buffer_load_dwordx4 v237, s[28:31], s98 offen offset:1024 lds\n"
    "v_mfma_f32_16x16x32_bf16 v[80:83], v[176:179], a[156:159], v[80:83]\n"
    "ds_read_b64_tr_b16 v[174:175], v231 offset:33280\n"
    "s_add_u32 s99, s16, 0x1000\n"
    "v_mfma_f32_16x16x32_bf16 v[84:87], v[180:183], a[156:159], v[84:87]\n"
    "ds_read_b64_tr_b16 v[176:177], v228 offset:36864\n"
    "buffer_store_dwordx4 v[136:139], v240, s[44:47], s99 offen sc1\n"
    "v_mfma_f32_16x16x32_bf16 v[68:71], v[180:183], a[152:155], v[68:71]\n"
    "ds_read_b64_tr_b16 v[178:179], v228 offset:37376\n"
    "s_waitcnt lgkmcnt(10)\n"
    "v_mfma_f32_16x16x32_bf16 v[72:75], v[184:187], a[152:155], v[72:75]\n"
    "ds_read_b64_tr_b16 v[180:181], v229 offset:36864\n"
    "s_sub_u32 s89, 0x300, s89\n"
    "v_mfma_f32_16x16x32_bf16 v[88:91], v[184:187], a[156:159], v[88:91]\n"
    "ds_read_b64_tr_b16 v[182:183], v229 offset:37376\n"
    "s_lshr_b32 s91, s48, 3\n"
    "v_mfma_f32_16x16x32_bf16 v[92:95], v[188:191], a[156:159], v[92:95]\n"
    "ds_read_b64_tr_b16 v[184:185], v230 offset:36864\n"
    "buffer_load_dwordx4 v238, s[28:31], s98 offen offset:2048 lds\n"
    "v_mfma_f32_16x16x32_bf16 v[76:79], v[188:191], a[152:155], v[76:79]\n"
    "ds_read_b64_tr_b16 v[186:187], v230 offset:37376\n"
    "s_waitcnt lgkmcnt(10)\n"
    "v_mfma_f32_16x16x32_bf16 v[64:67], v[160:163], a[160:163], v[64:67]\n"
    "ds_read_b64_tr_b16 v[188:189], v231 offset:36864\n"
    "s_xor_b32 s90, s55, s91\n"
    "v_mfma_f32_16x16x32_bf16 v[80:83], v[160:163], a[164:167], v[80:83]\n"
    "ds_read_b64_tr_b16 v[190:191], v231 offset:37376\n"
    "s_lshl_b32 s90, s90, 1\n"
    "v_mfma_f32_16x16x32_bf16 v[84:87], v[164:167], a[164:167], v[84:87]\n"
    "ds_read_b64_tr_b16 v[160:161], v228 offset:40960\n"
    "s_lshl_b32 s91, s91, 1\n"
    "v_mfma_f32_16x16x32_bf16 v[68:71], v[164:167], a[160:163], v[68:71]\n"
    "ds_read_b64_tr_b16 v[162:163], v228 offset:41472\n"
    "s_waitcnt lgkmcnt(10)\n"
    "v_mfma_f32_16x16x32_bf16 v[72:75], v[168:171], a[160:163], v[72:75]\n"
    "ds_read_b64_tr_b16 v[164:165], v229 offset:40960\n"
    "buffer_load_dwordx4 v239, s[28:31], s98 offen offset:3072 lds\n"
    "v_mfma_f32_16x16x32_bf16 v[88:91], v[168:171], a[164:167], v[88:91]\n"
    "ds_read_b64_tr_b16 v[166:167], v229 offset:41472\n"
    "s_xor_b32 s91, s91, s50\n"
    "v_mfma_f32_16x16x32_bf16 v[92:95], v[172:175], a[164:167], v[92:95]\n"
    "ds_read_b64_tr_b16 v[168:169], v230 offset:40960\n"
    "s_sub_u32 s90, s90, s91\n"
    "v_mfma_f32_16x16x32_bf16 v[76:79], v[172:175], a[160:163], v[76:79]\n"
    "ds_read_b64_tr_b16 v[170:171], v230 offset:41472\n"
    "s_waitcnt lgkmcnt(10)\n"
    "v_mfma_f32_16x16x32_bf16 v[64:67], v[176:179], a[168:171], v[64:67]\n"
    "ds_read_b64_tr_b16 v[172:173], v231 offset:40960\n"
    "s_waitcnt vmcnt(10)\n"
    "v_mfma_f32_16x16x32_bf16 v[80:83], v[176:179], a[172:175], v[80:83]\n"
    "ds_read_b64_tr_b16 v[174:175], v231 offset:41472\n"
    "v_xor_b32 v249, v246, v244\n"
    "v_mfma_f32_16x16x32_bf16 v[84:87], v[180:183], a[172:175], v[84:87]\n"
    "ds_read_b64_tr_b16 v[176:177], v228 offset:45056\n"
    "v_and_b32 v250, 63, v246\n"
    "v_mfma_f32_16x16x32_bf16 v[68:71], v[180:183], a[168:171], v[68:71]\n"
    "ds_read_b64_tr_b16 v[178:179], v228 offset:45568\n"
    "s_waitcnt lgkmcnt(10)\n"
    "v_mfma_f32_16x16x32_bf16 v[72:75], v[184:187], a[168:171], v[72:75]\n"
    "ds_read_b64_tr_b16 v[180:181], v229 offset:45056\n"
    "v_cmp_lt_u32 vcc, 0xff, v249\n"
    "v_mfma_f32_16x16x32_bf16 v[88:91], v[184:187], a[172:175], v[88:91]\n"
    "ds_read_b64_tr_b16 v[182:183], v229 offset:45568\n"
    "v_cmp_le_u32 s[96:97], 2, v250\n"
    "v_mfma_f32_16x16x32_bf16 v[92:95], v[188:191], a[172:175], v[92:95]\n"
    "ds_read_b64_tr_b16 v[184:185], v230 offset:45056\n"
    "s_and_b32 s96, vcc_lo, s96\n"
    "v_mfma_f32_16x16x32_bf16 v[76:79], v[188:191], a[168:171], v[76:79]\n"
    "ds_read_b64_tr_b16 v[186:187], v230 offset:45568\n"
    "s_waitcnt lgkmcnt(10)\n"
    "v_mfma_f32_16x16x32_bf16 v[64:67], v[160:163], a[176:179], v[64:67]\n"
    "ds_read_b64_tr_b16 v[188:189], v231 offset:45056\n"
    "s_andn2_b32 s96, s82, s96\n"
    "s_cbranch_scc1 .Lslow_kv\n"
    ".Lret_kv:\n"
    "s_waitcnt vmcnt(9)\n"
    "v_mfma_f32_16x16x32_bf16 v[80:83], v[160:163], a[180:183], v[80:83]\n"
    "ds_read_b64_tr_b16 v[190:191], v231 offset:45568\n"
    "s_barrier\n"
    "v_mfma_f32_16x16x32_bf16 v[84:87], v[164:167], a[180:183], v[84:87]\n"
    "ds_read_b64_tr_b16 v[160:161], v224\n"
    "s_add_u32 m0, s85, 0x10000\n"
    "v_mfma_f32_16x16x32_bf16 v[68:71], v[164:167], a[176:179], v[68:71]\n"
    "ds_read_b64_tr_b16 v[162:163], v224 offset:512\n"
    "s_waitcnt lgkmcnt(10)\n"
    "v_mfma_f32_16x16x32_bf16 v[72:75], v[168:171], a[176:179], v[72:75]\n"
    "ds_read_b64_tr_b16 v[164:165], v225\n"
    "s_add_u32 s98, s66, 0x100\n"
    "v_mfma_f32_16x16x32_bf16 v[88:91], v[168:171], a[180:183], v[88:91]\n"
    "ds_read_b64_tr_b16 v[166:167], v225 offset:512\n"
    "buffer_load_dwordx4 v236, s[28:31], s98 offen lds\n"
    "v_mfma_f32_16x16x32_bf16 v[92:95], v[172:175], a[180:183], v[92:95]\n"
    "ds_read_b64_tr_b16 v[168:169], v226\n"
    "s_mul_i32 s90, s90, 0x38000\n"
    "v_mfma_f32_16x16x32_bf16 v[76:79], v[172:175], a[176:179], v[76:79]\n"
    "ds_read_b64_tr_b16 v[170:171], v226 offset:512\n"
    "s_waitcnt lgkmcnt(10)\n"
    "v_mfma_f32_16x16x32_bf16 v[64:67], v[176:179], a[184:187], v[64:67]\n"
    "ds_read_b64_tr_b16 v[172:173], v227\n"
    "buffer_load_dwordx4 v237, s[28:31], s98 offen offset:1024 lds\n"
    "v_mfma_f32_16x16x32_bf16 v[80:83], v[176:179], a[188:191], v[80:83]\n"
    "ds_read_b64_tr_b16 v[174:175], v227 offset:512\n"
    "s_add_u32 s19, s89, s90\n"
    "v_mfma_f32_16x16x32_bf16 v[84:87], v[180:183], a[188:191], v[84:87]\n"
    "ds_read_b64_tr_b16 v[176:177], v224 offset:4096\n"
    "v_mfma_f32_16x16x32_bf16 v[68:71], v[180:183], a[184:187], v[68:71]\n"
    "ds_read_b64_tr_b16 v[178:179], v224 offset:4608\n"
    "s_waitcnt lgkmcnt(10)\n"
    "v_mfma_f32_16x16x32_bf16 v[72:75], v[184:187], a[184:187], v[72:75]\n"
    "ds_read_b64_tr_b16 v[180:181], v225 offset:4096\n"
    "buffer_load_dwordx4 v238, s[28:31], s98 offen offset:2048 lds\n"
    "v_mfma_f32_16x16x32_bf16 v[88:91], v[184:187], a[188:191], v[88:91]\n"
    "ds_read_b64_tr_b16 v[182:183], v225 offset:4608\n"
    "v_mfma_f32_16x16x32_bf16 v[92:95], v[188:191], a[188:191], v[92:95]\n"
    "ds_read_b64_tr_b16 v[184:185], v226 offset:4096\n"
    "v_mfma_f32_16x16x32_bf16 v[76:79], v[188:191], a[184:187], v[76:79]\n"
    "ds_read_b64_tr_b16 v[186:187], v226 offset:4608\n"
    "s_waitcnt lgkmcnt(10)\n"
    "v_mfma_f32_16x16x32_bf16 v[64:67], v[160:163], a[192:195], v[64:67]\n"
    "ds_read_b64_tr_b16 v[188:189], v227 offset:4096\n"
    "v_mfma_f32_16x16x32_bf16 v[80:83], v[160:163], a[196:199], v[80:83]\n"
    "ds_read_b64_tr_b16 v[190:191], v227 offset:4608\n"
    "buffer_load_dwordx4 v239, s[28:31], s98 offen offset:3072 lds\n"
    "v_mfma_f32_16x16x32_bf16 v[84:87], v[164:167], a[196:199], v[84:87]\n"
    "ds_read_b64_tr_b16 v[160:161], v224 offset:8192\n"
    "v_mfma_f32_16x16x32_bf16 v[68:71], v[164:167], a[192:195], v[68:71]\n"
    "ds_read_b64_tr_b16 v[162:163], v224 offset:8704\n"
    "s_waitcnt lgkmcnt(10)\n"
    "v_mfma_f32_16x16x32_bf16 v[72:75], v[168:171], a[192:195], v[72:75]\n"
    "ds_read_b64_tr_b16 v[164:165], v225 offset:8192\n"
    "s_add_u32 m0, s75, 0x1c000\n"
    "v_mfma_f32_16x16x32_bf16 v[88:91], v[168:171], a[196:199], v[88:91]\n"
    "ds_read_b64_tr_b16 v[166:167], v225 offset:8704\n"
    "s_add_u32 s98, s72, 0\n"
    "v_mfma_f32_16x16x32_bf16 v[92:95], v[172:175], a[196:199], v[92:95]\n"
    "ds_read_b64_tr_b16 v[168:169], v226 offset:8192\n"
    "buffer_load_dwordx4 v252, s[44:47], s98 offen sc1 lds\n"
    "v_mfma_f32_16x16x32_bf16 v[76:79], v[172:175], a[192:195], v[76:79]\n"
    "ds_read_b64_tr_b16 v[170:171], v226 offset:8704\n"
    "s_waitcnt lgkmcnt(10)\n"
    "v_mfma_f32_16x16x32_bf16 v[64:67], v[176:179], a[200:203], v[64:67]\n"
    "ds_read_b64_tr_b16 v[172:173], v227 offset:8192\n"
    "v_mfma_f32_16x16x32_bf16 v[80:83], v[176:179], a[204:207], v[80:83]\n"
    "ds_read_b64_tr_b16 v[174:175], v227 offset:8704\n"
    "v_mfma_f32_16x16x32_bf16 v[84:87], v[180:183], a[204:207], v[84:87]\n"
    "ds_read_b64_tr_b16 v[176:177], v224 offset:12288\n"
    "v_mfma_f32_16x16x32_bf16 v[68:71], v[180:183], a[200:203], v[68:71]\n"
    "ds_read_b64_tr_b16 v[178:179], v224 offset:12800\n"
    "s_waitcnt lgkmcnt(10)\n"
    "v_mfma_f32_16x16x32_bf16 v[72:75], v[184:187], a[200:203], v[72:75]\n"
    "ds_read_b64_tr_b16 v[180:181], v225 offset:12288\n"
    "buffer_load_dwordx4 v253, s[44:47], s98 offen offset:1024 sc1 lds\n"
    "v_mfma_f32_16x16x32_bf16 v[88:91], v[184:187], a[204:207], v[88:91]\n"
    "ds_read_b64_tr_b16 v[182:183], v225 offset:12800\n"
    "v_mfma_f32_16x16x32_bf16 v[92:95], v[188:191], a[204:207], v[92:95]\n"
    "ds_read_b64_tr_b16 v[184:185], v226 offset:12288\n"
    "v_mfma_f32_16x16x32_bf16 v[76:79], v[188:191], a[200:203], v[76:79]\n"
    "ds_read_b64_tr_b16 v[186:187], v226 offset:12800\n"
    "s_waitcnt lgkmcnt(10)\n"
    "v_mfma_f32_16x16x32_bf16 v[64:67], v[160:163], a[208:211], v[64:67]\n"
    "ds_read_b64_tr_b16 v[188:189], v227 offset:12288\n"
    "s_add_u32 m0, s85, 0x14000\n"
    "v_mfma_f32_16x16x32_bf16 v[80:83], v[160:163], a[212:215], v[80:83]\n"
    "ds_read_b64_tr_b16 v[190:191], v227 offset:12800\n"
    "s_add_u32 s98, s67, 0x100\n"
    "v_mfma_f32_16x16x32_bf16 v[84:87], v[164:167], a[212:215], v[84:87]\n"
    "ds_read_b64_tr_b16 v[160:161], v224 offset:16384\n"
    "buffer_load_dwordx4 v236, s[28:31], s98 offen lds\n"
    "v_mfma_f32_16x16x32_bf16 v[68:71], v[164:167], a[208:211], v[68:71]\n"
    "ds_read_b64_tr_b16 v[162:163], v224 offset:16896\n"
    "s_waitcnt lgkmcnt(10)\n"
    "v_mfma_f32_16x16x32_bf16 v[72:75], v[168:171], a[208:211], v[72:75]\n"
    "ds_read_b64_tr_b16 v[164:165], v225 offset:16384\n"
    "v_mfma_f32_16x16x32_bf16 v[88:91], v[168:171], a[212:215], v[88:91]\n"
    "ds_read_b64_tr_b16 v[166:167], v225 offset:16896\n"
    "v_mfma_f32_16x16x32_bf16 v[92:95], v[172:175], a[212:215], v[92:95]\n"
    "ds_read_b64_tr_b16 v[168:169], v226 offset:16384\n"
    "v_mfma_f32_16x16x32_bf16 v[76:79], v[172:175], a[208:211], v[76:79]\n"
    "ds_read_b64_tr_b16 v[170:171], v226 offset:16896\n"
    "s_waitcnt lgkmcnt(10)\n"
    "v_mfma_f32_16x16x32_bf16 v[64:67], v[176:179], a[216:219], v[64:67]\n"
    "ds_read_b64_tr_b16 v[172:173], v227 offset:16384\n"
    "buffer_load_dwordx4 v237, s[28:31], s98 offen offset:1024 lds\n"
    "v_mfma_f32_16x16x32_bf16 v[80:83], v[176:179], a[220:223], v[80:83]\n"
    "ds_read_b64_tr_b16 v[174:175], v227 offset:16896\n"
    "buffer_store_dwordx4 v[140:143], v240, s[44:47], s99 offen offset:1024 sc1\n"
    "v_mfma_f32_16x16x32_bf16 v[84:87], v[180:183], a[220:223], v[84:87]\n"
    "ds_read_b64_tr_b16 v[176:177], v224 offset:20480\n"
    "v_mfma_f32_16x16x32_bf16 v[68:71], v[180:183], a[216:219], v[68:71]\n"
    "ds_read_b64_tr_b16 v[178:179], v224 offset:20992\n"
    "s_waitcnt lgkmcnt(10)\n"
    "v_mfma_f32_16x16x32_bf16 v[72:75], v[184:187], a[216:219], v[72:75]\n"
    "ds_read_b64_tr_b16 v[180:181], v225 offset:20480\n"
    "buffer_load_dwordx4 v238, s[28:31], s98 offen offset:2048 lds\n"
    "v_mfma_f32_16x16x32_bf16 v[88:91], v[184:187], a[220:223], v[88:91]\n"
    "ds_read_b64_tr_b16 v[182:183], v225 offset:20992\n"
    "v_mfma_f32_16x16x32_bf16 v[92:95], v[188:191], a[220:223], v[92:95]\n"
    "ds_read_b64_tr_b16 v[184:185], v226 offset:20480\n"
    "v_mfma_f32_16x16x32_bf16 v[76:79], v[188:191], a[216:219], v[76:79]\n"
    "ds_read_b64_tr_b16 v[186:187], v226 offset:20992\n"
    "s_waitcnt lgkmcnt(10)\n"
    "v_mfma_f32_16x16x32_bf16 v[64:67], v[160:163], a[224:227], v[64:67]\n"
    "ds_read_b64_tr_b16 v[188:189], v227 offset:20480\n"
    "v_mfma_f32_16x16x32_bf16 v[80:83], v[160:163], a[228:231], v[80:83]\n"
    "ds_read_b64_tr_b16 v[190:191], v227 offset:20992\n"
    "buffer_load_dwordx4 v239, s[28:31], s98 offen offset:3072 lds\n"
    "v_mfma_f32_16x16x32_bf16 v[84:87], v[164:167], a[228:231], v[84:87]\n"
    "ds_read_b64_tr_b16 v[160:161], v224 offset:24576\n"
    "v_mfma_f32_16x16x32_bf16 v[68:71], v[164:167], a[224:227], v[68:71]\n"
    "ds_read_b64_tr_b16 v[162:163], v224 offset:25088\n"
    "s_waitcnt lgkmcnt(10)\n"
    "v_mfma_f32_16x16x32_bf16 v[72:75], v[168:171], a[224:227], v[72:75]\n"
    "ds_read_b64_tr_b16 v[164:165], v225 offset:24576\n"
    "s_add_u32 m0, s75, 0x1e000\n"
    "v_mfma_f32_16x16x32_bf16 v[88:91], v[168:171], a[228:231], v[88:91]\n"
    "ds_read_b64_tr_b16 v[166:167], v225 offset:25088\n"
    "s_add_u32 s98, s73, 0\n"
    "v_mfma_f32_16x16x32_bf16 v[92:95], v[172:175], a[228:231], v[92:95]\n"
    "ds_read_b64_tr_b16 v[168:169], v226 offset:24576\n"
    "buffer_load_dwordx4 v207, s[44:47], s98 offen sc1 lds\n"
    "v_mfma_f32_16x16x32_bf16 v[76:79], v[172:175], a[224:227], v[76:79]\n"
    "ds_read_b64_tr_b16 v[170:171], v226 offset:25088\n"
    "s_waitcnt lgkmcnt(10)\n"
    "v_mfma_f32_16x16x32_bf16 v[64:67], v[176:179], a[232:235], v[64:67]\n"
    "ds_read_b64_tr_b16 v[172:173], v227 offset:24576\n"
    "v_mfma_f32_16x16x32_bf16 v[80:83], v[176:179], a[236:239], v[80:83]\n"
    "ds_read_b64_tr_b16 v[174:175], v227 offset:25088\n"
    "v_mfma_f32_16x16x32_bf16 v[84:87], v[180:183], a[236:239], v[84:87]\n"
    "ds_read_b64_tr_b16 v[176:177], v224 offset:28672\n"
    "v_mfma_f32_16x16x32_bf16 v[68:71], v[180:183], a[232:235], v[68:71]\n"
    "ds_read_b64_tr_b16 v[178:179], v224 offset:29184\n"
    "s_waitcnt lgkmcnt(10)\n"
    "v_mfma_f32_16x16x32_bf16 v[72:75], v[184:187], a[232:235], v[72:75]\n"
    "ds_read_b64_tr_b16 v[180:181], v225 offset:28672\n"
    "buffer_load_dwordx4 v242, s[44:47], s98 offen offset:1024 sc1 lds\n"
    "v_mfma_f32_16x16x32_bf16 v[88:91], v[184:187], a[236:239], v[88:91]\n"
    "ds_read_b64_tr_b16 v[182:183], v225 offset:29184\n"
    "v_mfma_f32_16x16x32_bf16 v[92:95], v[188:191], a[236:239], v[92:95]\n"
    "ds_read_b64_tr_b16 v[184:185], v226 offset:28672\n"
    "v_mfma_f32_16x16x32_bf16 v[76:79], v[188:191], a[232:235], v[76:79]\n"
    "ds_read_b64_tr_b16 v[186:187], v226 offset:29184\n"
    "s_waitcnt lgkmcnt(10)\n"
    "v_mfma_f32_16x16x32_bf16 v[64:67], v[160:163], a[240:243], v[64:67]\n"
    "ds_read_b64_tr_b16 v[188:189], v227 offset:28672\n"
    "s_waitcnt vmcnt(13)\n"
    "v_mfma_f32_16x16x32_bf16 v[80:83], v[160:163], a[244:247], v[80:83]\n"
    "ds_read_b64_tr_b16 v[190:191], v227 offset:29184\n"
    "s_barrier\n"
    "v_mfma_f32_16x16x32_bf16 v[84:87], v[164:167], a[244:247], v[84:87]\n"
    "ds_read_b64_tr_b16 v[160:161], v224 offset:32768\n"
    "s_add_u32 m0, s85, 0x18000\n"
    "v_mfma_f32_16x16x32_bf16 v[68:71], v[164:167], a[240:243], v[68:71]\n"
    "ds_read_b64_tr_b16 v[162:163], v224 offset:33280\n"
    "s_waitcnt lgkmcnt(10)\n"
    "v_mfma_f32_16x16x32_bf16 v[72:75], v[168:171], a[240:243], v[72:75]\n"
    "ds_read_b64_tr_b16 v[164:165], v225 offset:32768\n"
    "s_add_u32 s98, s68, 0x100\n"
    "v_mfma_f32_16x16x32_bf16 v[88:91], v[168:171], a[244:247], v[88:91]\n"
    "ds_read_b64_tr_b16 v[166:167], v225 offset:33280\n"
    "buffer_load_dwordx4 v236, s[28:31], s98 offen lds\n"
    "v_mfma_f32_16x16x32_bf16 v[92:95], v[172:175], a[244:247], v[92:95]\n"
    "ds_read_b64_tr_b16 v[168:169], v226 offset:32768\n"
    "v_mfma_f32_16x16x32_bf16 v[76:79], v[172:175], a[240:243], v[76:79]\n"
    "ds_read_b64_tr_b16 v[170:171], v226 offset:33280\n"
    "s_waitcnt lgkmcnt(10)\n"
    "v_mfma_f32_16x16x32_bf16 v[64:67], v[176:179], a[248:251], v[64:67]\n"
    "ds_read_b64_tr_b16 v[172:173], v227 offset:32768\n"
    "buffer_load_dwordx4 v237, s[28:31], s98 offen offset:1024 lds\n"
    "v_mfma_f32_16x16x32_bf16 v[80:83], v[176:179], a[252:255], v[80:83]\n"
    "ds_read_b64_tr_b16 v[174:175], v227 offset:33280\n"
    "v_mfma_f32_16x16x32_bf16 v[84:87], v[180:183], a[252:255], v[84:87]\n"
    "ds_read_b64_tr_b16 v[176:177], v224 offset:36864\n"
    "v_mfma_f32_16x16x32_bf16 v[68:71], v[180:183], a[248:251], v[68:71]\n"
    "ds_read_b64_tr_b16 v[178:179], v224 offset:37376\n"
    "s_waitcnt lgkmcnt(10)\n"
    "v_mfma_f32_16x16x32_bf16 v[72:75], v[184:187], a[248:251], v[72:75]\n"
    "ds_read_b64_tr_b16 v[180:181], v225 offset:36864\n"
    "buffer_load_dwordx4 v238, s[28:31], s98 offen offset:2048 lds\n"
    "v_mfma_f32_16x16x32_bf16 v[88:91], v[184:187], a[252:255], v[88:91]\n"
    "ds_read_b64_tr_b16 v[182:183], v225 offset:37376\n"
    "v_mfma_f32_16x16x32_bf16 v[92:95], v[188:191], a[252:255], v[92:95]\n"
    "ds_read_b64_tr_b16 v[184:185], v226 offset:36864\n"
    "v_mfma_f32_16x16x32_bf16 v[76:79], v[188:191], a[248:251], v[76:79]\n"
    "ds_read_b64_tr_b16 v[186:187], v226 offset:37376\n"
    "s_waitcnt lgkmcnt(10)\n"
    "v_mfma_f32_16x16x32_bf16 v[96:99], v[160:163], a[0:3], 0\n"
    "ds_read_b64_tr_b16 v[188:189], v227 offset:36864\n"
    "v_mfma_f32_16x16x32_bf16 v[112:115], v[160:163], a[4:7], 0\n"
    "ds_read_b64_tr_b16 v[190:191], v227 offset:37376\n"
    "buffer_load_dwordx4 v239, s[28:31], s98 offen offset:3072 lds\n"
    "v_mfma_f32_16x16x32_bf16 v[116:119], v[164:167], a[4:7], 0\n"
    "ds_read_b64_tr_b16 v[160:161], v224 offset:40960\n"
    "v_mfma_f32_16x16x32_bf16 v[100:103], v[164:167], a[0:3], 0\n"
    "ds_read_b64_tr_b16 v[162:163], v224 offset:41472\n"
    "s_waitcnt lgkmcnt(10)\n"
    "v_mfma_f32_16x16x32_bf16 v[104:107], v[168:171], a[0:3], 0\n"
    "ds_read_b64_tr_b16 v[164:165], v225 offset:40960\n"
    "s_add_u32 m0, s75, 0x20000\n"
    "v_mfma_f32_16x16x32_bf16 v[120:123], v[168:171], a[4:7], 0\n"
    "ds_read_b64_tr_b16 v[166:167], v225 offset:41472\n"
    "s_add_u32 s98, s74, 0\n"
    "v_mfma_f32_16x16x32_bf16 v[124:127], v[172:175], a[4:7], 0\n"
    "ds_read_b64_tr_b16 v[168:169], v226 offset:40960\n"
    "buffer_load_dwordx4 v240, s[44:47], s98 offen sc1 lds\n"
    "v_mfma_f32_16x16x32_bf16 v[108:111], v[172:175], a[0:3], 0\n"
    "ds_read_b64_tr_b16 v[170:171], v226 offset:41472\n"
    "s_waitcnt lgkmcnt(10)\n"
    "v_mfma_f32_16x16x32_bf16 v[96:99], v[176:179], a[8:11], v[96:99]\n"
    "ds_read_b64_tr_b16 v[172:173], v227 offset:40960\n"
    "v_cvt_pk_bf16_f32 v144, v64, v65\n"
    "v_mfma_f32_16x16x32_bf16 v[112:115], v[176:179], a[12:15], v[112:115]\n"
    "ds_read_b64_tr_b16 v[174:175], v227 offset:41472\n"
    "v_cvt_pk_bf16_f32 v145, v66, v67\n"
    "v_mfma_f32_16x16x32_bf16 v[116:119], v[180:183], a[12:15], v[116:119]\n"
    "ds_read_b64_tr_b16 v[176:177], v224 offset:45056\n"
    "v_cvt_pk_bf16_f32 v146, v68, v69\n"
    "v_mfma_f32_16x16x32_bf16 v[100:103], v[180:183], a[8:11], v[100:103]\n"
    "ds_read_b64_tr_b16 v[178:179], v224 offset:45568\n"
    "s_waitcnt lgkmcnt(10)\n"
    "v_mfma_f32_16x16x32_bf16 v[104:107], v[184:187], a[8:11], v[104:107]\n"
    "ds_read_b64_tr_b16 v[180:181], v225 offset:45056\n"
    "buffer_load_dwordx4 v240, s[44:47], s98 offen offset:1024 sc1 lds\n"
    "v_mfma_f32_16x16x32_bf16 v[120:123], v[184:187], a[12:15], v[120:123]\n"
    "ds_read_b64_tr_b16 v[182:183], v225 offset:45568\n"
    "v_cvt_pk_bf16_f32 v147, v70, v71\n"
    "v_mfma_f32_16x16x32_bf16 v[124:127], v[188:191], a[12:15], v[124:127]\n"
    "ds_read_b64_tr_b16 v[184:185], v226 offset:45056\n"
    "v_pk_max_i16 v144, v144, 0\n"
    "v_mfma_f32_16x16x32_bf16 v[108:111], v[188:191], a[8:11], v[108:111]\n"
    "ds_read_b64_tr_b16 v[186:187], v226 offset:45568\n"
    "s_waitcnt lgkmcnt(10)\n"
    "v_mfma_f32_16x16x32_bf16 v[96:99], v[160:163], a[16:19], v[96:99]\n"
    "ds_read_b64_tr_b16 v[188:189], v227 offset:45056\n"
    "s_add_u32 m0, s85, 0\n"
    "v_mfma_f32_16x16x32_bf16 v[112:115], v[160:163], a[20:23], v[112:115]\n"
    "ds_read_b64_tr_b16 v[190:191], v227 offset:45568\n"
    "s_add_u32 s98, s69, 0x100\n"
    "v_mfma_f32_16x16x32_bf16 v[116:119], v[164:167], a[20:23], v[116:119]\n"
    "ds_read_b64_tr_b16 v[160:161], v224 offset:49152\n"
    "buffer_load_dwordx4 v236, s[28:31], s98 offen lds\n"
    "v_mfma_f32_16x16x32_bf16 v[100:103], v[164:167], a[16:19], v[100:103]\n"
    "ds_read_b64_tr_b16 v[162:163], v224 offset:49664\n"
    "s_waitcnt lgkmcnt(10)\n"
    "v_mfma_f32_16x16x32_bf16 v[104:107], v[168:171], a[16:19], v[104:107]\n"
    "ds_read_b64_tr_b16 v[164:165], v225 offset:49152\n"
    "v_pk_max_i16 v145, v145, 0\n"
    "v_mfma_f32_16x16x32_bf16 v[120:123], v[168:171], a[20:23], v[120:123]\n"
    "ds_read_b64_tr_b16 v[166:167], v225 offset:49664\n"
    "v_pk_max_i16 v146, v146, 0\n"
    "v_mfma_f32_16x16x32_bf16 v[124:127], v[172:175], a[20:23], v[124:127]\n"
    "ds_read_b64_tr_b16 v[168:169], v226 offset:49152\n"
    "v_pk_max_i16 v147, v147, 0\n"
    "v_mfma_f32_16x16x32_bf16 v[108:111], v[172:175], a[16:19], v[108:111]\n"
    "ds_read_b64_tr_b16 v[170:171], v226 offset:49664\n"
    "s_waitcnt lgkmcnt(10)\n"
    "v_mfma_f32_16x16x32_bf16 v[96:99], v[176:179], a[24:27], v[96:99]\n"
    "ds_read_b64_tr_b16 v[172:173], v227 offset:49152\n"
    "buffer_load_dwordx4 v237, s[28:31], s98 offen offset:1024 lds\n"
    "v_mfma_f32_16x16x32_bf16 v[112:115], v[176:179], a[28:31], v[112:115]\n"
    "ds_read_b64_tr_b16 v[174:175], v227 offset:49664\n"
    "s_add_u32 s99, s16, 0x2000\n"
    "v_mfma_f32_16x16x32_bf16 v[116:119], v[180:183], a[28:31], v[116:119]\n"
    "ds_read_b64_tr_b16 v[176:177], v224 offset:53248\n"
    "buffer_store_dwordx4 v[144:147], v240, s[44:47], s99 offen sc1\n"
    "v_mfma_f32_16x16x32_bf16 v[100:103], v[180:183], a[24:27], v[100:103]\n"
    "ds_read_b64_tr_b16 v[178:179], v224 offset:53760\n"
    "s_waitcnt lgkmcnt(10)\n"
    "v_mfma_f32_16x16x32_bf16 v[104:107], v[184:187], a[24:27], v[104:107]\n"
    "ds_read_b64_tr_b16 v[180:181], v225 offset:53248\n"
    "buffer_load_dwordx4 v238, s[28:31], s98 offen offset:2048 lds\n"
    "v_mfma_f32_16x16x32_bf16 v[120:123], v[184:187], a[28:31], v[120:123]\n"
    "ds_read_b64_tr_b16 v[182:183], v225 offset:53760\n"
    "v_cvt_pk_bf16_f32 v148, v80, v81\n"
    "v_mfma_f32_16x16x32_bf16 v[124:127], v[188:191], a[28:31], v[124:127]\n"
    "ds_read_b64_tr_b16 v[184:185], v226 offset:53248\n"
    "v_cvt_pk_bf16_f32 v149, v82, v83\n"
    "v_mfma_f32_16x16x32_bf16 v[108:111], v[188:191], a[24:27], v[108:111]\n"
    "ds_read_b64_tr_b16 v[186:187], v226 offset:53760\n"
    "s_waitcnt lgkmcnt(10)\n"
    "v_mfma_f32_16x16x32_bf16 v[96:99], v[160:163], a[32:35], v[96:99]\n"
    "ds_read_b64_tr_b16 v[188:189], v227 offset:53248\n"
    "v_cvt_pk_bf16_f32 v150, v84, v85\n"
    "v_mfma_f32_16x16x32_bf16 v[112:115], v[160:163], a[36:39], v[112:115]\n"
    "ds_read_b64_tr_b16 v[190:191], v227 offset:53760\n"
    "buffer_load_dwordx4 v239, s[28:31], s98 offen offset:3072 lds\n"
    "v_mfma_f32_16x16x32_bf16 v[116:119], v[164:167], a[36:39], v[116:119]\n"
    "ds_read_b64_tr_b16 v[160:161], v224 offset:57344\n"
    "v_cvt_pk_bf16_f32 v151, v86, v87\n"
    "v_mfma_f32_16x16x32_bf16 v[100:103], v[164:167], a[32:35], v[100:103]\n"
    "ds_read_b64_tr_b16 v[162:163], v224 offset:57856\n"
    "s_waitcnt lgkmcnt(10)\n"
    "v_mfma_f32_16x16x32_bf16 v[104:107], v[168:171], a[32:35], v[104:107]\n"
    "ds_read_b64_tr_b16 v[164:165], v225 offset:57344\n"
    "s_add_u32 m0, s75, 0x22000\n"
    "v_mfma_f32_16x16x32_bf16 v[120:123], v[168:171], a[36:39], v[120:123]\n"
    "ds_read_b64_tr_b16 v[166:167], v225 offset:57856\n"
    "s_add_u32 s98, s76, 0x400000\n"
    "v_mfma_f32_16x16x32_bf16 v[124:127], v[172:175], a[36:39], v[124:127]\n"
    "ds_read_b64_tr_b16 v[168:169], v226 offset:57344\n"
    "buffer_load_dwordx4 v240, s[44:47], s98 offen sc1 lds\n"
    "v_mfma_f32_16x16x32_bf16 v[108:111], v[172:175], a[32:35], v[108:111]\n"
    "ds_read_b64_tr_b16 v[170:171], v226 offset:57856\n"
    "s_waitcnt lgkmcnt(10)\n"
    "v_mfma_f32_16x16x32_bf16 v[96:99], v[176:179], a[40:43], v[96:99]\n"
    "ds_read_b64_tr_b16 v[172:173], v227 offset:57344\n"
    "v_pk_max_i16 v148, v148, 0\n"
    "v_mfma_f32_16x16x32_bf16 v[112:115], v[176:179], a[44:47], v[112:115]\n"
    "ds_read_b64_tr_b16 v[174:175], v227 offset:57856\n"
    "v_pk_max_i16 v149, v149, 0\n"
    "v_mfma_f32_16x16x32_bf16 v[116:119], v[180:183], a[44:47], v[116:119]\n"
    "ds_read_b64_tr_b16 v[176:177], v224 offset:61440\n"
    "v_pk_max_i16 v150, v150, 0\n"
    "v_mfma_f32_16x16x32_bf16 v[100:103], v[180:183], a[40:43], v[100:103]\n"
    "ds_read_b64_tr_b16 v[178:179], v224 offset:61952\n"
    "s_waitcnt lgkmcnt(10)\n"
    "v_mfma_f32_16x16x32_bf16 v[104:107], v[184:187], a[40:43], v[104:107]\n"
    "ds_read_b64_tr_b16 v[180:181], v225 offset:61440\n"
    "buffer_load_dwordx4 v240, s[44:47], s98 offen offset:1024 sc1 lds\n"
    "v_mfma_f32_16x16x32_bf16 v[120:123], v[184:187], a[44:47], v[120:123]\n"
    "ds_read_b64_tr_b16 v[182:183], v225 offset:61952\n"
    "v_pk_max_i16 v151, v151, 0\n"
    "v_mfma_f32_16x16x32_bf16 v[124:127], v[188:191], a[44:47], v[124:127]\n"
    "ds_read_b64_tr_b16 v[184:185], v226 offset:61440\n"
    "v_cvt_pk_bf16_f32 v152, v72, v73\n"
    "v_mfma_f32_16x16x32_bf16 v[108:111], v[188:191], a[40:43], v[108:111]\n"
    "ds_read_b64_tr_b16 v[186:187], v226 offset:61952\n"
    "s_waitcnt lgkmcnt(10)\n"
    "v_mfma_f32_16x16x32_bf16 v[96:99], v[160:163], a[48:51], v[96:99]\n"
    "ds_read_b64_tr_b16 v[188:189], v227 offset:61440\n"
    "s_waitcnt vmcnt(15)\n"
    "v_mfma_f32_16x16x32_bf16 v[112:115], v[160:163], a[52:55], v[112:115]\n"
    "ds_read_b64_tr_b16 v[190:191], v227 offset:61952\n"
    "s_barrier\n"
    "v_mfma_f32_16x16x32_bf16 v[116:119], v[164:167], a[52:55], v[116:119]\n"
    "ds_read_b64_tr_b16 v[160:161], v228\n"
    "s_add_u32 m0, s85, 0x4000\n"
    "v_mfma_f32_16x16x32_bf16 v[100:103], v[164:167], a[48:51], v[100:103]\n"
    "ds_read_b64_tr_b16 v[162:163], v228 offset:512\n"
    "s_waitcnt lgkmcnt(10)\n"
    "v_mfma_f32_16x16x32_bf16 v[104:107], v[168:171], a[48:51], v[104:107]\n"
    "ds_read_b64_tr_b16 v[164:165], v229\n"
    "s_add_u32 s98, s70, 0x100\n"
    "v_mfma_f32_16x16x32_bf16 v[120:123], v[168:171], a[52:55], v[120:123]\n"
    "ds_read_b64_tr_b16 v[166:167], v229 offset:512\n"
    "buffer_load_dwordx4 v236, s[28:31], s98 offen lds\n"
    "v_mfma_f32_16x16x32_bf16 v[124:127], v[172:175], a[52:55], v[124:127]\n"
    "ds_read_b64_tr_b16 v[168:169], v230\n"
    "v_cvt_pk_bf16_f32 v153, v74, v75\n"
    "v_mfma_f32_16x16x32_bf16 v[108:111], v[172:175], a[48:51], v[108:111]\n"
    "ds_read_b64_tr_b16 v[170:171], v230 offset:512\n"
    "s_waitcnt lgkmcnt(10)\n"
    "v_mfma_f32_16x16x32_bf16 v[96:99], v[176:179], a[56:59], v[96:99]\n"
    "ds_read_b64_tr_b16 v[172:173], v231\n"
    "buffer_load_dwordx4 v237, s[28:31], s98 offen offset:1024 lds\n"
    "v_mfma_f32_16x16x32_bf16 v[112:115], v[176:179], a[60:63], v[112:115]\n"
    "ds_read_b64_tr_b16 v[174:175], v231 offset:512\n"
    "v_cvt_pk_bf16_f32 v154, v76, v77\n"
    "v_mfma_f32_16x16x32_bf16 v[116:119], v[180:183], a[60:63], v[116:119]\n"
    "ds_read_b64_tr_b16 v[176:177], v228 offset:4096\n"
    "v_cvt_pk_bf16_f32 v155, v78, v79\n"
    "v_mfma_f32_16x16x32_bf16 v[100:103], v[180:183], a[56:59], v[100:103]\n"
    "ds_read_b64_tr_b16 v[178:179], v228 offset:4608\n"
    "s_waitcnt lgkmcnt(10)\n"
    "v_mfma_f32_16x16x32_bf16 v[104:107], v[184:187], a[56:59], v[104:107]\n"
    "ds_read_b64_tr_b16 v[180:181], v229 offset:4096\n"
    "buffer_load_dwordx4 v238, s[28:31], s98 offen offset:2048 lds\n"
    "v_mfma_f32_16x16x32_bf16 v[120:123], v[184:187], a[60:63], v[120:123]\n"
    "ds_read_b64_tr_b16 v[182:183], v229 offset:4608\n"
    "v_pk_max_i16 v152, v152, 0\n"
    "v_mfma_f32_16x16x32_bf16 v[124:127], v[188:191], a[60:63], v[124:127]\n"
    "ds_read_b64_tr_b16 v[184:185], v230 offset:4096\n"
    "v_pk_max_i16 v153, v153, 0\n"
    "v_mfma_f32_16x16x32_bf16 v[108:111], v[188:191], a[56:59], v[108:111]\n"
    "ds_read_b64_tr_b16 v[186:187], v230 offset:4608\n"
    "s_waitcnt lgkmcnt(10)\n"
    "v_mfma_f32_16x16x32_bf16 v[96:99], v[160:163], a[64:67], v[96:99]\n"
    "ds_read_b64_tr_b16 v[188:189], v231 offset:4096\n"
    "v_pk_max_i16 v154, v154, 0\n"
    "v_mfma_f32_16x16x32_bf16 v[112:115], v[160:163], a[68:71], v[112:115]\n"
    "ds_read_b64_tr_b16 v[190:191], v231 offset:4608\n"
    "buffer_load_dwordx4 v239, s[28:31], s98 offen offset:3072 lds\n"
    "v_mfma_f32_16x16x32_bf16 v[116:119], v[164:167], a[68:71], v[116:119]\n"
    "ds_read_b64_tr_b16 v[160:161], v228 offset:8192\n"
    "v_pk_max_i16 v155, v155, 0\n"
    "v_mfma_f32_16x16x32_bf16 v[100:103], v[164:167], a[64:67], v[100:103]\n"
    "ds_read_b64_tr_b16 v[162:163], v228 offset:8704\n"
    "s_waitcnt lgkmcnt(10)\n"
    "v_mfma_f32_16x16x32_bf16 v[104:107], v[168:171], a[64:67], v[104:107]\n"
    "ds_read_b64_tr_b16 v[164:165], v229 offset:8192\n"
    "s_add_u32 m0, s75, 0x24000\n"
    "v_mfma_f32_16x16x32_bf16 v[120:123], v[168:171], a[68:71], v[120:123]\n"
    "ds_read_b64_tr_b16 v[166:167], v229 offset:8704\n"
    "s_add_u32 s98, s77, 0x400000\n"
    "v_mfma_f32_16x16x32_bf16 v[124:127], v[172:175], a[68:71], v[124:127]\n"
    "ds_read_b64_tr_b16 v[168:169], v230 offset:8192\n"
    "buffer_load_dwordx4 v240, s[44:47], s98 offen sc1 lds\n"
    "v_mfma_f32_16x16x32_bf16 v[108:111], v[172:175], a[64:67], v[108:111]\n"
    "ds_read_b64_tr_b16 v[170:171], v230 offset:8704\n"
    "s_waitcnt lgkmcnt(10)\n"
    "v_mfma_f32_16x16x32_bf16 v[96:99], v[176:179], a[72:75], v[96:99]\n"
    "ds_read_b64_tr_b16 v[172:173], v231 offset:8192\n"
    "v_cvt_pk_bf16_f32 v156, v88, v89\n"
    "v_mfma_f32_16x16x32_bf16 v[112:115], v[176:179], a[76:79], v[112:115]\n"
    "ds_read_b64_tr_b16 v[174:175], v231 offset:8704\n"
    "v_cvt_pk_bf16_f32 v157, v90, v91\n"
    "v_mfma_f32_16x16x32_bf16 v[116:119], v[180:183], a[76:79], v[116:119]\n"
    "ds_read_b64_tr_b16 v[176:177], v228 offset:12288\n"
    "v_cvt_pk_bf16_f32 v158, v92, v93\n"
    "v_mfma_f32_16x16x32_bf16 v[100:103], v[180:183], a[72:75], v[100:103]\n"
    "ds_read_b64_tr_b16 v[178:179], v228 offset:12800\n"
    "s_waitcnt lgkmcnt(10)\n"
    "v_mfma_f32_16x16x32_bf16 v[104:107], v[184:187], a[72:75], v[104:107]\n"
    "ds_read_b64_tr_b16 v[180:181], v229 offset:12288\n"
    "buffer_load_dwordx4 v240, s[44:47], s98 offen offset:1024 sc1 lds\n"
    "v_mfma_f32_16x16x32_bf16 v[120:123], v[184:187], a[76:79], v[120:123]\n"
    "ds_read_b64_tr_b16 v[182:183], v229 offset:12800\n"
    "v_cvt_pk_bf16_f32 v159, v94, v95\n"
    "v_mfma_f32_16x16x32_bf16 v[124:127], v[188:191], a[76:79], v[124:127]\n"
    "ds_read_b64_tr_b16 v[184:185], v230 offset:12288\n"
    "v_pk_max_i16 v156, v156, 0\n"
    "v_mfma_f32_16x16x32_bf16 v[108:111], v[188:191], a[72:75], v[108:111]\n"
    "ds_read_b64_tr_b16 v[186:187], v230 offset:12800\n"
    "s_waitcnt lgkmcnt(10)\n"
    "v_mfma_f32_16x16x32_bf16 v[96:99], v[160:163], a[80:83], v[96:99]\n"
    "ds_read_b64_tr_b16 v[188:189], v231 offset:12288\n"
    "s_add_u32 m0, s85, 0x8000\n"
    "v_mfma_f32_16x16x32_bf16 v[112:115], v[160:163], a[84:87], v[112:115]\n"
    "ds_read_b64_tr_b16 v[190:191], v231 offset:12800\n"
    "s_add_u32 s98, s71, 0x100\n"
    "v_mfma_f32_16x16x32_bf16 v[116:119], v[164:167], a[84:87], v[116:119]\n"
    "ds_read_b64_tr_b16 v[160:161], v228 offset:16384\n"
    "buffer_load_dwordx4 v236, s[28:31], s98 offen lds\n"
    "v_mfma_f32_16x16x32_bf16 v[100:103], v[164:167], a[80:83], v[100:103]\n"
    "ds_read_b64_tr_b16 v[162:163], v228 offset:16896\n"
    "s_waitcnt lgkmcnt(10)\n"
    "v_mfma_f32_16x16x32_bf16 v[104:107], v[168:171], a[80:83], v[104:107]\n"
    "ds_read_b64_tr_b16 v[164:165], v229 offset:16384\n"
    "v_pk_max_i16 v157, v157, 0\n"
    "v_mfma_f32_16x16x32_bf16 v[120:123], v[168:171], a[84:87], v[120:123]\n"
    "ds_read_b64_tr_b16 v[166:167], v229 offset:16896\n"
    "v_pk_max_i16 v158, v158, 0\n"
    "v_mfma_f32_16x16x32_bf16 v[124:127], v[172:175], a[84:87], v[124:127]\n"
    "ds_read_b64_tr_b16 v[168:169], v230 offset:16384\n"
    "v_pk_max_i16 v159, v159, 0\n"
    "v_mfma_f32_16x16x32_bf16 v[108:111], v[172:175], a[80:83], v[108:111]\n"
    "ds_read_b64_tr_b16 v[170:171], v230 offset:16896\n"
    "s_waitcnt lgkmcnt(10)\n"
    "v_mfma_f32_16x16x32_bf16 v[96:99], v[176:179], a[88:91], v[96:99]\n"
    "ds_read_b64_tr_b16 v[172:173], v231 offset:16384\n"
    "buffer_load_dwordx4 v237, s[28:31], s98 offen offset:1024 lds\n"
    "v_mfma_f32_16x16x32_bf16 v[112:115], v[176:179], a[92:95], v[112:115]\n"
    "ds_read_b64_tr_b16 v[174:175], v231 offset:16896\n"
    "buffer_store_dwordx4 v[148:151], v240, s[44:47], s99 offen offset:1024 sc1\n"
    "v_mfma_f32_16x16x32_bf16 v[116:119], v[180:183], a[92:95], v[116:119]\n"
    "ds_read_b64_tr_b16 v[176:177], v228 offset:20480\n"
    "v_mfma_f32_16x16x32_bf16 v[100:103], v[180:183], a[88:91], v[100:103]\n"
    "ds_read_b64_tr_b16 v[178:179], v228 offset:20992\n"
    "s_waitcnt lgkmcnt(10)\n"
    "v_mfma_f32_16x16x32_bf16 v[104:107], v[184:187], a[88:91], v[104:107]\n"
    "ds_read_b64_tr_b16 v[180:181], v229 offset:20480\n"
    "buffer_load_dwordx4 v238, s[28:31], s98 offen offset:2048 lds\n"
    "v_mfma_f32_16x16x32_bf16 v[120:123], v[184:187], a[92:95], v[120:123]\n"
    "ds_read_b64_tr_b16 v[182:183], v229 offset:20992\n"
    "v_mfma_f32_16x16x32_bf16 v[124:127], v[188:191], a[92:95], v[124:127]\n"
    "ds_read_b64_tr_b16 v[184:185], v230 offset:20480\n"
    "v_mfma_f32_16x16x32_bf16 v[108:111], v[188:191], a[88:91], v[108:111]\n"
    "ds_read_b64_tr_b16 v[186:187], v230 offset:20992\n"
    "s_waitcnt lgkmcnt(10)\n"
    "v_mfma_f32_16x16x32_bf16 v[96:99], v[160:163], a[96:99], v[96:99]\n"
    "ds_read_b64_tr_b16 v[188:189], v231 offset:20480\n"
    "s_getpc_b64 s[92:93]\n"
    ".Lct5:\n"
    "v_mfma_f32_16x16x32_bf16 v[112:115], v[160:163], a[100:103], v[112:115]\n"
    "ds_read_b64_tr_b16 v[190:191], v231 offset:20992\n"
    "buffer_load_dwordx4 v239, s[28:31], s98 offen offset:3072 lds\n"
    "v_mfma_f32_16x16x32_bf16 v[116:119], v[164:167], a[100:103], v[116:119]\n"
    "ds_read_b64_tr_b16 v[160:161], v228 offset:24576\n"
    "s_and_b32 s93, s93, 0xffff\n"
    "v_mfma_f32_16x16x32_bf16 v[100:103], v[164:167], a[96:99], v[100:103]\n"
    "ds_read_b64_tr_b16 v[162:163], v228 offset:25088\n"
    "s_waitcnt lgkmcnt(10)\n"
    "v_mfma_f32_16x16x32_bf16 v[104:107], v[168:171], a[96:99], v[104:107]\n"
    "ds_read_b64_tr_b16 v[164:165], v229 offset:24576\n"
    "s_add_u32 m0, s75, 0x26000\n"
    "v_mfma_f32_16x16x32_bf16 v[120:123], v[168:171], a[100:103], v[120:123]\n"
    "ds_read_b64_tr_b16 v[166:167], v229 offset:25088\n"
    "s_add_u32 s98, s74, 0x400000\n"
    "v_mfma_f32_16x16x32_bf16 v[124:127], v[172:175], a[100:103], v[124:127]\n"
    "ds_read_b64_tr_b16 v[168:169], v230 offset:24576\n"
    "buffer_load_dwordx4 v240, s[44:47], s98 offen sc1 lds\n"
    "v_mfma_f32_16x16x32_bf16 v[108:111], v[172:175], a[96:99], v[108:111]\n"
    "ds_read_b64_tr_b16 v[170:171], v230 offset:25088\n"
    "s_waitcnt lgkmcnt(10)\n"
    "v_mfma_f32_16x16x32_bf16 v[96:99], v[176:179], a[104:107], v[96:99]\n"
    "ds_read_b64_tr_b16 v[172:173], v231 offset:24576\n"
    "s_mov_b32 s94, .Lend-.Lct5\n"
    "v_mfma_f32_16x16x32_bf16 v[112:115], v[176:179], a[108:111], v[112:115]\n"
    "ds_read_b64_tr_b16 v[174:175], v231 offset:25088\n"
    "s_min_u32 s94, s94, 0x3000\n"
    "v_mfma_f32_16x16x32_bf16 v[116:119], v[180:183], a[108:111], v[116:119]\n"
    "ds_read_b64_tr_b16 v[176:177], v228 offset:28672\n"
    "s_mov_b32 s95, 0x20000\n"
    "v_mfma_f32_16x16x32_bf16 v[100:103], v[180:183], a[104:107], v[100:103]\n"
    "ds_read_b64_tr_b16 v[178:179], v228 offset:29184\n"
    "s_waitcnt lgkmcnt(10)\n"
    "v_mfma_f32_16x16x32_bf16 v[104:107], v[184:187], a[104:107], v[104:107]\n"
    "ds_read_b64_tr_b16 v[180:181], v229 offset:28672\n"
    "buffer_load_dwordx4 v240, s[44:47], s98 offen offset:1024 sc1 lds\n"
    "v_mfma_f32_16x16x32_bf16 v[120:123], v[184:187], a[108:111], v[120:123]\n"
    "ds_read_b64_tr_b16 v[182:183], v229 offset:29184\n"
    "s_add_u32 s64, s64, s19\n"
    "v_mfma_f32_16x16x32_bf16 v[124:127], v[188:191], a[108:111], v[124:127]\n"
    "ds_read_b64_tr_b16 v[184:185], v230 offset:28672\n"
    "s_add_u32 s65, s65, s19\n"
    "v_mfma_f32_16x16x32_bf16 v[108:111], v[188:191], a[104:107], v[108:111]\n"
    "ds_read_b64_tr_b16 v[186:187], v230 offset:29184\n"
    "s_waitcnt lgkmcnt(10)\n"
    "v_mfma_f32_16x16x32_bf16 v[96:99], v[160:163], a[112:115], v[96:99]\n"
    "ds_read_b64_tr_b16 v[188:189], v231 offset:28672\n"
    "s_waitcnt vmcnt(15)\n"
    "v_mfma_f32_16x16x32_bf16 v[112:115], v[160:163], a[116:119], v[112:115]\n"
    "ds_read_b64_tr_b16 v[190:191], v231 offset:29184\n"
    "s_barrier\n"
    "v_mfma_f32_16x16x32_bf16 v[116:119], v[164:167], a[116:119], v[116:119]\n"
    "ds_read_b64_tr_b16 v[160:161], v228 offset:32768\n"
    "s_add_u32 s66, s66, s19\n"
    "v_mfma_f32_16x16x32_bf16 v[100:103], v[164:167], a[112:115], v[100:103]\n"
    "ds_read_b64_tr_b16 v[162:163], v228 offset:33280\n"
    "s_waitcnt lgkmcnt(10)\n"
    "v_mfma_f32_16x16x32_bf16 v[104:107], v[168:171], a[112:115], v[104:107]\n"
    "ds_read_b64_tr_b16 v[164:165], v229 offset:32768\n"
    "s_add_u32 s67, s67, s19\n"
    "v_mfma_f32_16x16x32_bf16 v[120:123], v[168:171], a[116:119], v[120:123]\n"
    "ds_read_b64_tr_b16 v[166:167], v229 offset:33280\n"
    "s_add_u32 s68, s68, s19\n"
    "v_mfma_f32_16x16x32_bf16 v[124:127], v[172:175], a[116:119], v[124:127]\n"
    "ds_read_b64_tr_b16 v[168:169], v230 offset:32768\n"
    "s_add_u32 s69, s69, s19\n"
    "v_mfma_f32_16x16x32_bf16 v[108:111], v[172:175], a[112:115], v[108:111]\n"
    "ds_read_b64_tr_b16 v[170:171], v230 offset:33280\n"
    "s_waitcnt lgkmcnt(10)\n"
    "v_mfma_f32_16x16x32_bf16 v[96:99], v[176:179], a[120:123], v[96:99]\n"
    "ds_read_b64_tr_b16 v[172:173], v231 offset:32768\n"
    "s_add_u32 s70, s70, s19\n"
    "v_mfma_f32_16x16x32_bf16 v[112:115], v[176:179], a[124:127], v[112:115]\n"
    "ds_read_b64_tr_b16 v[174:175], v231 offset:33280\n"
    "s_add_u32 s71, s71, s19\n"
    "v_mfma_f32_16x16x32_bf16 v[116:119], v[180:183], a[124:127], v[116:119]\n"
    "ds_read_b64_tr_b16 v[176:177], v228 offset:36864\n"
    "v_add_u32 v236, 0, v204\n"
    "v_add_u32 v237, 0x1bc00, v204\n"
    "v_add_u32 v238, 0x37800, v204\n"
    "v_add_u32 v239, 0x53400, v204\n"
    "s_add_u32 m0, s85, 0xc000\n"
    "s_cmp_eq_u32 s54, 0\n"
    "s_cselect_b32 s98, s64, s65\n"
    "buffer_load_dwordx4 v236, s[28:31], s98 offen lds\n"
    "v_mfma_f32_16x16x32_bf16 v[100:103], v[180:183], a[120:123], v[100:103]\n"
    "ds_read_b64_tr_b16 v[178:179], v228 offset:37376\n"
    "s_waitcnt lgkmcnt(10)\n"
    "v_mfma_f32_16x16x32_bf16 v[104:107], v[184:187], a[120:123], v[104:107]\n"
    "ds_read_b64_tr_b16 v[180:181], v229 offset:36864\n"
    "s_and_b32 s91, s13, 0x7f\n"
    "v_mfma_f32_16x16x32_bf16 v[120:123], v[184:187], a[124:127], v[120:123]\n"
    "ds_read_b64_tr_b16 v[182:183], v229 offset:37376\n"
    "s_lshl_b32 s91, s91, 10\n"
    "v_mfma_f32_16x16x32_bf16 v[124:127], v[188:191], a[124:127], v[124:127]\n"
    "ds_read_b64_tr_b16 v[184:185], v230 offset:36864\n"
    "buffer_load_dword v206, v240, s[92:95], s91 offen\n"
    "v_mfma_f32_16x16x32_bf16 v[108:111], v[188:191], a[120:123], v[108:111]\n"
    "ds_read_b64_tr_b16 v[186:187], v230 offset:37376\n"
    "s_waitcnt lgkmcnt(10)\n"
    "v_mfma_f32_16x16x32_bf16 v[96:99], v[160:163], a[128:131], v[96:99]\n"
    "ds_read_b64_tr_b16 v[188:189], v231 offset:36864\n"
    "buffer_load_dwordx4 v237, s[28:31], s98 offen offset:1024 lds\n"
    "v_mfma_f32_16x16x32_bf16 v[112:115], v[160:163], a[132:135], v[112:115]\n"
    "ds_read_b64_tr_b16 v[190:191], v231 offset:37376\n"
    "v_mfma_f32_16x16x32_bf16 v[116:119], v[164:167], a[132:135], v[116:119]\n"
    "ds_read_b64_tr_b16 v[160:161], v228 offset:40960\n"
    "buffer_load_dwordx4 v238, s[28:31], s98 offen offset:2048 lds\n"
    "v_mfma_f32_16x16x32_bf16 v[100:103], v[164:167], a[128:131], v[100:103]\n"
    "ds_read_b64_tr_b16 v[162:163], v228 offset:41472\n"
    "s_waitcnt lgkmcnt(10)\n"
    "v_mfma_f32_16x16x32_bf16 v[104:107], v[168:171], a[128:131], v[104:107]\n"
    "ds_read_b64_tr_b16 v[164:165], v229 offset:40960\n"
    "v_mfma_f32_16x16x32_bf16 v[120:123], v[168:171], a[132:135], v[120:123]\n"
    "ds_read_b64_tr_b16 v[166:167], v229 offset:41472\n"
    "v_mfma_f32_16x16x32_bf16 v[124:127], v[172:175], a[132:135], v[124:127]\n"
    "ds_read_b64_tr_b16 v[168:169], v230 offset:40960\n"
    "v_mfma_f32_16x16x32_bf16 v[108:111], v[172:175], a[128:131], v[108:111]\n"
    "ds_read_b64_tr_b16 v[170:171], v230 offset:41472\n"
    "s_waitcnt lgkmcnt(10)\n"
    "v_mfma_f32_16x16x32_bf16 v[96:99], v[176:179], a[136:139], v[96:99]\n"
    "ds_read_b64_tr_b16 v[172:173], v231 offset:40960\n"
    "v_mfma_f32_16x16x32_bf16 v[112:115], v[176:179], a[140:143], v[112:115]\n"
    "ds_read_b64_tr_b16 v[174:175], v231 offset:41472\n"
    "v_mfma_f32_16x16x32_bf16 v[116:119], v[180:183], a[140:143], v[116:119]\n"
    "ds_read_b64_tr_b16 v[176:177], v228 offset:45056\n"
    "buffer_load_dwordx4 v239, s[28:31], s98 offen offset:3072 lds\n"
    "v_mfma_f32_16x16x32_bf16 v[100:103], v[180:183], a[136:139], v[100:103]\n"
    "ds_read_b64_tr_b16 v[178:179], v228 offset:45568\n"
    "s_waitcnt lgkmcnt(10)\n"
    "v_mfma_f32_16x16x32_bf16 v[104:107], v[184:187], a[136:139], v[104:107]\n"
    "ds_read_b64_tr_b16 v[180:181], v229 offset:45056\n"
    "v_mfma_f32_16x16x32_bf16 v[120:123], v[184:187], a[140:143], v[120:123]\n"
    "ds_read_b64_tr_b16 v[182:183], v229 offset:45568\n"
    "v_mfma_f32_16x16x32_bf16 v[124:127], v[188:191], a[140:143], v[124:127]\n"
    "ds_read_b64_tr_b16 v[184:185], v230 offset:45056\n"
    "v_mfma_f32_16x16x32_bf16 v[108:111], v[188:191], a[136:139], v[108:111]\n"
    "ds_read_b64_tr_b16 v[186:187], v230 offset:45568\n"
    "s_waitcnt lgkmcnt(10)\n"
    "v_mfma_f32_16x16x32_bf16 v[96:99], v[160:163], a[144:147], v[96:99]\n"
    "ds_read_b64_tr_b16 v[188:189], v231 offset:45056\n"
    "s_add_u32 m0, s85, 0x10000\n"
    "v_mfma_f32_16x16x32_bf16 v[112:115], v[160:163], a[148:151], v[112:115]\n"
    "ds_read_b64_tr_b16 v[190:191], v231 offset:45568\n"
    "s_cmp_eq_u32 s54, 0\n"
    "s_cselect_b32 s98, s66, s67\n"
    "v_mfma_f32_16x16x32_bf16 v[116:119], v[164:167], a[148:151], v[116:119]\n"
    "ds_read_b64_tr_b16 v[160:161], v224\n"
    "buffer_load_dwordx4 v236, s[28:31], s98 offen lds\n"
    "v_mfma_f32_16x16x32_bf16 v[100:103], v[164:167], a[144:147], v[100:103]\n"
    "ds_read_b64_tr_b16 v[162:163], v224 offset:512\n"
    "s_waitcnt lgkmcnt(10)\n"
    "v_mfma_f32_16x16x32_bf16 v[104:107], v[168:171], a[144:147], v[104:107]\n"
    "ds_read_b64_tr_b16 v[164:165], v225\n"
    "v_mfma_f32_16x16x32_bf16 v[120:123], v[168:171], a[148:151], v[120:123]\n"
    "ds_read_b64_tr_b16 v[166:167], v225 offset:512\n"
    "v_mfma_f32_16x16x32_bf16 v[124:127], v[172:175], a[148:151], v[124:127]\n"
    "ds_read_b64_tr_b16 v[168:169], v226\n"
    "v_mfma_f32_16x16x32_bf16 v[108:111], v[172:175], a[144:147], v[108:111]\n"
    "ds_read_b64_tr_b16 v[170:171], v226 offset:512\n"
    "s_waitcnt lgkmcnt(10)\n"
    "v_mfma_f32_16x16x32_bf16 v[96:99], v[176:179], a[152:155], v[96:99]\n"
    "ds_read_b64_tr_b16 v[172:173], v227\n"
    "buffer_load_dwordx4 v237, s[28:31], s98 offen offset:1024 lds\n"
    "v_mfma_f32_16x16x32_bf16 v[112:115], v[176:179], a[156:159], v[112:115]\n"
    "ds_read_b64_tr_b16 v[174:175], v227 offset:512\n"
    "s_add_u32 s99, s16, 0x3000\n"
    "v_mfma_f32_16x16x32_bf16 v[116:119], v[180:183], a[156:159], v[116:119]\n"
    "ds_read_b64_tr_b16 v[176:177], v224 offset:4096\n"
    "buffer_store_dwordx4 v[152:155], v240, s[44:47], s99 offen sc1\n"
    "v_mfma_f32_16x16x32_bf16 v[100:103], v[180:183], a[152:155], v[100:103]\n"
    "ds_read_b64_tr_b16 v[178:179], v224 offset:4608\n"
    "s_waitcnt lgkmcnt(10)\n"
    "v_mfma_f32_16x16x32_bf16 v[104:107], v[184:187], a[152:155], v[104:107]\n"
    "ds_read_b64_tr_b16 v[180:181], v225 offset:4096\n"
    "v_mfma_f32_16x16x32_bf16 v[120:123], v[184:187], a[156:159], v[120:123]\n"
    "ds_read_b64_tr_b16 v[182:183], v225 offset:4608\n"
    "v_mfma_f32_16x16x32_bf16 v[124:127], v[188:191], a[156:159], v[124:127]\n"
    "ds_read_b64_tr_b16 v[184:185], v226 offset:4096\n"
    "buffer_load_dwordx4 v238, s[28:31], s98 offen offset:2048 lds\n"
    "v_mfma_f32_16x16x32_bf16 v[108:111], v[188:191], a[152:155], v[108:111]\n"
    "ds_read_b64_tr_b16 v[186:187], v226 offset:4608\n"
    "s_waitcnt lgkmcnt(10)\n"
    "v_mfma_f32_16x16x32_bf16 v[96:99], v[160:163], a[160:163], v[96:99]\n"
    "ds_read_b64_tr_b16 v[188:189], v227 offset:4096\n"
    "v_mfma_f32_16x16x32_bf16 v[112:115], v[160:163], a[164:167], v[112:115]\n"
    "ds_read_b64_tr_b16 v[190:191], v227 offset:4608\n"
    "v_mfma_f32_16x16x32_bf16 v[116:119], v[164:167], a[164:167], v[116:119]\n"
    "ds_read_b64_tr_b16 v[160:161], v224 offset:8192\n"
    "v_mfma_f32_16x16x32_bf16 v[100:103], v[164:167], a[160:163], v[100:103]\n"
    "ds_read_b64_tr_b16 v[162:163], v224 offset:8704\n"
    "s_waitcnt lgkmcnt(10)\n"
    "v_mfma_f32_16x16x32_bf16 v[104:107], v[168:171], a[160:163], v[104:107]\n"
    "ds_read_b64_tr_b16 v[164:165], v225 offset:8192\n"
    "buffer_load_dwordx4 v239, s[28:31], s98 offen offset:3072 lds\n"
    "v_mfma_f32_16x16x32_bf16 v[120:123], v[168:171], a[164:167], v[120:123]\n"
    "ds_read_b64_tr_b16 v[166:167], v225 offset:8704\n"
    "v_mfma_f32_16x16x32_bf16 v[124:127], v[172:175], a[164:167], v[124:127]\n"
    "ds_read_b64_tr_b16 v[168:169], v226 offset:8192\n"
    "v_mfma_f32_16x16x32_bf16 v[108:111], v[172:175], a[160:163], v[108:111]\n"
    "ds_read_b64_tr_b16 v[170:171], v226 offset:8704\n"
    "s_waitcnt lgkmcnt(10)\n"
    "v_mfma_f32_16x16x32_bf16 v[96:99], v[176:179], a[168:171], v[96:99]\n"
    "ds_read_b64_tr_b16 v[172:173], v227 offset:8192\n"
    "v_mfma_f32_16x16x32_bf16 v[112:115], v[176:179], a[172:175], v[112:115]\n"
    "ds_read_b64_tr_b16 v[174:175], v227 offset:8704\n"
    "v_mfma_f32_16x16x32_bf16 v[116:119], v[180:183], a[172:175], v[116:119]\n"
    "ds_read_b64_tr_b16 v[176:177], v224 offset:12288\n"
    "v_mfma_f32_16x16x32_bf16 v[100:103], v[180:183], a[168:171], v[100:103]\n"
    "ds_read_b64_tr_b16 v[178:179], v224 offset:12800\n"
    "s_waitcnt lgkmcnt(10)\n"
    "v_mfma_f32_16x16x32_bf16 v[104:107], v[184:187], a[168:171], v[104:107]\n"
    "ds_read_b64_tr_b16 v[180:181], v225 offset:12288\n"
    "v_mfma_f32_16x16x32_bf16 v[120:123], v[184:187], a[172:175], v[120:123]\n"
    "ds_read_b64_tr_b16 v[182:183], v225 offset:12800\n"
    "v_mfma_f32_16x16x32_bf16 v[124:127], v[188:191], a[172:175], v[124:127]\n"
    "ds_read_b64_tr_b16 v[184:185], v226 offset:12288\n"
    "v_mfma_f32_16x16x32_bf16 v[108:111], v[188:191], a[168:171], v[108:111]\n"
    "ds_read_b64_tr_b16 v[186:187], v226 offset:12800\n"
    "s_waitcnt lgkmcnt(10)\n"
    "v_mfma_f32_16x16x32_bf16 v[96:99], v[160:163], a[176:179], v[96:99]\n"
    "ds_read_b64_tr_b16 v[188:189], v227 offset:12288\n"
    "s_waitcnt vmcnt(12)\n"
    "v_mfma_f32_16x16x32_bf16 v[112:115], v[160:163], a[180:183], v[112:115]\n"
    "ds_read_b64_tr_b16 v[190:191], v227 offset:12800\n"
    "s_barrier\n"
    "v_mfma_f32_16x16x32_bf16 v[116:119], v[164:167], a[180:183], v[116:119]\n"
    "ds_read_b64_tr_b16 v[160:161], v224 offset:16384\n"
    "s_add_u32 m0, s85, 0x14000\n"
    "v_mfma_f32_16x16x32_bf16 v[100:103], v[164:167], a[176:179], v[100:103]\n"
    "ds_read_b64_tr_b16 v[162:163], v224 offset:16896\n"
    "s_waitcnt lgkmcnt(10)\n"
    "v_mfma_f32_16x16x32_bf16 v[104:107], v[168:171], a[176:179], v[104:107]\n"
    "ds_read_b64_tr_b16 v[164:165], v225 offset:16384\n"
    "s_cmp_eq_u32 s54, 0\n"
    "s_cselect_b32 s98, s68, s69\n"
    "v_mfma_f32_16x16x32_bf16 v[120:123], v[168:171], a[180:183], v[120:123]\n"
    "ds_read_b64_tr_b16 v[166:167], v225 offset:16896\n"
    "buffer_load_dwordx4 v236, s[28:31], s98 offen lds\n"
    "v_mfma_f32_16x16x32_bf16 v[124:127], v[172:175], a[180:183], v[124:127]\n"
    "ds_read_b64_tr_b16 v[168:169], v226 offset:16384\n"
    "v_mfma_f32_16x16x32_bf16 v[108:111], v[172:175], a[176:179], v[108:111]\n"
    "ds_read_b64_tr_b16 v[170:171], v226 offset:16896\n"
    "s_waitcnt lgkmcnt(10)\n"
    "v_mfma_f32_16x16x32_bf16 v[96:99], v[176:179], a[184:187], v[96:99]\n"
    "ds_read_b64_tr_b16 v[172:173], v227 offset:16384\n"
    "v_mfma_f32_16x16x32_bf16 v[112:115], v[176:179], a[188:191], v[112:115]\n"
    "ds_read_b64_tr_b16 v[174:175], v227 offset:16896\n"
    "v_mfma_f32_16x16x32_bf16 v[116:119], v[180:183], a[188:191], v[116:119]\n"
    "ds_read_b64_tr_b16 v[176:177], v224 offset:20480\n"
    "buffer_load_dwordx4 v237, s[28:31], s98 offen offset:1024 lds\n"
    "v_mfma_f32_16x16x32_bf16 v[100:103], v[180:183], a[184:187], v[100:103]\n"
    "ds_read_b64_tr_b16 v[178:179], v224 offset:20992\n"
    "s_waitcnt lgkmcnt(10)\n"
    "v_mfma_f32_16x16x32_bf16 v[104:107], v[184:187], a[184:187], v[104:107]\n"
    "ds_read_b64_tr_b16 v[180:181], v225 offset:20480\n"
    "v_mfma_f32_16x16x32_bf16 v[120:123], v[184:187], a[188:191], v[120:123]\n"
    "ds_read_b64_tr_b16 v[182:183], v225 offset:20992\n"
    "v_mfma_f32_16x16x32_bf16 v[124:127], v[188:191], a[188:191], v[124:127]\n"
    "ds_read_b64_tr_b16 v[184:185], v226 offset:20480\n"
    "v_mfma_f32_16x16x32_bf16 v[108:111], v[188:191], a[184:187], v[108:111]\n"
    "ds_read_b64_tr_b16 v[186:187], v226 offset:20992\n"
    "s_waitcnt lgkmcnt(10)\n"
    "v_mfma_f32_16x16x32_bf16 v[96:99], v[160:163], a[192:195], v[96:99]\n"
    "ds_read_b64_tr_b16 v[188:189], v227 offset:20480\n"
    "v_mfma_f32_16x16x32_bf16 v[112:115], v[160:163], a[196:199], v[112:115]\n"
    "ds_read_b64_tr_b16 v[190:191], v227 offset:20992\n"
    "v_mfma_f32_16x16x32_bf16 v[116:119], v[164:167], a[196:199], v[116:119]\n"
    "ds_read_b64_tr_b16 v[160:161], v224 offset:24576\n"
    "buffer_load_dwordx4 v238, s[28:31], s98 offen offset:2048 lds\n"
    "v_mfma_f32_16x16x32_bf16 v[100:103], v[164:167], a[192:195], v[100:103]\n"
    "ds_read_b64_tr_b16 v[162:163], v224 offset:25088\n"
    "s_waitcnt lgkmcnt(10)\n"
    "v_mfma_f32_16x16x32_bf16 v[104:107], v[168:171], a[192:195], v[104:107]\n"
    "ds_read_b64_tr_b16 v[164:165], v225 offset:24576\n"
    "v_mfma_f32_16x16x32_bf16 v[120:123], v[168:171], a[196:199], v[120:123]\n"
    "ds_read_b64_tr_b16 v[166:167], v225 offset:25088\n"
    "v_mfma_f32_16x16x32_bf16 v[124:127], v[172:175], a[196:199], v[124:127]\n"
    "ds_read_b64_tr_b16 v[168:169], v226 offset:24576\n"
    "v_mfma_f32_16x16x32_bf16 v[108:111], v[172:175], a[192:195], v[108:111]\n"
    "ds_read_b64_tr_b16 v[170:171], v226 offset:25088\n"
    "s_waitcnt lgkmcnt(10)\n"
    "v_mfma_f32_16x16x32_bf16 v[96:99], v[176:179], a[200:203], v[96:99]\n"
    "ds_read_b64_tr_b16 v[172:173], v227 offset:24576\n"
    "v_mfma_f32_16x16x32_bf16 v[112:115], v[176:179], a[204:207], v[112:115]\n"
    "ds_read_b64_tr_b16 v[174:175], v227 offset:25088\n"
    "v_mfma_f32_16x16x32_bf16 v[116:119], v[180:183], a[204:207], v[116:119]\n"
    "ds_read_b64_tr_b16 v[176:177], v224 offset:28672\n"
    "buffer_load_dwordx4 v239, s[28:31], s98 offen offset:3072 lds\n"
    "v_mfma_f32_16x16x32_bf16 v[100:103], v[180:183], a[200:203], v[100:103]\n"
    "ds_read_b64_tr_b16 v[178:179], v224 offset:29184\n"
    "s_waitcnt lgkmcnt(10)\n"
    "v_mfma_f32_16x16x32_bf16 v[104:107], v[184:187], a[200:203], v[104:107]\n"
    "ds_read_b64_tr_b16 v[180:181], v225 offset:28672\n"
    "v_mfma_f32_16x16x32_bf16 v[120:123], v[184:187], a[204:207], v[120:123]\n"
    "ds_read_b64_tr_b16 v[182:183], v225 offset:29184\n"
    "v_mfma_f32_16x16x32_bf16 v[124:127], v[188:191], a[204:207], v[124:127]\n"
    "ds_read_b64_tr_b16 v[184:185], v226 offset:28672\n"
    "v_mfma_f32_16x16x32_bf16 v[108:111], v[188:191], a[200:203], v[108:111]\n"
    "ds_read_b64_tr_b16 v[186:187], v226 offset:29184\n"
    "s_waitcnt lgkmcnt(10)\n"
    "v_mfma_f32_16x16x32_bf16 v[96:99], v[160:163], a[208:211], v[96:99]\n"
    "ds_read_b64_tr_b16 v[188:189], v227 offset:28672\n"
    "s_add_u32 m0, s85, 0x18000\n"
    "v_mfma_f32_16x16x32_bf16 v[112:115], v[160:163], a[212:215], v[112:115]\n"
    "ds_read_b64_tr_b16 v[190:191], v227 offset:29184\n"
    "s_cmp_eq_u32 s54, 0\n"
    "s_cselect_b32 s98, s70, s71\n"
    "v_mfma_f32_16x16x32_bf16 v[116:119], v[164:167], a[212:215], v[116:119]\n"
    "ds_read_b64_tr_b16 v[160:161], v224 offset:32768\n"
    "buffer_load_dwordx4 v236, s[28:31], s98 offen lds\n"
    "v_mfma_f32_16x16x32_bf16 v[100:103], v[164:167], a[208:211], v[100:103]\n"
    "ds_read_b64_tr_b16 v[162:163], v224 offset:33280\n"
    "s_waitcnt lgkmcnt(10)\n"
    "v_mfma_f32_16x16x32_bf16 v[104:107], v[168:171], a[208:211], v[104:107]\n"
    "ds_read_b64_tr_b16 v[164:165], v225 offset:32768\n"
    "v_mfma_f32_16x16x32_bf16 v[120:123], v[168:171], a[212:215], v[120:123]\n"
    "ds_read_b64_tr_b16 v[166:167], v225 offset:33280\n"
    "v_mfma_f32_16x16x32_bf16 v[124:127], v[172:175], a[212:215], v[124:127]\n"
    "ds_read_b64_tr_b16 v[168:169], v226 offset:32768\n"
    "v_mfma_f32_16x16x32_bf16 v[108:111], v[172:175], a[208:211], v[108:111]\n"
    "ds_read_b64_tr_b16 v[170:171], v226 offset:33280\n"
    "s_waitcnt lgkmcnt(10)\n"
    "v_mfma_f32_16x16x32_bf16 v[96:99], v[176:179], a[216:219], v[96:99]\n"
    "ds_read_b64_tr_b16 v[172:173], v227 offset:32768\n"
    "buffer_load_dwordx4 v237, s[28:31], s98 offen offset:1024 lds\n"
    "v_mfma_f32_16x16x32_bf16 v[112:115], v[176:179], a[220:223], v[112:115]\n"
    "ds_read_b64_tr_b16 v[174:175], v227 offset:33280\n"
    "buffer_store_dwordx4 v[156:159], v240, s[44:47], s99 offen offset:1024 sc1\n"
    "v_mfma_f32_16x16x32_bf16 v[116:119], v[180:183], a[220:223], v[116:119]\n"
    "ds_read_b64_tr_b16 v[176:177], v224 offset:36864\n"
    "v_mfma_f32_16x16x32_bf16 v[100:103], v[180:183], a[216:219], v[100:103]\n"
    "ds_read_b64_tr_b16 v[178:179], v224 offset:37376\n"
    "s_waitcnt lgkmcnt(10)\n"
    "v_mfma_f32_16x16x32_bf16 v[104:107], v[184:187], a[216:219], v[104:107]\n"
    "ds_read_b64_tr_b16 v[180:181], v225 offset:36864\n"
    "v_mfma_f32_16x16x32_bf16 v[120:123], v[184:187], a[220:223], v[120:123]\n"
    "ds_read_b64_tr_b16 v[182:183], v225 offset:37376\n"
    "v_mfma_f32_16x16x32_bf16 v[124:127], v[188:191], a[220:223], v[124:127]\n"
    "ds_read_b64_tr_b16 v[184:185], v226 offset:36864\n"
    "buffer_load_dwordx4 v238, s[28:31], s98 offen offset:2048 lds\n"
    "v_mfma_f32_16x16x32_bf16 v[108:111], v[188:191], a[216:219], v[108:111]\n"
    "ds_read_b64_tr_b16 v[186:187], v226 offset:37376\n"
    "s_waitcnt lgkmcnt(10)\n"
    "v_mfma_f32_16x16x32_bf16 v[96:99], v[160:163], a[224:227], v[96:99]\n"
    "ds_read_b64_tr_b16 v[188:189], v227 offset:36864\n"
    "v_mfma_f32_16x16x32_bf16 v[112:115], v[160:163], a[228:231], v[112:115]\n"
    "ds_read_b64_tr_b16 v[190:191], v227 offset:37376\n"
    "v_mfma_f32_16x16x32_bf16 v[116:119], v[164:167], a[228:231], v[116:119]\n"
    "ds_read_b64_tr_b16 v[160:161], v224 offset:40960\n"
    "v_mfma_f32_16x16x32_bf16 v[100:103], v[164:167], a[224:227], v[100:103]\n"
    "ds_read_b64_tr_b16 v[162:163], v224 offset:41472\n"
    "s_waitcnt lgkmcnt(10)\n"
    "v_mfma_f32_16x16x32_bf16 v[104:107], v[168:171], a[224:227], v[104:107]\n"
    "ds_read_b64_tr_b16 v[164:165], v225 offset:40960\n"
    "buffer_load_dwordx4 v239, s[28:31], s98 offen offset:3072 lds\n"
    "v_mfma_f32_16x16x32_bf16 v[120:123], v[168:171], a[228:231], v[120:123]\n"
    "ds_read_b64_tr_b16 v[166:167], v225 offset:41472\n"
    "v_mfma_f32_16x16x32_bf16 v[124:127], v[172:175], a[228:231], v[124:127]\n"
    "ds_read_b64_tr_b16 v[168:169], v226 offset:40960\n"
    "v_mfma_f32_16x16x32_bf16 v[108:111], v[172:175], a[224:227], v[108:111]\n"
    "ds_read_b64_tr_b16 v[170:171], v226 offset:41472\n"
    "s_waitcnt lgkmcnt(10)\n"
    "v_mfma_f32_16x16x32_bf16 v[96:99], v[176:179], a[232:235], v[96:99]\n"
    "ds_read_b64_tr_b16 v[172:173], v227 offset:40960\n"
    "v_mfma_f32_16x16x32_bf16 v[112:115], v[176:179], a[236:239], v[112:115]\n"
    "ds_read_b64_tr_b16 v[174:175], v227 offset:41472\n"
    "v_mfma_f32_16x16x32_bf16 v[116:119], v[180:183], a[236:239], v[116:119]\n"
    "ds_read_b64_tr_b16 v[176:177], v224 offset:45056\n"
    "v_mbcnt_lo_u32_b32 v251, -1, 0\n"
    "v_mfma_f32_16x16x32_bf16 v[100:103], v[180:183], a[232:235], v[100:103]\n"
    "ds_read_b64_tr_b16 v[178:179], v224 offset:45568\n"
    "s_waitcnt lgkmcnt(10)\n"
    "v_mfma_f32_16x16x32_bf16 v[104:107], v[184:187], a[232:235], v[104:107]\n"
    "ds_read_b64_tr_b16 v[180:181], v225 offset:45056\n"
    "v_mbcnt_hi_u32_b32 v251, -1, v251\n"
    "v_mfma_f32_16x16x32_bf16 v[120:123], v[184:187], a[236:239], v[120:123]\n"
    "ds_read_b64_tr_b16 v[182:183], v225 offset:45568\n"
    "v_lshlrev_b32 v249, 7, v251\n"
    "v_mfma_f32_16x16x32_bf16 v[124:127], v[188:191], a[236:239], v[124:127]\n"
    "ds_read_b64_tr_b16 v[184:185], v226 offset:45056\n"
    "v_mov_b32 v250, 0x80000000\n"
    "v_mfma_f32_16x16x32_bf16 v[108:111], v[188:191], a[232:235], v[108:111]\n"
    "ds_read_b64_tr_b16 v[186:187], v226 offset:45568\n"
    "s_waitcnt lgkmcnt(10)\n"
    "v_mfma_f32_16x16x32_bf16 v[96:99], v[160:163], a[240:243], v[96:99]\n"
    "ds_read_b64_tr_b16 v[188:189], v227 offset:45056\n"
    "s_waitcnt vmcnt(9)\n"
    "v_mfma_f32_16x16x32_bf16 v[112:115], v[160:163], a[244:247], v[112:115]\n"
    "ds_read_b64_tr_b16 v[190:191], v227 offset:45568\n"
    "s_barrier\n"
    "v_mfma_f32_16x16x32_bf16 v[116:119], v[164:167], a[244:247], v[116:119]\n"
    "ds_read_b64_tr_b16 v[160:161], v232 offset:49152\n"
    "v_cmp_gt_u32 vcc, 16, v251\n"
    "v_mfma_f32_16x16x32_bf16 v[100:103], v[164:167], a[240:243], v[100:103]\n"
    "ds_read_b64_tr_b16 v[162:163], v232 offset:49408\n"
    "s_waitcnt lgkmcnt(10)\n"
    "v_mfma_f32_16x16x32_bf16 v[104:107], v[168:171], a[240:243], v[104:107]\n"
    "ds_read_b64_tr_b16 v[164:165], v233 offset:49152\n"
    "v_cndmask_b32 v249, v250, v249, vcc\n"
    "v_mfma_f32_16x16x32_bf16 v[120:123], v[168:171], a[244:247], v[120:123]\n"
    "ds_read_b64_tr_b16 v[166:167], v233 offset:49408\n"
    "s_lshl_b32 s89, s13, 11\n"
    "v_mfma_f32_16x16x32_bf16 v[124:127], v[172:175], a[244:247], v[124:127]\n"
    "ds_read_b64_tr_b16 v[168:169], v232 offset:51200\n"
    "s_lshl_b32 s90, s51, 19\n"
    "v_mfma_f32_16x16x32_bf16 v[108:111], v[172:175], a[240:243], v[108:111]\n"
    "ds_read_b64_tr_b16 v[170:171], v232 offset:51456\n"
    "s_waitcnt lgkmcnt(10)\n"
    "v_mfma_f32_16x16x32_bf16 v[96:99], v[176:179], a[248:251], v[96:99]\n"
    "ds_read_b64_tr_b16 v[172:173], v233 offset:51200\n"
    "s_add_u32 s89, s89, s90\n"
    "v_mfma_f32_16x16x32_bf16 v[112:115], v[176:179], a[252:255], v[112:115]\n"
    "ds_read_b64_tr_b16 v[174:175], v233 offset:51456\n"
    "buffer_load_dword v206, v249, s[36:39], s89 offen\n"
    "v_mfma_f32_16x16x32_bf16 v[116:119], v[180:183], a[252:255], v[116:119]\n"
    "ds_read_b64_tr_b16 v[176:177], v232 offset:53248\n"
    "v_mfma_f32_16x16x32_bf16 v[100:103], v[180:183], a[248:251], v[100:103]\n"
    "ds_read_b64_tr_b16 v[178:179], v232 offset:53504\n"
    "s_waitcnt lgkmcnt(10)\n"
    "v_mfma_f32_16x16x32_bf16 v[104:107], v[184:187], a[248:251], v[104:107]\n"
    "ds_read_b64_tr_b16 v[180:181], v233 offset:53248\n"
    "v_mfma_f32_16x16x32_bf16 v[120:123], v[184:187], a[252:255], v[120:123]\n"
    "ds_read_b64_tr_b16 v[182:183], v233 offset:53504\n"
    "v_mfma_f32_16x16x32_bf16 v[124:127], v[188:191], a[252:255], v[124:127]\n"
    "ds_read_b64_tr_b16 v[184:185], v232 offset:55296\n"
    "v_mfma_f32_16x16x32_bf16 v[108:111], v[188:191], a[248:251], v[108:111]\n"
    "ds_read_b64_tr_b16 v[186:187], v232 offset:55552\n"
    "s_waitcnt lgkmcnt(10)\n"
    "v_mfma_f32_16x16x32_bf16 v[64:67], v[160:163], a[0:3], 0\n"
    "ds_read_b64_tr_b16 v[188:189], v233 offset:55296\n"
    "v_mfma_f32_16x16x32_bf16 v[72:75], v[160:163], a[4:7], 0\n"
    "ds_read_b64_tr_b16 v[190:191], v233 offset:55552\n"
    "v_mfma_f32_16x16x32_bf16 v[76:79], v[164:167], a[4:7], 0\n"
    "ds_read_b64_tr_b16 v[160:161], v232 offset:57344\n"
    "v_mfma_f32_16x16x32_bf16 v[68:71], v[164:167], a[0:3], 0\n"
    "ds_read_b64_tr_b16 v[162:163], v232 offset:57600\n"
    "s_waitcnt lgkmcnt(10)\n"
    "v_mfma_f32_16x16x32_bf16 v[64:67], v[168:171], a[8:11], v[64:67]\n"
    "ds_read_b64_tr_b16 v[164:165], v233 offset:57344\n"
    "v_cvt_pk_bf16_f32 v128, v96, v97\n"
    "v_mfma_f32_16x16x32_bf16 v[72:75], v[168:171], a[12:15], v[72:75]\n"
    "ds_read_b64_tr_b16 v[166:167], v233 offset:57600\n"
    "v_cvt_pk_bf16_f32 v129, v98, v99\n"
    "v_mfma_f32_16x16x32_bf16 v[76:79], v[172:175], a[12:15], v[76:79]\n"
    "ds_read_b64_tr_b16 v[168:169], v232 offset:59392\n"
    "v_cvt_pk_bf16_f32 v130, v100, v101\n"
    "v_mfma_f32_16x16x32_bf16 v[68:71], v[172:175], a[8:11], v[68:71]\n"
    "ds_read_b64_tr_b16 v[170:171], v232 offset:59648\n"
    "s_waitcnt lgkmcnt(10)\n"
    "v_mfma_f32_16x16x32_bf16 v[64:67], v[176:179], a[16:19], v[64:67]\n"
    "ds_read_b64_tr_b16 v[172:173], v233 offset:59392\n"
    "v_cvt_pk_bf16_f32 v131, v102, v103\n"
    "v_mfma_f32_16x16x32_bf16 v[72:75], v[176:179], a[20:23], v[72:75]\n"
    "ds_read_b64_tr_b16 v[174:175], v233 offset:59648\n"
    "v_pk_max_i16 v128, v128, 0\n"
    "v_mfma_f32_16x16x32_bf16 v[76:79], v[180:183], a[20:23], v[76:79]\n"
    "ds_read_b64_tr_b16 v[176:177], v232 offset:61440\n"
    "v_pk_max_i16 v129, v129, 0\n"
    "v_mfma_f32_16x16x32_bf16 v[68:71], v[180:183], a[16:19], v[68:71]\n"
    "ds_read_b64_tr_b16 v[178:179], v232 offset:61696\n"
    "s_waitcnt lgkmcnt(10)\n"
    "v_mfma_f32_16x16x32_bf16 v[64:67], v[184:187], a[24:27], v[64:67]\n"
    "ds_read_b64_tr_b16 v[180:181], v233 offset:61440\n"
    "v_pk_max_i16 v130, v130, 0\n"
    "v_mfma_f32_16x16x32_bf16 v[72:75], v[184:187], a[28:31], v[72:75]\n"
    "ds_read_b64_tr_b16 v[182:183], v233 offset:61696\n"
    "v_pk_max_i16 v131, v131, 0\n"
    "v_mfma_f32_16x16x32_bf16 v[76:79], v[188:191], a[28:31], v[76:79]\n"
    "ds_read_b64_tr_b16 v[184:185], v232 offset:63488\n"
    "v_cvt_pk_bf16_f32 v132, v112, v113\n"
    "v_mfma_f32_16x16x32_bf16 v[68:71], v[188:191], a[24:27], v[68:71]\n"
    "ds_read_b64_tr_b16 v[186:187], v232 offset:63744\n"
    "s_waitcnt lgkmcnt(10)\n"
    "v_mfma_f32_16x16x32_bf16 v[64:67], v[160:163], a[32:35], v[64:67]\n"
    "ds_read_b64_tr_b16 v[188:189], v233 offset:63488\n"
    "v_cvt_pk_bf16_f32 v133, v114, v115\n"
    "v_mfma_f32_16x16x32_bf16 v[72:75], v[160:163], a[36:39], v[72:75]\n"
    "ds_read_b64_tr_b16 v[190:191], v233 offset:63744\n"
    "v_cvt_pk_bf16_f32 v134, v116, v117\n"
    "v_mfma_f32_16x16x32_bf16 v[76:79], v[164:167], a[36:39], v[76:79]\n"
    "ds_read_b64_tr_b16 v[160:161], v234\n"
    "v_cvt_pk_bf16_f32 v135, v118, v119\n"
    "v_mfma_f32_16x16x32_bf16 v[68:71], v[164:167], a[32:35], v[68:71]\n"
    "ds_read_b64_tr_b16 v[162:163], v234 offset:256\n"
    "s_waitcnt lgkmcnt(10)\n"
    "v_mfma_f32_16x16x32_bf16 v[64:67], v[168:171], a[40:43], v[64:67]\n"
    "ds_read_b64_tr_b16 v[164:165], v235\n"
    "v_pk_max_i16 v132, v132, 0\n"
    "v_mfma_f32_16x16x32_bf16 v[72:75], v[168:171], a[44:47], v[72:75]\n"
    "ds_read_b64_tr_b16 v[166:167], v235 offset:256\n"
    "v_pk_max_i16 v133, v133, 0\n"
    "v_mfma_f32_16x16x32_bf16 v[76:79], v[172:175], a[44:47], v[76:79]\n"
    "ds_read_b64_tr_b16 v[168:169], v234 offset:2048\n"
    "v_pk_max_i16 v134, v134, 0\n"
    "v_mfma_f32_16x16x32_bf16 v[68:71], v[172:175], a[40:43], v[68:71]\n"
    "ds_read_b64_tr_b16 v[170:171], v234 offset:2304\n"
    "s_waitcnt lgkmcnt(10)\n"
    "v_mfma_f32_16x16x32_bf16 v[64:67], v[176:179], a[48:51], v[64:67]\n"
    "ds_read_b64_tr_b16 v[172:173], v235 offset:2048\n"
    "s_add_u32 s99, s16, 0x4000\n"
    "v_mfma_f32_16x16x32_bf16 v[72:75], v[176:179], a[52:55], v[72:75]\n"
    "ds_read_b64_tr_b16 v[174:175], v235 offset:2304\n"
    "buffer_store_dwordx4 v[128:131], v240, s[44:47], s99 offen sc1\n"
    "v_mfma_f32_16x16x32_bf16 v[76:79], v[180:183], a[52:55], v[76:79]\n"
    "ds_read_b64_tr_b16 v[176:177], v234 offset:4096\n"
    "v_pk_max_i16 v135, v135, 0\n"
    "v_mfma_f32_16x16x32_bf16 v[68:71], v[180:183], a[48:51], v[68:71]\n"
    "ds_read_b64_tr_b16 v[178:179], v234 offset:4352\n"
    "s_waitcnt lgkmcnt(10)\n"
    "v_mfma_f32_16x16x32_bf16 v[64:67], v[184:187], a[56:59], v[64:67]\n"
    "ds_read_b64_tr_b16 v[180:181], v235 offset:4096\n"
    "v_cvt_pk_bf16_f32 v136, v104, v105\n"
    "v_mfma_f32_16x16x32_bf16 v[72:75], v[184:187], a[60:63], v[72:75]\n"
    "ds_read_b64_tr_b16 v[182:183], v235 offset:4352\n"
    "v_cvt_pk_bf16_f32 v137, v106, v107\n"
    "v_mfma_f32_16x16x32_bf16 v[76:79], v[188:191], a[60:63], v[76:79]\n"
    "ds_read_b64_tr_b16 v[184:185], v234 offset:6144\n"
    "v_cvt_pk_bf16_f32 v138, v108, v109\n"
    "v_mfma_f32_16x16x32_bf16 v[68:71], v[188:191], a[56:59], v[68:71]\n"
    "ds_read_b64_tr_b16 v[186:187], v234 offset:6400\n"
    "s_waitcnt lgkmcnt(10)\n"
    "v_mfma_f32_16x16x32_bf16 v[64:67], v[160:163], a[64:67], v[64:67]\n"
    "ds_read_b64_tr_b16 v[188:189], v235 offset:6144\n"
    "v_cvt_pk_bf16_f32 v139, v110, v111\n"
    "v_mfma_f32_16x16x32_bf16 v[72:75], v[160:163], a[68:71], v[72:75]\n"
    "ds_read_b64_tr_b16 v[190:191], v235 offset:6400\n"
    "v_pk_max_i16 v136, v136, 0\n"
    "v_mfma_f32_16x16x32_bf16 v[76:79], v[164:167], a[68:71], v[76:79]\n"
    "ds_read_b64_tr_b16 v[160:161], v234 offset:8192\n"
    "v_pk_max_i16 v137, v137, 0\n"
    "v_mfma_f32_16x16x32_bf16 v[68:71], v[164:167], a[64:67], v[68:71]\n"
    "ds_read_b64_tr_b16 v[162:163], v234 offset:8448\n"
    "s_waitcnt lgkmcnt(10)\n"
    "v_mfma_f32_16x16x32_bf16 v[64:67], v[168:171], a[72:75], v[64:67]\n"
    "ds_read_b64_tr_b16 v[164:165], v235 offset:8192\n"
    "v_pk_max_i16 v138, v138, 0\n"
    "v_mfma_f32_16x16x32_bf16 v[72:75], v[168:171], a[76:79], v[72:75]\n"
    "ds_read_b64_tr_b16 v[166:167], v235 offset:8448\n"
    "v_pk_max_i16 v139, v139, 0\n"
    "v_mfma_f32_16x16x32_bf16 v[76:79], v[172:175], a[76:79], v[76:79]\n"
    "ds_read_b64_tr_b16 v[168:169], v234 offset:10240\n"
    "v_cvt_pk_bf16_f32 v140, v120, v121\n"
    "v_mfma_f32_16x16x32_bf16 v[68:71], v[172:175], a[72:75], v[68:71]\n"
    "ds_read_b64_tr_b16 v[170:171], v234 offset:10496\n"
    "s_waitcnt lgkmcnt(10)\n"
    "v_mfma_f32_16x16x32_bf16 v[64:67], v[176:179], a[80:83], v[64:67]\n"
    "ds_read_b64_tr_b16 v[172:173], v235 offset:10240\n"
    "v_mbcnt_lo_u32_b32 v251, -1, 0\n"
    "v_mfma_f32_16x16x32_bf16 v[72:75], v[176:179], a[84:87], v[72:75]\n"
    "ds_read_b64_tr_b16 v[174:175], v235 offset:10496\n"
    "v_mbcnt_hi_u32_b32 v251, -1, v251\n"
    "v_mfma_f32_16x16x32_bf16 v[76:79], v[180:183], a[84:87], v[76:79]\n"
    "ds_read_b64_tr_b16 v[176:177], v234 offset:12288\n"
    "s_add_u32 s89, s75, 0x1c000\n"
    "v_mfma_f32_16x16x32_bf16 v[68:71], v[180:183], a[80:83], v[68:71]\n"
    "ds_read_b64_tr_b16 v[178:179], v234 offset:12544\n"
    "s_waitcnt lgkmcnt(10)\n"
    "v_mfma_f32_16x16x32_bf16 v[64:67], v[184:187], a[88:91], v[64:67]\n"
    "ds_read_b64_tr_b16 v[180:181], v235 offset:12288\n"
    "v_add_u32 v228, s89, v240\n"
    "v_mfma_f32_16x16x32_bf16 v[72:75], v[184:187], a[92:95], v[72:75]\n"
    "ds_read_b64_tr_b16 v[182:183], v235 offset:12544\n"
    "v_and_b32 v249, 15, v251\n"
    "v_mfma_f32_16x16x32_bf16 v[76:79], v[188:191], a[92:95], v[76:79]\n"
    "ds_read_b64_tr_b16 v[184:185], v234 offset:14336\n"
    "v_lshrrev_b32 v250, 4, v251\n"
    "v_mfma_f32_16x16x32_bf16 v[68:71], v[188:191], a[88:91], v[68:71]\n"
    "ds_read_b64_tr_b16 v[186:187], v234 offset:14592\n"
    "s_waitcnt lgkmcnt(10)\n"
    "v_mfma_f32_16x16x32_bf16 v[64:67], v[160:163], a[96:99], v[64:67]\n"
    "ds_read_b64_tr_b16 v[188:189], v235 offset:14336\n"
    "s_waitcnt vmcnt(2)\n"
    "v_mfma_f32_16x16x32_bf16 v[72:75], v[160:163], a[100:103], v[72:75]\n"
    "ds_read_b64_tr_b16 v[190:191], v235 offset:14592\n"
    "s_barrier\n"
    "v_mfma_f32_16x16x32_bf16 v[76:79], v[164:167], a[100:103], v[76:79]\n"
    "ds_read_b64_tr_b16 v[160:161], v234 offset:16384\n"
    "buffer_atomic_add v248, v247, s[44:47], 0 offen\n"
    "v_mfma_f32_16x16x32_bf16 v[68:71], v[164:167], a[96:99], v[68:71]\n"
    "ds_read_b64_tr_b16 v[162:163], v234 offset:16640\n"
    "s_waitcnt lgkmcnt(10)\n"
    "v_mfma_f32_16x16x32_bf16 v[64:67], v[168:171], a[104:107], v[64:67]\n"
    "ds_read_b64_tr_b16 v[164:165], v235 offset:16384\n"
    "buffer_store_dwordx4 v[132:135], v240, s[44:47], s99 offen offset:1024 sc1\n"
    "v_mfma_f32_16x16x32_bf16 v[72:75], v[168:171], a[108:111], v[72:75]\n"
    "ds_read_b64_tr_b16 v[166:167], v235 offset:16640\n"
    "v_lshlrev_b32 v250, 2, v250\n"
    "v_mfma_f32_16x16x32_bf16 v[76:79], v[172:175], a[108:111], v[76:79]\n"
    "ds_read_b64_tr_b16 v[168:169], v234 offset:18432\n"
    "v_sub_u32 v249, v249, v250\n"
    "v_mfma_f32_16x16x32_bf16 v[68:71], v[172:175], a[104:107], v[68:71]\n"
    "ds_read_b64_tr_b16 v[170:171], v234 offset:18688\n"
    "s_waitcnt lgkmcnt(10)\n"
    "v_mfma_f32_16x16x32_bf16 v[64:67], v[176:179], a[112:115], v[64:67]\n"
    "ds_read_b64_tr_b16 v[172:173], v235 offset:18432\n"
    "v_cmp_gt_i32 s[4:5], 0, v249\n"
    "v_mfma_f32_16x16x32_bf16 v[72:75], v[176:179], a[116:119], v[72:75]\n"
    "ds_read_b64_tr_b16 v[174:175], v235 offset:18688\n"
    "v_cmp_gt_i32 s[6:7], 1, v249\n"
    "v_mfma_f32_16x16x32_bf16 v[76:79], v[180:183], a[116:119], v[76:79]\n"
    "ds_read_b64_tr_b16 v[176:177], v234 offset:20480\n"
    "v_cmp_gt_i32 s[8:9], 2, v249\n"
    "v_mfma_f32_16x16x32_bf16 v[68:71], v[180:183], a[112:115], v[68:71]\n"
    "ds_read_b64_tr_b16 v[178:179], v234 offset:20736\n"
    "s_waitcnt lgkmcnt(10)\n"
    "v_mfma_f32_16x16x32_bf16 v[64:67], v[184:187], a[120:123], v[64:67]\n"
    "ds_read_b64_tr_b16 v[180:181], v235 offset:20480\n"
    "v_cmp_gt_i32 s[10:11], 3, v249\n"
    "v_mfma_f32_16x16x32_bf16 v[72:75], v[184:187], a[124:127], v[72:75]\n"
    "ds_read_b64_tr_b16 v[182:183], v235 offset:20736\n"
    "v_mov_b32 v250, 0xffff\n"
    "v_mfma_f32_16x16x32_bf16 v[76:79], v[188:191], a[124:127], v[76:79]\n"
    "ds_read_b64_tr_b16 v[184:185], v234 offset:22528\n"
    "v_mov_b32 v251, 0xffff0000\n"
    "v_mfma_f32_16x16x32_bf16 v[68:71], v[188:191], a[120:123], v[68:71]\n"
    "ds_read_b64_tr_b16 v[186:187], v234 offset:22784\n"
    "s_waitcnt lgkmcnt(10)\n"
    "v_mfma_f32_16x16x32_bf16 v[64:67], v[160:163], a[128:131], v[64:67]\n"
    "ds_read_b64_tr_b16 v[188:189], v235 offset:22528\n"
    "v_cndmask_b32 v229, 0, v250, s[4:5]\n"
    "v_mfma_f32_16x16x32_bf16 v[72:75], v[160:163], a[132:135], v[72:75]\n"
    "ds_read_b64_tr_b16 v[190:191], v235 offset:22784\n"
    "v_cndmask_b32 v249, 0, v251, s[6:7]\n"
    "v_mfma_f32_16x16x32_bf16 v[76:79], v[164:167], a[132:135], v[76:79]\n"
    "ds_read_b64_tr_b16 v[160:161], v234 offset:24576\n"
    "v_or_b32 v229, v229, v249\n"
    "v_mfma_f32_16x16x32_bf16 v[68:71], v[164:167], a[128:131], v[68:71]\n"
    "ds_read_b64_tr_b16 v[162:163], v234 offset:24832\n"
    "s_waitcnt lgkmcnt(10)\n"
    "v_mfma_f32_16x16x32_bf16 v[64:67], v[168:171], a[136:139], v[64:67]\n"
    "ds_read_b64_tr_b16 v[164:165], v235 offset:24576\n"
    "v_cndmask_b32 v230, 0, v250, s[8:9]\n"
    "v_mfma_f32_16x16x32_bf16 v[72:75], v[168:171], a[140:143], v[72:75]\n"
    "ds_read_b64_tr_b16 v[166:167], v235 offset:24832\n"
    "v_cndmask_b32 v249, 0, v251, s[10:11]\n"
    "v_mfma_f32_16x16x32_bf16 v[76:79], v[172:175], a[140:143], v[76:79]\n"
    "ds_read_b64_tr_b16 v[168:169], v234 offset:26624\n"
    "v_or_b32 v230, v230, v249\n"
    "v_mfma_f32_16x16x32_bf16 v[68:71], v[172:175], a[136:139], v[68:71]\n"
    "ds_read_b64_tr_b16 v[170:171], v234 offset:26880\n"
    "s_waitcnt lgkmcnt(10)\n"
    "v_mfma_f32_16x16x32_bf16 v[64:67], v[176:179], a[144:147], v[64:67]\n"
    "ds_read_b64_tr_b16 v[172:173], v235 offset:26624\n"
    "s_lshl_b32 s89, s48, 2\n"
    "v_mfma_f32_16x16x32_bf16 v[72:75], v[176:179], a[148:151], v[72:75]\n"
    "ds_read_b64_tr_b16 v[174:175], v235 offset:26880\n"
    "s_add_u32 s89, s89, s50\n"
    "v_mfma_f32_16x16x32_bf16 v[76:79], v[180:183], a[148:151], v[76:79]\n"
    "ds_read_b64_tr_b16 v[176:177], v234 offset:28672\n"
    "s_sub_i32 s17, 8, s89\n"
    "v_mfma_f32_16x16x32_bf16 v[68:71], v[180:183], a[144:147], v[68:71]\n"
    "ds_read_b64_tr_b16 v[178:179], v234 offset:28928\n"
    "s_waitcnt lgkmcnt(10)\n"
    "v_mfma_f32_16x16x32_bf16 v[64:67], v[184:187], a[152:155], v[64:67]\n"
    "ds_read_b64_tr_b16 v[180:181], v235 offset:28672\n"
    "v_cvt_pk_bf16_f32 v141, v122, v123\n"
    "v_mfma_f32_16x16x32_bf16 v[72:75], v[184:187], a[156:159], v[72:75]\n"
    "ds_read_b64_tr_b16 v[182:183], v235 offset:28928\n"
    "v_cvt_pk_bf16_f32 v142, v124, v125\n"
    "v_mfma_f32_16x16x32_bf16 v[76:79], v[188:191], a[156:159], v[76:79]\n"
    "ds_read_b64_tr_b16 v[184:185], v234 offset:30720\n"
    "v_cvt_pk_bf16_f32 v143, v126, v127\n"
    "v_mfma_f32_16x16x32_bf16 v[68:71], v[188:191], a[152:155], v[68:71]\n"
    "ds_read_b64_tr_b16 v[186:187], v234 offset:30976\n"
    "s_waitcnt lgkmcnt(10)\n"
    "v_mfma_f32_16x16x32_bf16 v[64:67], v[160:163], a[160:163], v[64:67]\n"
    "ds_read_b64_tr_b16 v[188:189], v235 offset:30720\n"
    "s_add_u32 s99, s16, 0x5000\n"
    "v_mfma_f32_16x16x32_bf16 v[72:75], v[160:163], a[164:167], v[72:75]\n"
    "ds_read_b64_tr_b16 v[190:191], v235 offset:30976\n"
    "buffer_store_dwordx4 v[136:139], v240, s[44:47], s99 offen sc1\n"
    "v_mfma_f32_16x16x32_bf16 v[76:79], v[164:167], a[164:167], v[76:79]\n"
    "ds_read_b64_tr_b16 v[160:161], v234 offset:32768\n"
    "v_pk_max_i16 v140, v140, 0\n"
    "v_mfma_f32_16x16x32_bf16 v[68:71], v[164:167], a[160:163], v[68:71]\n"
    "ds_read_b64_tr_b16 v[162:163], v234 offset:33024\n"
    "s_waitcnt lgkmcnt(10)\n"
    "v_mfma_f32_16x16x32_bf16 v[64:67], v[168:171], a[168:171], v[64:67]\n"
    "ds_read_b64_tr_b16 v[164:165], v235 offset:32768\n"
    "v_pk_max_i16 v141, v141, 0\n"
    "v_mfma_f32_16x16x32_bf16 v[72:75], v[168:171], a[172:175], v[72:75]\n"
    "ds_read_b64_tr_b16 v[166:167], v235 offset:33024\n"
    "v_pk_max_i16 v142, v142, 0\n"
    "v_mfma_f32_16x16x32_bf16 v[76:79], v[172:175], a[172:175], v[76:79]\n"
    "ds_read_b64_tr_b16 v[168:169], v234 offset:34816\n"
    "v_pk_max_i16 v143, v143, 0\n"
    "v_mfma_f32_16x16x32_bf16 v[68:71], v[172:175], a[168:171], v[68:71]\n"
    "ds_read_b64_tr_b16 v[170:171], v234 offset:35072\n"
    "s_waitcnt lgkmcnt(10)\n"
    "v_mfma_f32_16x16x32_bf16 v[64:67], v[176:179], a[176:179], v[64:67]\n"
    "ds_read_b64_tr_b16 v[172:173], v235 offset:34816\n"
    "v_mfma_f32_16x16x32_bf16 v[72:75], v[176:179], a[180:183], v[72:75]\n"
    "ds_read_b64_tr_b16 v[174:175], v235 offset:35072\n"
    "v_mfma_f32_16x16x32_bf16 v[76:79], v[180:183], a[180:183], v[76:79]\n"
    "ds_read_b64_tr_b16 v[176:177], v234 offset:36864\n"
    "v_mfma_f32_16x16x32_bf16 v[68:71], v[180:183], a[176:179], v[68:71]\n"
    "ds_read_b64_tr_b16 v[178:179], v234 offset:37120\n"
    "s_waitcnt lgkmcnt(10)\n"
    "v_mfma_f32_16x16x32_bf16 v[64:67], v[184:187], a[184:187], v[64:67]\n"
    "ds_read_b64_tr_b16 v[180:181], v235 offset:36864\n"
    "v_mfma_f32_16x16x32_bf16 v[72:75], v[184:187], a[188:191], v[72:75]\n"
    "ds_read_b64_tr_b16 v[182:183], v235 offset:37120\n"
    "v_mfma_f32_16x16x32_bf16 v[76:79], v[188:191], a[188:191], v[76:79]\n"
    "ds_read_b64_tr_b16 v[184:185], v234 offset:38912\n"
    "v_mfma_f32_16x16x32_bf16 v[68:71], v[188:191], a[184:187], v[68:71]\n"
    "ds_read_b64_tr_b16 v[186:187], v234 offset:39168\n"
    "s_waitcnt lgkmcnt(10)\n"
    "v_mfma_f32_16x16x32_bf16 v[64:67], v[160:163], a[192:195], v[64:67]\n"
    "ds_read_b64_tr_b16 v[188:189], v235 offset:38912\n"
    "v_mfma_f32_16x16x32_bf16 v[72:75], v[160:163], a[196:199], v[72:75]\n"
    "ds_read_b64_tr_b16 v[190:191], v235 offset:39168\n"
    "v_mfma_f32_16x16x32_bf16 v[76:79], v[164:167], a[196:199], v[76:79]\n"
    "ds_read_b64_tr_b16 v[160:161], v234 offset:40960\n"
    "v_mfma_f32_16x16x32_bf16 v[68:71], v[164:167], a[192:195], v[68:71]\n"
    "ds_read_b64_tr_b16 v[162:163], v234 offset:41216\n"
    "s_waitcnt lgkmcnt(10)\n"
    "v_mfma_f32_16x16x32_bf16 v[64:67], v[168:171], a[200:203], v[64:67]\n"
    "ds_read_b64_tr_b16 v[164:165], v235 offset:40960\n"
    "v_mfma_f32_16x16x32_bf16 v[72:75], v[168:171], a[204:207], v[72:75]\n"
    "ds_read_b64_tr_b16 v[166:167], v235 offset:41216\n"
    "v_mfma_f32_16x16x32_bf16 v[76:79], v[172:175], a[204:207], v[76:79]\n"
    "ds_read_b64_tr_b16 v[168:169], v234 offset:43008\n"
    "v_mfma_f32_16x16x32_bf16 v[68:71], v[172:175], a[200:203], v[68:71]\n"
    "ds_read_b64_tr_b16 v[170:171], v234 offset:43264\n"
    "s_waitcnt lgkmcnt(10)\n"
    "v_mfma_f32_16x16x32_bf16 v[64:67], v[176:179], a[208:211], v[64:67]\n"
    "ds_read_b64_tr_b16 v[172:173], v235 offset:43008\n"
    "v_mfma_f32_16x16x32_bf16 v[72:75], v[176:179], a[212:215], v[72:75]\n"
    "ds_read_b64_tr_b16 v[174:175], v235 offset:43264\n"
    "v_mfma_f32_16x16x32_bf16 v[76:79], v[180:183], a[212:215], v[76:79]\n"
    "ds_read_b64_tr_b16 v[176:177], v234 offset:45056\n"
    "v_mfma_f32_16x16x32_bf16 v[68:71], v[180:183], a[208:211], v[68:71]\n"
    "ds_read_b64_tr_b16 v[178:179], v234 offset:45312\n"
    "s_waitcnt lgkmcnt(10)\n"
    "v_mfma_f32_16x16x32_bf16 v[64:67], v[184:187], a[216:219], v[64:67]\n"
    "ds_read_b64_tr_b16 v[180:181], v235 offset:45056\n"
    "buffer_store_dwordx4 v[140:143], v240, s[44:47], s99 offen offset:1024 sc1\n"
    "v_mfma_f32_16x16x32_bf16 v[72:75], v[184:187], a[220:223], v[72:75]\n"
    "ds_read_b64_tr_b16 v[182:183], v235 offset:45312\n"
    "v_mfma_f32_16x16x32_bf16 v[76:79], v[188:191], a[220:223], v[76:79]\n"
    "ds_read_b64_tr_b16 v[184:185], v234 offset:47104\n"
    "v_mfma_f32_16x16x32_bf16 v[68:71], v[188:191], a[216:219], v[68:71]\n"
    "ds_read_b64_tr_b16 v[186:187], v234 offset:47360\n"
    "s_waitcnt lgkmcnt(10)\n"
    "v_mfma_f32_16x16x32_bf16 v[64:67], v[160:163], a[224:227], v[64:67]\n"
    "ds_read_b64_tr_b16 v[188:189], v235 offset:47104\n"
    "v_mfma_f32_16x16x32_bf16 v[72:75], v[160:163], a[228:231], v[72:75]\n"
    "ds_read_b64_tr_b16 v[190:191], v235 offset:47360\n"
    "v_mfma_f32_16x16x32_bf16 v[76:79], v[164:167], a[228:231], v[76:79]\n"
    "v_mfma_f32_16x16x32_bf16 v[68:71], v[164:167], a[224:227], v[68:71]\n"
    "s_waitcnt lgkmcnt(8)\n"
    "v_mfma_f32_16x16x32_bf16 v[64:67], v[168:171], a[232:235], v[64:67]\n"
    "v_mfma_f32_16x16x32_bf16 v[72:75], v[168:171], a[236:239], v[72:75]\n"
    "v_mfma_f32_16x16x32_bf16 v[76:79], v[172:175], a[236:239], v[76:79]\n"
    "v_mfma_f32_16x16x32_bf16 v[68:71], v[172:175], a[232:235], v[68:71]\n"
    "s_waitcnt lgkmcnt(4)\n"
    "v_mfma_f32_16x16x32_bf16 v[64:67], v[176:179], a[240:243], v[64:67]\n"
    "v_mfma_f32_16x16x32_bf16 v[72:75], v[176:179], a[244:247], v[72:75]\n"
    "v_mfma_f32_16x16x32_bf16 v[76:79], v[180:183], a[244:247], v[76:79]\n"
    "v_mfma_f32_16x16x32_bf16 v[68:71], v[180:183], a[240:243], v[68:71]\n"
    "s_waitcnt lgkmcnt(0)\n"
    "v_mfma_f32_16x16x32_bf16 v[64:67], v[184:187], a[248:251], v[64:67]\n"
    "v_mfma_f32_16x16x32_bf16 v[72:75], v[184:187], a[252:255], v[72:75]\n"
    "v_mfma_f32_16x16x32_bf16 v[76:79], v[188:191], a[252:255], v[76:79]\n"
    "v_mfma_f32_16x16x32_bf16 v[68:71], v[188:191], a[248:251], v[68:71]\n"
    "s_and_b32 s89, s49, 1\n"
    "s_mul_i32 s89, s89, 0x5000\n"
    "s_sub_u32 s99, s16, s89\n"
    "s_add_u32 s99, s99, 0xc000\n"
    "s_nop 0\n"
    "v_cvt_pk_bf16_f32 v144, v64, v65\n"
    "v_cvt_pk_bf16_f32 v145, v66, v67\n"
    "s_nop 0\n"
    "v_cvt_pk_bf16_f32 v146, v68, v69\n"
    "v_cvt_pk_bf16_f32 v147, v70, v71\n"
    "v_pk_max_i16 v144, v144, 0\n"
    "v_pk_max_i16 v145, v145, 0\n"
    "v_pk_max_i16 v146, v146, 0\n"
    "v_pk_max_i16 v147, v147, 0\n"
    "v_cvt_pk_bf16_f32 v148, v72, v73\n"
    "v_cvt_pk_bf16_f32 v149, v74, v75\n"
    "v_cvt_pk_bf16_f32 v150, v76, v77\n"
    "v_cvt_pk_bf16_f32 v151, v78, v79\n"
    "v_pk_max_i16 v148, v148, 0\n"
    "v_pk_max_i16 v149, v149, 0\n"
    "v_pk_max_i16 v150, v150, 0\n"
    "v_pk_max_i16 v151, v151, 0\n"
    "buffer_store_dwordx4 v[144:147], v240, s[44:47], s99 offen sc1\n"
    "ds_read_b128 v[176:179], v228\n"
    "ds_read_b128 v[180:183], v228 offset:1024\n"
    "ds_read_b128 v[184:187], v228 offset:2048\n"
    "ds_read_b128 v[188:191], v228 offset:3072\n"
    "s_waitcnt lgkmcnt(2)\n"
    "v_mfma_f32_16x16x32_bf16 v[0:3], v[176:179], v[208:211], 0\n"
    "ds_read_b128 v[176:179], v228 offset:4096\n"
    "v_mfma_f32_16x16x32_bf16 v[4:7], v[180:183], v[208:211], 0\n"
    "v_mfma_f32_16x16x32_bf16 v[76:79], v[180:183], v[212:215], 0\n"
    "s_waitcnt lgkmcnt(1)\n"
    "ds_read_b128 v[180:183], v228 offset:5120\n"
    "v_mfma_f32_16x16x32_bf16 v[8:11], v[184:187], v[208:211], 0\n"
    "s_barrier\n"
    "v_mfma_f32_16x16x32_bf16 v[80:83], v[184:187], v[212:215], 0\n"
    "ds_read_b128 v[184:187], v228 offset:6144\n"
    "v_mfma_f32_16x16x32_bf16 v[12:15], v[188:191], v[208:211], 0\n"
    "v_max3_f32 v168, v4, v5, v6\n"
    "v_max_f32 v168, v168, v7\n"
    "v_mbcnt_lo_u32_b32 v251, -1, 0\n"
    "v_mfma_f32_16x16x32_bf16 v[84:87], v[188:191], v[212:215], 0\n"
    "s_waitcnt lgkmcnt(1)\n"
    "ds_read_b128 v[188:191], v228 offset:7168\n"
    "v_mbcnt_hi_u32_b32 v251, -1, v251\n"
    "v_mfma_f32_16x16x32_bf16 v[16:19], v[176:179], v[208:211], 0\n"
    "v_max3_f32 v168, v168, v8, v9\n"
    "v_max3_f32 v168, v168, v10, v11\n"
    "v_max3_f32 v170, v80, v81, v82\n"
    "v_mfma_f32_16x16x32_bf16 v[88:91], v[176:179], v[212:215], 0\n"
    "ds_read_b128 v[176:179], v228 offset:8192\n"
    "v_max_f32 v170, v170, v83\n"
    "s_lshl_b32 s89, s50, 8\n"
    "v_mfma_f32_16x16x32_bf16 v[20:23], v[180:183], v[208:211], 0\n"
    "v_max3_f32 v168, v168, v12, v13\n"
    "v_max3_f32 v168, v168, v14, v15\n"
    "v_max3_f32 v170, v170, v84, v85\n"
    "v_mfma_f32_16x16x32_bf16 v[92:95], v[180:183], v[212:215], 0\n"
    "s_waitcnt lgkmcnt(1)\n"
    "ds_read_b128 v[180:183], v228 offset:9216\n"
    "v_max3_f32 v170, v170, v86, v87\n"
    "v_mfma_f32_16x16x32_bf16 v[24:27], v[184:187], v[208:211], 0\n"
    "v_max3_f32 v168, v168, v16, v17\n"
    "v_max3_f32 v168, v168, v18, v19\n"
    "v_max3_f32 v170, v170, v88, v89\n"
    "v_mfma_f32_16x16x32_bf16 v[96:99], v[184:187], v[212:215], 0\n"
    "ds_read_b128 v[184:187], v228 offset:10240\n"
    "v_max3_f32 v170, v170, v90, v91\n"
    "s_add_u32 s89, s89, 0\n"
    "v_mfma_f32_16x16x32_bf16 v[28:31], v[188:191], v[208:211], 0\n"
    "v_max3_f32 v168, v168, v20, v21\n"
    "v_max3_f32 v168, v168, v22, v23\n"
    "v_max3_f32 v170, v170, v92, v93\n"
    "v_mfma_f32_16x16x32_bf16 v[100:103], v[188:191], v[212:215], 0\n"
    "s_waitcnt lgkmcnt(1)\n"
    "ds_read_b128 v[188:191], v228 offset:11264\n"
    "v_max3_f32 v170, v170, v94, v95\n"
    "v_mfma_f32_16x16x32_bf16 v[32:35], v[176:179], v[208:211], 0\n"
    "v_max3_f32 v168, v168, v24, v25\n"
    "v_max3_f32 v168, v168, v26, v27\n"
    "v_max3_f32 v170, v170, v96, v97\n"
    "v_mfma_f32_16x16x32_bf16 v[104:107], v[176:179], v[212:215], 0\n"
    "ds_read_b128 v[176:179], v228 offset:12288\n"
    "v_max3_f32 v170, v170, v98, v99\n"
    "v_mov_b32 v231, s89\n"
    "v_mfma_f32_16x16x32_bf16 v[36:39], v[180:183], v[208:211], 0\n"
    "v_max3_f32 v168, v168, v28, v29\n"
    "v_max3_f32 v168, v168, v30, v31\n"
    "v_max3_f32 v170, v170, v100, v101\n"
    "v_mfma_f32_16x16x32_bf16 v[108:111], v[180:183], v[212:215], 0\n"
    "s_waitcnt lgkmcnt(1)\n"
    "ds_read_b128 v[180:183], v228 offset:13312\n"
    "v_max3_f32 v170, v170, v102, v103\n"
    "v_mfma_f32_16x16x32_bf16 v[40:43], v[184:187], v[208:211], 0\n"
    "v_max3_f32 v168, v168, v32, v33\n"
    "v_max3_f32 v168, v168, v34, v35\n"
    "v_max3_f32 v170, v170, v104, v105\n"
    "v_mfma_f32_16x16x32_bf16 v[112:115], v[184:187], v[212:215], 0\n"
    "ds_read_b128 v[184:187], v228 offset:14336\n"
    "v_max3_f32 v170, v170, v106, v107\n"
    "v_lshl_add_u32 v250, v251, 2, v231\n"
    "v_mfma_f32_16x16x32_bf16 v[44:47], v[188:191], v[208:211], 0\n"
    "v_max3_f32 v168, v168, v36, v37\n"
    "v_max3_f32 v168, v168, v38, v39\n"
    "v_max3_f32 v170, v170, v108, v109\n"
    "v_mfma_f32_16x16x32_bf16 v[116:119], v[188:191], v[212:215], 0\n"
    "s_waitcnt lgkmcnt(1)\n"
    "ds_read_b128 v[188:191], v228 offset:15360\n"
    "v_max3_f32 v170, v170, v110, v111\n"
    "v_mfma_f32_16x16x32_bf16 v[48:51], v[176:179], v[208:211], 0\n"
    "v_max3_f32 v168, v168, v40, v41\n"
    "v_max3_f32 v168, v168, v42, v43\n"
    "v_max3_f32 v170, v170, v112, v113\n"
    "v_mfma_f32_16x16x32_bf16 v[120:123], v[176:179], v[212:215], 0\n"
    "ds_read_b128 v[176:179], v228 offset:16384\n"
    "v_max3_f32 v170, v170, v114, v115\n"
    "v_lshrrev_b32 v251, 2, v251\n"
    "v_mfma_f32_16x16x32_bf16 v[52:55], v[180:183], v[208:211], 0\n"
    "v_max3_f32 v168, v168, v44, v45\n"
    "v_max3_f32 v168, v168, v46, v47\n"
    "v_max3_f32 v170, v170, v116, v117\n"
    "v_mfma_f32_16x16x32_bf16 v[124:127], v[180:183], v[212:215], 0\n"
    "s_waitcnt lgkmcnt(1)\n"
    "ds_read_b128 v[180:183], v228 offset:17408\n"
    "v_max3_f32 v170, v170, v118, v119\n"
    "v_mfma_f32_16x16x32_bf16 v[56:59], v[184:187], v[208:211], 0\n"
    "v_max3_f32 v168, v168, v48, v49\n"
    "v_max3_f32 v168, v168, v50, v51\n"
    "v_max3_f32 v170, v170, v120, v121\n"
    "v_mfma_f32_16x16x32_bf16 v[128:131], v[184:187], v[212:215], 0\n"
    "v_max3_f32 v170, v170, v122, v123\n"
    "v_cmp_le_i32 vcc, s17, v251\n"
    "v_mov_b32 v249, 0x3f803f80\n"
    "v_mfma_f32_16x16x32_bf16 v[60:63], v[188:191], v[208:211], 0\n"
    "v_max3_f32 v168, v168, v52, v53\n"
    "v_max3_f32 v168, v168, v54, v55\n"
    "v_max3_f32 v170, v170, v124, v125\n"
    "v_mfma_f32_16x16x32_bf16 v[132:135], v[188:191], v[212:215], 0\n"
    "s_waitcnt lgkmcnt(0)\n"
    "v_max3_f32 v170, v170, v126, v127\n"
    "v_cndmask_b32 v249, 0, v249, vcc\n"
    "v_mfma_f32_16x16x32_bf16 v[64:67], v[176:179], v[208:211], 0\n"
    "v_max3_f32 v168, v168, v56, v57\n"
    "v_max3_f32 v168, v168, v58, v59\n"
    "v_max3_f32 v170, v170, v128, v129\n"
    "v_mfma_f32_16x16x32_bf16 v[136:139], v[176:179], v[212:215], 0\n"
    "v_max3_f32 v170, v170, v130, v131\n"
    "ds_write_b32 v250, v249\n"
    "buffer_store_dwordx4 v[148:151], v240, s[44:47], s99 offen offset:1024 sc1\n"
    "v_mfma_f32_16x16x32_bf16 v[140:143], v[180:183], v[212:215], 0\n"
    "v_max3_f32 v168, v168, v60, v61\n"
    "v_max3_f32 v168, v168, v62, v63\n"
    "v_max3_f32 v170, v170, v132, v133\n"
    "v_max3_f32 v170, v170, v134, v135\n"
    "v_max3_f32 v170, v170, v136, v137\n"
    "v_max3_f32 v170, v170, v138, v139\n"
    "v_cndmask_b32 v0, v64, v0, s[4:5]\n"
    "v_cndmask_b32 v1, v65, v1, s[6:7]\n"
    "v_cndmask_b32 v2, v66, v2, s[8:9]\n"
    "v_cndmask_b32 v3, v67, v3, s[10:11]\n"
    "v_max3_f32 v168, v168, v0, v1\n"
    "v_max3_f32 v168, v168, v2, v3\n"
    "v_cndmask_b32 v76, v140, v76, s[4:5]\n"
    "v_cndmask_b32 v77, v141, v77, s[6:7]\n"
    "v_cndmask_b32 v78, v142, v78, s[8:9]\n"
    "v_cndmask_b32 v79, v143, v79, s[10:11]\n"
    "v_max3_f32 v170, v170, v76, v77\n"
    "v_max3_f32 v170, v170, v78, v79\n"
    "v_mov_b32 v169, v168\n"
    "v_mov_b32 v171, v170\n"
    "ds_read_b128 v[176:179], v228 offset:24576\n"
    "ds_read_b128 v[180:183], v228 offset:25600\n"
    "v_permlane16_swap_b32 v168, v169\n"
    "v_permlane16_swap_b32 v170, v171\n"
    "v_max_f32 v168, v168, v169\n"
    "v_max_f32 v170, v170, v171\n"
    "v_mov_b32 v169, v168\n"
    "v_mov_b32 v171, v170\n"
    "ds_read_b128 v[232:235], v231\n"
    "v_permlane32_swap_b32 v168, v169\n"
    "v_permlane32_swap_b32 v170, v171\n"
    "v_max_f32 v168, v168, v169\n"
    "v_max_f32 v170, v170, v171\n"
    "s_add_u32 m0, s75, 0x10000\n"
    "v_pk_add_f32 v[0:1], v[0:1], v[168:169] op_sel_hi:[1,0] neg_lo:[0,1] neg_hi:[0,1]\n"
    "v_pk_add_f32 v[2:3], v[2:3], v[168:169] op_sel_hi:[1,0] neg_lo:[0,1] neg_hi:[0,1]\n"
    "v_pk_add_f32 v[4:5], v[4:5], v[168:169] op_sel_hi:[1,0] neg_lo:[0,1] neg_hi:[0,1]\n"
    "s_add_u32 s98, s72, 0x20000\n"
    "v_pk_add_f32 v[6:7], v[6:7], v[168:169] op_sel_hi:[1,0] neg_lo:[0,1] neg_hi:[0,1]\n"
    "v_pk_add_f32 v[76:77], v[76:77], v[170:171] op_sel_hi:[1,0] neg_lo:[0,1] neg_hi:[0,1]\n"
    "buffer_load_dwordx4 v252, s[44:47], s98 offen sc1 lds\n"
    "v_pk_add_f32 v[78:79], v[78:79], v[170:171] op_sel_hi:[1,0] neg_lo:[0,1] neg_hi:[0,1]\n"
    "v_exp_f32 v0, v0\n"
    "v_exp_f32 v1, v1\n"
    "buffer_load_dwordx4 v253, s[44:47], s98 offen offset:1024 sc1 lds\n"
    "v_exp_f32 v2, v2\n"
    "v_exp_f32 v3, v3\n"
    "s_add_u32 m0, s75, 0x12000\n"
    "v_exp_f32 v4, v4\n"
    "v_exp_f32 v5, v5\n"
    "v_exp_f32 v6, v6\n"
    "s_add_u32 s98, s73, 0x20000\n"
    "v_exp_f32 v7, v7\n"
    "v_exp_f32 v76, v76\n"
    "buffer_load_dwordx4 v207, s[44:47], s98 offen sc1 lds\n"
    "v_exp_f32 v77, v77\n"
    "v_exp_f32 v78, v78\n"
    "v_exp_f32 v79, v79\n"
    "buffer_load_dwordx4 v242, s[44:47], s98 offen offset:1024 sc1 lds\n"
    "v_cvt_pk_bf16_f32 v172, v0, v1\n"
    "v_cvt_pk_bf16_f32 v173, v2, v3\n"
    "s_add_u32 m0, s75, 0x14000\n"
    "v_cvt_pk_bf16_f32 v2, v4, v5\n"
    "v_cvt_pk_bf16_f32 v3, v6, v7\n"
    "s_add_u32 s98, s74, 0x20000\n"
    "v_and_b32 v0, v172, v229\n"
    "v_and_b32 v1, v173, v230\n"
    "v_bfi_b32 v64, v229, 0, v172\n"
    "buffer_load_dwordx4 v240, s[44:47], s98 offen sc1 lds\n"
    "v_bfi_b32 v65, v230, 0, v173\n"
    "v_mov_b32 v66, 0\n"
    "buffer_load_dwordx4 v240, s[44:47], s98 offen offset:1024 sc1 lds\n"
    "v_mov_b32 v67, 0\n"
    "v_mov_b32 v72, 0\n"
    "v_mov_b32 v73, 0\n"
    "s_add_u32 m0, s75, 0x16000\n"
    "v_cvt_pk_bf16_f32 v174, v76, v77\n"
    "v_cvt_pk_bf16_f32 v175, v78, v79\n"
    "s_add_u32 s98, s76, 0x420000\n"
    "v_and_b32 v74, v174, v229\n"
    "v_and_b32 v75, v175, v230\n"
    "v_pk_add_f32 v[8:9], v[8:9], v[168:169] op_sel_hi:[1,0] neg_lo:[0,1] neg_hi:[0,1]\n"
    "buffer_load_dwordx4 v240, s[44:47], s98 offen sc1 lds\n"
    "v_pk_add_f32 v[10:11], v[10:11], v[168:169] op_sel_hi:[1,0] neg_lo:[0,1] neg_hi:[0,1]\n"
    "v_pk_add_f32 v[12:13], v[12:13], v[168:169] op_sel_hi:[1,0] neg_lo:[0,1] neg_hi:[0,1]\n"
    "buffer_load_dwordx4 v240, s[44:47], s98 offen offset:1024 sc1 lds\n"
    "v_pk_add_f32 v[14:15], v[14:15], v[168:169] op_sel_hi:[1,0] neg_lo:[0,1] neg_hi:[0,1]\n"
    "v_pk_add_f32 v[80:81], v[80:81], v[170:171] op_sel_hi:[1,0] neg_lo:[0,1] neg_hi:[0,1]\n"
    "s_add_u32 m0, s75, 0x18000\n"
    "v_pk_add_f32 v[82:83], v[82:83], v[170:171] op_sel_hi:[1,0] neg_lo:[0,1] neg_hi:[0,1]\n"
    "v_pk_add_f32 v[84:85], v[84:85], v[170:171] op_sel_hi:[1,0] neg_lo:[0,1] neg_hi:[0,1]\n"
    "v_pk_add_f32 v[86:87], v[86:87], v[170:171] op_sel_hi:[1,0] neg_lo:[0,1] neg_hi:[0,1]\n"
    "s_add_u32 s98, s77, 0x420000\n"
    "s_waitcnt lgkmcnt(0)\n"
    "v_mfma_f32_16x16x32_bf16 v[144:147], v[176:179], v[0:3], 0\n"
    "buffer_load_dwordx4 v240, s[44:47], s98 offen sc1 lds\n"
    "ds_read_b128 v[184:187], v228 offset:26624\n"
    "ds_read_b128 v[188:191], v228 offset:27648\n"
    "ds_read_b128 v[236:239], v231 offset:16\n"
    "buffer_load_dwordx4 v240, s[44:47], s98 offen offset:1024 sc1 lds\n"
    "v_mfma_f32_16x16x32_bf16 v[148:151], v[180:183], v[0:3], 0\n"
    "v_exp_f32 v8, v8\n"
    "s_add_u32 m0, s75, 0x1a000\n"
    "v_exp_f32 v9, v9\n"
    "v_mfma_f32_16x16x32_bf16 v[160:163], v[232:235], v[0:3], 0\n"
    "v_exp_f32 v10, v10\n"
    "s_add_u32 s98, s74, 0x420000\n"
    "v_exp_f32 v11, v11\n"
    "v_mfma_f32_16x16x32_bf16 v[152:155], v[176:179], v[72:75], 0\n"
    "buffer_load_dwordx4 v240, s[44:47], s98 offen sc1 lds\n"
    "v_exp_f32 v12, v12\n"
    "v_exp_f32 v13, v13\n"
    "buffer_load_dwordx4 v240, s[44:47], s98 offen offset:1024 sc1 lds\n"
    "v_exp_f32 v14, v14\n"
    "v_mfma_f32_16x16x32_bf16 v[156:159], v[180:183], v[72:75], 0\n"
    "v_exp_f32 v15, v15\n"
    "buffer_load_dword v246, v245, s[44:47], 0 offen sc1\n"
    "v_exp_f32 v80, v80\n"
    "v_mfma_f32_16x16x32_bf16 v[164:167], v[232:235], v[72:75], 0\n"
    "s_getpc_b64 s[92:93]\n"
    ".Lct8:\n"
    "v_exp_f32 v81, v81\n"
    "v_exp_f32 v82, v82\n"
    "v_exp_f32 v83, v83\n"
    "v_exp_f32 v84, v84\n"
    "v_exp_f32 v85, v85\n"
    "s_and_b32 s93, s93, 0xffff\n"
    "v_exp_f32 v86, v86\n"
    "v_exp_f32 v87, v87\n"
    "v_cvt_pk_bf16_f32 v8, v8, v9\n"
    "s_mov_b32 s94, .Lend-.Lct8\n"
    "v_cvt_pk_bf16_f32 v9, v10, v11\n"
    "v_cvt_pk_bf16_f32 v10, v12, v13\n"
    "s_min_u32 s94, s94, 0x3000\n"
    "v_cvt_pk_bf16_f32 v11, v14, v15\n"
    "v_cvt_pk_bf16_f32 v80, v80, v81\n"
    "s_mov_b32 s95, 0x20000\n"
    "v_cvt_pk_bf16_f32 v81, v82, v83\n"
    "v_cvt_pk_bf16_f32 v82, v84, v85\n"
    "v_cvt_pk_bf16_f32 v83, v86, v87\n"
    "s_and_b32 s91, s13, 0x7f\n"
    "v_pk_add_f32 v[16:17], v[16:17], v[168:169] op_sel_hi:[1,0] neg_lo:[0,1] neg_hi:[0,1]\n"
    "v_pk_add_f32 v[18:19], v[18:19], v[168:169] op_sel_hi:[1,0] neg_lo:[0,1] neg_hi:[0,1]\n"
    "s_lshl_b32 s91, s91, 10\n"
    "v_pk_add_f32 v[20:21], v[20:21], v[168:169] op_sel_hi:[1,0] neg_lo:[0,1] neg_hi:[0,1]\n"
    "v_pk_add_f32 v[22:23], v[22:23], v[168:169] op_sel_hi:[1,0] neg_lo:[0,1] neg_hi:[0,1]\n"
    "v_pk_add_f32 v[88:89], v[88:89], v[170:171] op_sel_hi:[1,0] neg_lo:[0,1] neg_hi:[0,1]\n"
    "buffer_load_dword v206, v240, s[92:95], s91 offen\n"
    "v_pk_add_f32 v[90:91], v[90:91], v[170:171] op_sel_hi:[1,0] neg_lo:[0,1] neg_hi:[0,1]\n"
    "v_pk_add_f32 v[92:93], v[92:93], v[170:171] op_sel_hi:[1,0] neg_lo:[0,1] neg_hi:[0,1]\n"
    "v_mbcnt_lo_u32_b32 v251, -1, 0\n"
    "v_pk_add_f32 v[94:95], v[94:95], v[170:171] op_sel_hi:[1,0] neg_lo:[0,1] neg_hi:[0,1]\n"
    "s_waitcnt lgkmcnt(0)\n"
    "v_mfma_f32_16x16x32_bf16 v[144:147], v[184:187], v[8:11], v[144:147]\n"
    "v_mbcnt_hi_u32_b32 v251, -1, v251\n"
    "ds_read_b128 v[176:179], v228 offset:28672\n"
    "ds_read_b128 v[180:183], v228 offset:29696\n"
    "v_lshlrev_b32 v249, 7, v251\n"
    "ds_read_b128 v[232:235], v231 offset:32\n"
    "v_mfma_f32_16x16x32_bf16 v[148:151], v[188:191], v[8:11], v[148:151]\n"
    "v_mov_b32 v250, 0x80000000\n"
    "v_exp_f32 v16, v16\n"
    "v_exp_f32 v17, v17\n"
    "v_mfma_f32_16x16x32_bf16 v[160:163], v[236:239], v[8:11], v[160:163]\n"
    "v_cmp_gt_u32 vcc, 56, v251\n"
    "v_exp_f32 v18, v18\n"
    "v_exp_f32 v19, v19\n"
    "v_cndmask_b32 v204, v250, v249, vcc\n"
    "v_mfma_f32_16x16x32_bf16 v[152:155], v[184:187], v[80:83], v[152:155]\n"
    "v_exp_f32 v20, v20\n"
    "v_exp_f32 v21, v21\n"
    "s_mul_i32 s89, s13, 0x1c00\n"
    "v_exp_f32 v22, v22\n"
    "v_mfma_f32_16x16x32_bf16 v[156:159], v[188:191], v[80:83], v[156:159]\n"
    "s_mul_i32 s90, s51, 0x1c0000\n"
    "v_exp_f32 v23, v23\n"
    "v_exp_f32 v88, v88\n"
    "v_mfma_f32_16x16x32_bf16 v[164:167], v[236:239], v[80:83], v[164:167]\n"
    "s_add_u32 s89, s89, s90\n"
    "v_exp_f32 v89, v89\n"
    "v_exp_f32 v90, v90\n"
    "buffer_load_dword v206, v204, s[32:35], s89 offen\n"
    "v_exp_f32 v91, v91\n"
    "v_exp_f32 v92, v92\n"
    "v_exp_f32 v93, v93\n"
    "v_exp_f32 v94, v94\n"
    "v_exp_f32 v95, v95\n"
    "v_cvt_pk_bf16_f32 v16, v16, v17\n"
    "v_cvt_pk_bf16_f32 v17, v18, v19\n"
    "v_cvt_pk_bf16_f32 v18, v20, v21\n"
    "v_cvt_pk_bf16_f32 v19, v22, v23\n"
    "v_cvt_pk_bf16_f32 v88, v88, v89\n"
    "v_cvt_pk_bf16_f32 v89, v90, v91\n"
    "v_cvt_pk_bf16_f32 v90, v92, v93\n"
    "v_cvt_pk_bf16_f32 v91, v94, v95\n"
    "v_pk_add_f32 v[24:25], v[24:25], v[168:169] op_sel_hi:[1,0] neg_lo:[0,1] neg_hi:[0,1]\n"
    "v_pk_add_f32 v[26:27], v[26:27], v[168:169] op_sel_hi:[1,0] neg_lo:[0,1] neg_hi:[0,1]\n"
    "v_pk_add_f32 v[28:29], v[28:29], v[168:169] op_sel_hi:[1,0] neg_lo:[0,1] neg_hi:[0,1]\n"
    "v_pk_add_f32 v[30:31], v[30:31], v[168:169] op_sel_hi:[1,0] neg_lo:[0,1] neg_hi:[0,1]\n"
    "v_pk_add_f32 v[96:97], v[96:97], v[170:171] op_sel_hi:[1,0] neg_lo:[0,1] neg_hi:[0,1]\n"
    "v_pk_add_f32 v[98:99], v[98:99], v[170:171] op_sel_hi:[1,0] neg_lo:[0,1] neg_hi:[0,1]\n"
    "v_pk_add_f32 v[100:101], v[100:101], v[170:171] op_sel_hi:[1,0] neg_lo:[0,1] neg_hi:[0,1]\n"
    "v_pk_add_f32 v[102:103], v[102:103], v[170:171] op_sel_hi:[1,0] neg_lo:[0,1] neg_hi:[0,1]\n"
    "s_waitcnt lgkmcnt(0)\n"
    "v_mfma_f32_16x16x32_bf16 v[144:147], v[176:179], v[16:19], v[144:147]\n"
    "ds_read_b128 v[184:187], v228 offset:30720\n"
    "ds_read_b128 v[188:191], v228 offset:31744\n"
    "ds_read_b128 v[236:239], v231 offset:48\n"
    "v_mfma_f32_16x16x32_bf16 v[148:151], v[180:183], v[16:19], v[148:151]\n"
    "v_exp_f32 v24, v24\n"
    "v_exp_f32 v25, v25\n"
    "v_mfma_f32_16x16x32_bf16 v[160:163], v[232:235], v[16:19], v[160:163]\n"
    "v_exp_f32 v26, v26\n"
    "v_exp_f32 v27, v27\n"
    "v_mfma_f32_16x16x32_bf16 v[152:155], v[176:179], v[88:91], v[152:155]\n"
    "v_exp_f32 v28, v28\n"
    "v_exp_f32 v29, v29\n"
    "v_exp_f32 v30, v30\n"
    "v_mfma_f32_16x16x32_bf16 v[156:159], v[180:183], v[88:91], v[156:159]\n"
    "v_exp_f32 v31, v31\n"
    "v_exp_f32 v96, v96\n"
    "v_mfma_f32_16x16x32_bf16 v[164:167], v[232:235], v[88:91], v[164:167]\n"
    "v_exp_f32 v97, v97\n"
    "v_exp_f32 v98, v98\n"
    "v_exp_f32 v99, v99\n"
    "v_exp_f32 v100, v100\n"
    "v_exp_f32 v101, v101\n"
    "v_exp_f32 v102, v102\n"
    "v_exp_f32 v103, v103\n"
    "v_cvt_pk_bf16_f32 v24, v24, v25\n"
    "v_cvt_pk_bf16_f32 v25, v26, v27\n"
    "v_cvt_pk_bf16_f32 v26, v28, v29\n"
    "v_cvt_pk_bf16_f32 v27, v30, v31\n"
    "v_cvt_pk_bf16_f32 v96, v96, v97\n"
    "v_cvt_pk_bf16_f32 v97, v98, v99\n"
    "v_cvt_pk_bf16_f32 v98, v100, v101\n"
    "v_cvt_pk_bf16_f32 v99, v102, v103\n"
    "v_pk_add_f32 v[32:33], v[32:33], v[168:169] op_sel_hi:[1,0] neg_lo:[0,1] neg_hi:[0,1]\n"
    "v_pk_add_f32 v[34:35], v[34:35], v[168:169] op_sel_hi:[1,0] neg_lo:[0,1] neg_hi:[0,1]\n"
    "v_pk_add_f32 v[36:37], v[36:37], v[168:169] op_sel_hi:[1,0] neg_lo:[0,1] neg_hi:[0,1]\n"
    "v_pk_add_f32 v[38:39], v[38:39], v[168:169] op_sel_hi:[1,0] neg_lo:[0,1] neg_hi:[0,1]\n"
    "v_pk_add_f32 v[104:105], v[104:105], v[170:171] op_sel_hi:[1,0] neg_lo:[0,1] neg_hi:[0,1]\n"
    "v_pk_add_f32 v[106:107], v[106:107], v[170:171] op_sel_hi:[1,0] neg_lo:[0,1] neg_hi:[0,1]\n"
    "v_pk_add_f32 v[108:109], v[108:109], v[170:171] op_sel_hi:[1,0] neg_lo:[0,1] neg_hi:[0,1]\n"
    "v_pk_add_f32 v[110:111], v[110:111], v[170:171] op_sel_hi:[1,0] neg_lo:[0,1] neg_hi:[0,1]\n"
    "s_waitcnt lgkmcnt(0)\n"
    "v_mfma_f32_16x16x32_bf16 v[144:147], v[184:187], v[24:27], v[144:147]\n"
    "ds_read_b128 v[176:179], v228 offset:32768\n"
    "ds_read_b128 v[180:183], v228 offset:33792\n"
    "ds_read_b128 v[232:235], v231 offset:64\n"
    "v_mfma_f32_16x16x32_bf16 v[148:151], v[188:191], v[24:27], v[148:151]\n"
    "v_exp_f32 v32, v32\n"
    "v_exp_f32 v33, v33\n"
    "v_mfma_f32_16x16x32_bf16 v[160:163], v[236:239], v[24:27], v[160:163]\n"
    "v_exp_f32 v34, v34\n"
    "v_exp_f32 v35, v35\n"
    "v_mfma_f32_16x16x32_bf16 v[152:155], v[184:187], v[96:99], v[152:155]\n"
    "v_exp_f32 v36, v36\n"
    "v_exp_f32 v37, v37\n"
    "v_exp_f32 v38, v38\n"
    "v_mfma_f32_16x16x32_bf16 v[156:159], v[188:191], v[96:99], v[156:159]\n"
    "v_exp_f32 v39, v39\n"
    "v_exp_f32 v104, v104\n"
    "v_mfma_f32_16x16x32_bf16 v[164:167], v[236:239], v[96:99], v[164:167]\n"
    "v_exp_f32 v105, v105\n"
    "v_exp_f32 v106, v106\n"
    "v_exp_f32 v107, v107\n"
    "v_exp_f32 v108, v108\n"
    "v_exp_f32 v109, v109\n"
    "v_exp_f32 v110, v110\n"
    "v_exp_f32 v111, v111\n"
    "v_cvt_pk_bf16_f32 v32, v32, v33\n"
    "v_cvt_pk_bf16_f32 v33, v34, v35\n"
    "v_cvt_pk_bf16_f32 v34, v36, v37\n"
    "v_cvt_pk_bf16_f32 v35, v38, v39\n"
    "v_cvt_pk_bf16_f32 v104, v104, v105\n"
    "v_cvt_pk_bf16_f32 v105, v106, v107\n"
    "v_cvt_pk_bf16_f32 v106, v108, v109\n"
    "v_cvt_pk_bf16_f32 v107, v110, v111\n"
    "v_pk_add_f32 v[40:41], v[40:41], v[168:169] op_sel_hi:[1,0] neg_lo:[0,1] neg_hi:[0,1]\n"
    "v_pk_add_f32 v[42:43], v[42:43], v[168:169] op_sel_hi:[1,0] neg_lo:[0,1] neg_hi:[0,1]\n"
    "v_pk_add_f32 v[44:45], v[44:45], v[168:169] op_sel_hi:[1,0] neg_lo:[0,1] neg_hi:[0,1]\n"
    "v_pk_add_f32 v[46:47], v[46:47], v[168:169] op_sel_hi:[1,0] neg_lo:[0,1] neg_hi:[0,1]\n"
    "v_pk_add_f32 v[112:113], v[112:113], v[170:171] op_sel_hi:[1,0] neg_lo:[0,1] neg_hi:[0,1]\n"
    "v_pk_add_f32 v[114:115], v[114:115], v[170:171] op_sel_hi:[1,0] neg_lo:[0,1] neg_hi:[0,1]\n"
    "v_pk_add_f32 v[116:117], v[116:117], v[170:171] op_sel_hi:[1,0] neg_lo:[0,1] neg_hi:[0,1]\n"
    "v_pk_add_f32 v[118:119], v[118:119], v[170:171] op_sel_hi:[1,0] neg_lo:[0,1] neg_hi:[0,1]\n"
    "s_waitcnt lgkmcnt(0)\n"
    "v_mfma_f32_16x16x32_bf16 v[144:147], v[176:179], v[32:35], v[144:147]\n"
    "ds_read_b128 v[184:187], v228 offset:34816\n"
    "ds_read_b128 v[188:191], v228 offset:35840\n"
    "ds_read_b128 v[236:239], v231 offset:80\n"
    "v_mfma_f32_16x16x32_bf16 v[148:151], v[180:183], v[32:35], v[148:151]\n"
    "v_exp_f32 v40, v40\n"
    "v_exp_f32 v41, v41\n"
    "v_mfma_f32_16x16x32_bf16 v[160:163], v[232:235], v[32:35], v[160:163]\n"
    "v_exp_f32 v42, v42\n"
    "v_exp_f32 v43, v43\n"
    "v_mfma_f32_16x16x32_bf16 v[152:155], v[176:179], v[104:107], v[152:155]\n"
    "v_exp_f32 v44, v44\n"
    "v_exp_f32 v45, v45\n"
    "v_exp_f32 v46, v46\n"
    "v_mfma_f32_16x16x32_bf16 v[156:159], v[180:183], v[104:107], v[156:159]\n"
    "v_exp_f32 v47, v47\n"
    "v_exp_f32 v112, v112\n"
    "v_mfma_f32_16x16x32_bf16 v[164:167], v[232:235], v[104:107], v[164:167]\n"
    "v_exp_f32 v113, v113\n"
    "v_exp_f32 v114, v114\n"
    "v_exp_f32 v115, v115\n"
    "v_exp_f32 v116, v116\n"
    "v_exp_f32 v117, v117\n"
    "v_exp_f32 v118, v118\n"
    "v_exp_f32 v119, v119\n"
    "v_cvt_pk_bf16_f32 v40, v40, v41\n"
    "v_cvt_pk_bf16_f32 v41, v42, v43\n"
    "v_cvt_pk_bf16_f32 v42, v44, v45\n"
    "v_cvt_pk_bf16_f32 v43, v46, v47\n"
    "v_cvt_pk_bf16_f32 v112, v112, v113\n"
    "v_cvt_pk_bf16_f32 v113, v114, v115\n"
    "v_cvt_pk_bf16_f32 v114, v116, v117\n"
    "v_cvt_pk_bf16_f32 v115, v118, v119\n"
    "v_pk_add_f32 v[48:49], v[48:49], v[168:169] op_sel_hi:[1,0] neg_lo:[0,1] neg_hi:[0,1]\n"
    "v_pk_add_f32 v[50:51], v[50:51], v[168:169] op_sel_hi:[1,0] neg_lo:[0,1] neg_hi:[0,1]\n"
    "v_pk_add_f32 v[52:53], v[52:53], v[168:169] op_sel_hi:[1,0] neg_lo:[0,1] neg_hi:[0,1]\n"
    "v_pk_add_f32 v[54:55], v[54:55], v[168:169] op_sel_hi:[1,0] neg_lo:[0,1] neg_hi:[0,1]\n"
    "v_pk_add_f32 v[120:121], v[120:121], v[170:171] op_sel_hi:[1,0] neg_lo:[0,1] neg_hi:[0,1]\n"
    "v_pk_add_f32 v[122:123], v[122:123], v[170:171] op_sel_hi:[1,0] neg_lo:[0,1] neg_hi:[0,1]\n"
    "v_pk_add_f32 v[124:125], v[124:125], v[170:171] op_sel_hi:[1,0] neg_lo:[0,1] neg_hi:[0,1]\n"
    "v_pk_add_f32 v[126:127], v[126:127], v[170:171] op_sel_hi:[1,0] neg_lo:[0,1] neg_hi:[0,1]\n"
    "s_waitcnt lgkmcnt(0)\n"
    "v_mfma_f32_16x16x32_bf16 v[144:147], v[184:187], v[40:43], v[144:147]\n"
    "ds_read_b128 v[176:179], v228 offset:36864\n"
    "ds_read_b128 v[180:183], v228 offset:37888\n"
    "ds_read_b128 v[232:235], v231 offset:96\n"
    "v_mfma_f32_16x16x32_bf16 v[148:151], v[188:191], v[40:43], v[148:151]\n"
    "v_exp_f32 v48, v48\n"
    "v_exp_f32 v49, v49\n"
    "v_mfma_f32_16x16x32_bf16 v[160:163], v[236:239], v[40:43], v[160:163]\n"
    "v_exp_f32 v50, v50\n"
    "v_exp_f32 v51, v51\n"
    "v_mfma_f32_16x16x32_bf16 v[152:155], v[184:187], v[112:115], v[152:155]\n"
    "v_exp_f32 v52, v52\n"
    "v_exp_f32 v53, v53\n"
    "v_exp_f32 v54, v54\n"
    "v_mfma_f32_16x16x32_bf16 v[156:159], v[188:191], v[112:115], v[156:159]\n"
    "v_exp_f32 v55, v55\n"
    "v_exp_f32 v120, v120\n"
    "v_mfma_f32_16x16x32_bf16 v[164:167], v[236:239], v[112:115], v[164:167]\n"
    "v_exp_f32 v121, v121\n"
    "v_exp_f32 v122, v122\n"
    "v_exp_f32 v123, v123\n"
    "v_exp_f32 v124, v124\n"
    "v_exp_f32 v125, v125\n"
    "v_exp_f32 v126, v126\n"
    "v_exp_f32 v127, v127\n"
    "v_cvt_pk_bf16_f32 v48, v48, v49\n"
    "v_cvt_pk_bf16_f32 v49, v50, v51\n"
    "v_cvt_pk_bf16_f32 v50, v52, v53\n"
    "v_cvt_pk_bf16_f32 v51, v54, v55\n"
    "v_cvt_pk_bf16_f32 v120, v120, v121\n"
    "v_cvt_pk_bf16_f32 v121, v122, v123\n"
    "v_cvt_pk_bf16_f32 v122, v124, v125\n"
    "v_cvt_pk_bf16_f32 v123, v126, v127\n"
    "v_pk_add_f32 v[56:57], v[56:57], v[168:169] op_sel_hi:[1,0] neg_lo:[0,1] neg_hi:[0,1]\n"
    "v_pk_add_f32 v[58:59], v[58:59], v[168:169] op_sel_hi:[1,0] neg_lo:[0,1] neg_hi:[0,1]\n"
    "v_pk_add_f32 v[60:61], v[60:61], v[168:169] op_sel_hi:[1,0] neg_lo:[0,1] neg_hi:[0,1]\n"
    "v_pk_add_f32 v[62:63], v[62:63], v[168:169] op_sel_hi:[1,0] neg_lo:[0,1] neg_hi:[0,1]\n"
    "v_pk_add_f32 v[128:129], v[128:129], v[170:171] op_sel_hi:[1,0] neg_lo:[0,1] neg_hi:[0,1]\n"
    "v_pk_add_f32 v[130:131], v[130:131], v[170:171] op_sel_hi:[1,0] neg_lo:[0,1] neg_hi:[0,1]\n"
    "v_pk_add_f32 v[132:133], v[132:133], v[170:171] op_sel_hi:[1,0] neg_lo:[0,1] neg_hi:[0,1]\n"
    "v_pk_add_f32 v[134:135], v[134:135], v[170:171] op_sel_hi:[1,0] neg_lo:[0,1] neg_hi:[0,1]\n"
    "s_waitcnt lgkmcnt(0)\n"
    "v_mfma_f32_16x16x32_bf16 v[144:147], v[176:179], v[48:51], v[144:147]\n"
    "ds_read_b128 v[184:187], v228 offset:38912\n"
    "ds_read_b128 v[188:191], v228 offset:39936\n"
    "ds_read_b128 v[236:239], v231 offset:112\n"
    "v_mfma_f32_16x16x32_bf16 v[148:151], v[180:183], v[48:51], v[148:151]\n"
    "v_exp_f32 v56, v56\n"
    "v_exp_f32 v57, v57\n"
    "v_mfma_f32_16x16x32_bf16 v[160:163], v[232:235], v[48:51], v[160:163]\n"
    "v_exp_f32 v58, v58\n"
    "v_exp_f32 v59, v59\n"
    "v_mfma_f32_16x16x32_bf16 v[152:155], v[176:179], v[120:123], v[152:155]\n"
    "v_exp_f32 v60, v60\n"
    "v_exp_f32 v61, v61\n"
    "v_exp_f32 v62, v62\n"
    "v_mfma_f32_16x16x32_bf16 v[156:159], v[180:183], v[120:123], v[156:159]\n"
    "v_exp_f32 v63, v63\n"
    "v_exp_f32 v128, v128\n"
    "v_mfma_f32_16x16x32_bf16 v[164:167], v[232:235], v[120:123], v[164:167]\n"
    "v_exp_f32 v129, v129\n"
    "v_exp_f32 v130, v130\n"
    "v_exp_f32 v131, v131\n"
    "v_exp_f32 v132, v132\n"
    "v_exp_f32 v133, v133\n"
    "v_exp_f32 v134, v134\n"
    "v_exp_f32 v135, v135\n"
    "v_cvt_pk_bf16_f32 v56, v56, v57\n"
    "v_cvt_pk_bf16_f32 v57, v58, v59\n"
    "v_cvt_pk_bf16_f32 v58, v60, v61\n"
    "v_cvt_pk_bf16_f32 v59, v62, v63\n"
    "v_cvt_pk_bf16_f32 v128, v128, v129\n"
    "v_cvt_pk_bf16_f32 v129, v130, v131\n"
    "v_cvt_pk_bf16_f32 v130, v132, v133\n"
    "v_cvt_pk_bf16_f32 v131, v134, v135\n"
    "v_pk_add_f32 v[136:137], v[136:137], v[170:171] op_sel_hi:[1,0] neg_lo:[0,1] neg_hi:[0,1]\n"
    "v_pk_add_f32 v[138:139], v[138:139], v[170:171] op_sel_hi:[1,0] neg_lo:[0,1] neg_hi:[0,1]\n"
    "s_waitcnt lgkmcnt(0)\n"
    "v_mfma_f32_16x16x32_bf16 v[144:147], v[184:187], v[56:59], v[144:147]\n"
    "ds_read_b128 v[176:179], v228 offset:40960\n"
    "ds_read_b128 v[180:183], v228 offset:41984\n"
    "ds_read_b128 v[232:235], v231 offset:128\n"
    "v_mfma_f32_16x16x32_bf16 v[148:151], v[188:191], v[56:59], v[148:151]\n"
    "v_mfma_f32_16x16x32_bf16 v[160:163], v[236:239], v[56:59], v[160:163]\n"
    "v_exp_f32 v136, v136\n"
    "v_mfma_f32_16x16x32_bf16 v[152:155], v[184:187], v[128:131], v[152:155]\n"
    "v_exp_f32 v137, v137\n"
    "v_mfma_f32_16x16x32_bf16 v[156:159], v[188:191], v[128:131], v[156:159]\n"
    "v_exp_f32 v138, v138\n"
    "v_mfma_f32_16x16x32_bf16 v[164:167], v[236:239], v[128:131], v[164:167]\n"
    "v_exp_f32 v139, v139\n"
    "v_cvt_pk_bf16_f32 v136, v136, v137\n"
    "v_cvt_pk_bf16_f32 v137, v138, v139\n"
    "v_bfi_b32 v138, v229, 0, v174\n"
    "v_bfi_b32 v139, v230, 0, v175\n"
    "s_waitcnt lgkmcnt(0)\n"
    "v_mfma_f32_16x16x32_bf16 v[144:147], v[176:179], v[64:67], v[144:147]\n"
    "v_mfma_f32_16x16x32_bf16 v[148:151], v[180:183], v[64:67], v[148:151]\n"
    "v_mfma_f32_16x16x32_bf16 v[160:163], v[232:235], v[64:67], v[160:163]\n"
    "v_mfma_f32_16x16x32_bf16 v[152:155], v[176:179], v[136:139], v[152:155]\n"
    "v_mfma_f32_16x16x32_bf16 v[156:159], v[180:183], v[136:139], v[156:159]\n"
    "v_mfma_f32_16x16x32_bf16 v[164:167], v[232:235], v[136:139], v[164:167]\n"
    "s_waitcnt vmcnt(2)\n"
    "v_xor_b32 v249, v246, v244\n"
    "v_and_b32 v250, 63, v246\n"
    "v_cmp_lt_u32 vcc, 0xff, v249\n"
    "v_cmp_le_u32 s[96:97], 3, v250\n"
    "s_and_b32 s96, vcc_lo, s96\n"
    "s_andn2_b32 s96, s83, s96\n"
    "s_cbranch_scc1 .Lslow_h01\n"
    ".Lret_h01:\n"
    "s_barrier\n"
    "buffer_atomic_add v248, v247, s[44:47], 0 offen\n"
    "v_add_u32 v228, 0xffff4000, v228\n"
    "ds_read_b128 v[176:179], v228\n"
    "ds_read_b128 v[180:183], v228 offset:1024\n"
    "ds_read_b128 v[184:187], v228 offset:2048\n"
    "ds_read_b128 v[188:191], v228 offset:3072\n"
    "s_waitcnt lgkmcnt(2)\n"
    "v_mfma_f32_16x16x32_bf16 v[0:3], v[176:179], v[216:219], 0\n"
    "ds_read_b128 v[176:179], v228 offset:4096\n"
    "v_mfma_f32_16x16x32_bf16 v[4:7], v[180:183], v[216:219], 0\n"
    "v_mfma_f32_16x16x32_bf16 v[76:79], v[180:183], v[220:223], 0\n"
    "s_waitcnt lgkmcnt(1)\n"
    "ds_read_b128 v[180:183], v228 offset:5120\n"
    "v_mfma_f32_16x16x32_bf16 v[8:11], v[184:187], v[216:219], 0\n"
    "v_mfma_f32_16x16x32_bf16 v[80:83], v[184:187], v[220:223], 0\n"
    "ds_read_b128 v[184:187], v228 offset:6144\n"
    "v_mfma_f32_16x16x32_bf16 v[12:15], v[188:191], v[216:219], 0\n"
    "s_nop 0\n"
    "v_max3_f32 v168, v4, v5, v6\n"
    "v_max_f32 v168, v168, v7\n"
    "v_mbcnt_lo_u32_b32 v251, -1, 0\n"
    "v_mfma_f32_16x16x32_bf16 v[84:87], v[188:191], v[220:223], 0\n"
    "s_waitcnt lgkmcnt(1)\n"
    "ds_read_b128 v[188:191], v228 offset:7168\n"
    "v_mbcnt_hi_u32_b32 v251, -1, v251\n"
    "v_mfma_f32_16x16x32_bf16 v[16:19], v[176:179], v[216:219], 0\n"
    "v_max3_f32 v168, v168, v8, v9\n"
    "v_max3_f32 v168, v168, v10, v11\n"
    "v_max3_f32 v170, v80, v81, v82\n"
    "v_mfma_f32_16x16x32_bf16 v[88:91], v[176:179], v[220:223], 0\n"
    "ds_read_b128 v[176:179], v228 offset:8192\n"
    "v_max_f32 v170, v170, v83\n"
    "s_lshl_b32 s89, s50, 8\n"
    "s_add_u32 m0, s85, 0\n"
    "v_mfma_f32_16x16x32_bf16 v[20:23], v[180:183], v[216:219], 0\n"
    "v_max3_f32 v168, v168, v12, v13\n"
    "v_max3_f32 v168, v168, v14, v15\n"
    "v_max3_f32 v170, v170, v84, v85\n"
    "v_mfma_f32_16x16x32_bf16 v[92:95], v[180:183], v[220:223], 0\n"
    "s_waitcnt lgkmcnt(1)\n"
    "ds_read_b128 v[180:183], v228 offset:9216\n"
    "v_max3_f32 v170, v170, v86, v87\n"
    "v_mfma_f32_16x16x32_bf16 v[24:27], v[184:187], v[216:219], 0\n"
    "v_max3_f32 v168, v168, v16, v17\n"
    "v_max3_f32 v168, v168, v18, v19\n"
    "v_max3_f32 v170, v170, v88, v89\n"
    "v_mfma_f32_16x16x32_bf16 v[96:99], v[184:187], v[220:223], 0\n"
    "s_lshl_b32 s98, s56, 4\n"
    "ds_read_b128 v[184:187], v228 offset:10240\n"
    "v_max3_f32 v170, v170, v90, v91\n"
    "s_add_u32 s89, s89, 0x1c000\n"
    "v_mfma_f32_16x16x32_bf16 v[28:31], v[188:191], v[216:219], 0\n"
    "v_max3_f32 v168, v168, v20, v21\n"
    "v_max3_f32 v168, v168, v22, v23\n"
    "v_max3_f32 v170, v170, v92, v93\n"
    "v_mfma_f32_16x16x32_bf16 v[100:103], v[188:191], v[220:223], 0\n"
    "s_waitcnt lgkmcnt(1)\n"
    "ds_read_b128 v[188:191], v228 offset:11264\n"
    "v_max3_f32 v170, v170, v94, v95\n"
    "v_mfma_f32_16x16x32_bf16 v[32:35], v[176:179], v[216:219], 0\n"
    "v_max3_f32 v168, v168, v24, v25\n"
    "buffer_load_dwordx4 v200, s[32:35], s98 offen lds\n"
    "v_max3_f32 v168, v168, v26, v27\n"
    "v_max3_f32 v170, v170, v96, v97\n"
    "v_mfma_f32_16x16x32_bf16 v[104:107], v[176:179], v[220:223], 0\n"
    "ds_read_b128 v[176:179], v228 offset:12288\n"
    "v_max3_f32 v170, v170, v98, v99\n"
    "v_mov_b32 v231, s89\n"
    "v_mfma_f32_16x16x32_bf16 v[36:39], v[180:183], v[216:219], 0\n"
    "v_max3_f32 v168, v168, v28, v29\n"
    "v_max3_f32 v168, v168, v30, v31\n"
    "v_max3_f32 v170, v170, v100, v101\n"
    "v_mfma_f32_16x16x32_bf16 v[108:111], v[180:183], v[220:223], 0\n"
    "s_waitcnt lgkmcnt(1)\n"
    "ds_read_b128 v[180:183], v228 offset:13312\n"
    "buffer_load_dwordx4 v201, s[32:35], s98 offen offset:1024 lds\n"
    "v_max3_f32 v170, v170, v102, v103\n"
    "v_mfma_f32_16x16x32_bf16 v[40:43], v[184:187], v[216:219], 0\n"
    "v_max3_f32 v168, v168, v32, v33\n"
    "v_max3_f32 v168, v168, v34, v35\n"
    "v_max3_f32 v170, v170, v104, v105\n"
    "v_mfma_f32_16x16x32_bf16 v[112:115], v[184:187], v[220:223], 0\n"
    "ds_read_b128 v[184:187], v228 offset:14336\n"
    "v_max3_f32 v170, v170, v106, v107\n"
    "v_lshl_add_u32 v250, v251, 2, v231\n"
    "v_mfma_f32_16x16x32_bf16 v[44:47], v[188:191], v[216:219], 0\n"
    "v_max3_f32 v168, v168, v36, v37\n"
    "v_max3_f32 v168, v168, v38, v39\n"
    "v_max3_f32 v170, v170, v108, v109\n"
    "buffer_load_dwordx4 v202, s[32:35], s98 offen offset:2048 lds\n"
    "v_mfma_f32_16x16x32_bf16 v[116:119], v[188:191], v[220:223], 0\n"
    "s_waitcnt lgkmcnt(1)\n"
    "ds_read_b128 v[188:191], v228 offset:15360\n"
    "v_max3_f32 v170, v170, v110, v111\n"
    "v_mfma_f32_16x16x32_bf16 v[48:51], v[176:179], v[216:219], 0\n"
    "v_max3_f32 v168, v168, v40, v41\n"
    "v_max3_f32 v168, v168, v42, v43\n"
    "v_max3_f32 v170, v170, v112, v113\n"
    "v_mfma_f32_16x16x32_bf16 v[120:123], v[176:179], v[220:223], 0\n"
    "ds_read_b128 v[176:179], v228 offset:16384\n"
    "v_max3_f32 v170, v170, v114, v115\n"
    "v_lshrrev_b32 v251, 2, v251\n"
    "v_mfma_f32_16x16x32_bf16 v[52:55], v[180:183], v[216:219], 0\n"
    "buffer_load_dwordx4 v203, s[32:35], s98 offen offset:3072 lds\n"
    "v_max3_f32 v168, v168, v44, v45\n"
    "v_max3_f32 v168, v168, v46, v47\n"
    "v_max3_f32 v170, v170, v116, v117\n"
    "v_mfma_f32_16x16x32_bf16 v[124:127], v[180:183], v[220:223], 0\n"
    "s_waitcnt lgkmcnt(1)\n"
    "ds_read_b128 v[180:183], v228 offset:17408\n"
    "v_max3_f32 v170, v170, v118, v119\n"
    "v_mfma_f32_16x16x32_bf16 v[56:59], v[184:187], v[216:219], 0\n"
    "v_max3_f32 v168, v168, v48, v49\n"
    "v_max3_f32 v168, v168, v50, v51\n"
    "v_max3_f32 v170, v170, v120, v121\n"
    "v_mfma_f32_16x16x32_bf16 v[128:131], v[184:187], v[220:223], 0\n"
    "v_max3_f32 v170, v170, v122, v123\n"
    "buffer_load_dwordx4 a[0:3], v192, s[44:47], s56 offen sc1\n"
    "v_cmp_le_i32 vcc, s17, v251\n"
    "v_mov_b32 v249, 0x3f803f80\n"
    "v_mfma_f32_16x16x32_bf16 v[60:63], v[188:191], v[216:219], 0\n"
    "v_max3_f32 v168, v168, v52, v53\n"
    "v_max3_f32 v168, v168, v54, v55\n"
    "v_max3_f32 v170, v170, v124, v125\n"
    "v_mfma_f32_16x16x32_bf16 v[132:135], v[188:191], v[220:223], 0\n"
    "s_waitcnt lgkmcnt(0)\n"
    "v_max3_f32 v170, v170, v126, v127\n"
    "v_cndmask_b32 v249, 0, v249, vcc\n"
    "v_mfma_f32_16x16x32_bf16 v[64:67], v[176:179], v[216:219], 0\n"
    "v_max3_f32 v168, v168, v56, v57\n"
    "buffer_load_dwordx4 a[4:7], v193, s[44:47], s56 offen sc1\n"
    "v_max3_f32 v168, v168, v58, v59\n"
    "v_max3_f32 v170, v170, v128, v129\n"
    "v_mfma_f32_16x16x32_bf16 v[136:139], v[176:179], v[220:223], 0\n"
    "v_max3_f32 v170, v170, v130, v131\n"
    "ds_write_b32 v250, v249\n"
    "s_lshl_b32 s89, s49, 13\n"
    "v_mfma_f32_16x16x32_bf16 v[140:143], v[180:183], v[220:223], 0\n"
    "v_max3_f32 v168, v168, v60, v61\n"
    "v_max3_f32 v168, v168, v62, v63\n"
    "v_max3_f32 v170, v170, v132, v133\n"
    "v_max3_f32 v170, v170, v134, v135\n"
    "v_max3_f32 v170, v170, v136, v137\n"
    "v_max3_f32 v170, v170, v138, v139\n"
    "buffer_load_dwordx4 a[8:11], v194, s[44:47], s56 offen sc1\n"
    "s_and_b32 s89, s89, 0x6000\n"
    "s_add_u32 s89, s89, s88\n"
    "s_add_u32 s89, s89, 0x1c000\n"
    "v_rcp_f32 v160, v160\n"
    "v_rcp_f32 v164, v164\n"
    "v_mul_f32 v144, v144, v160\n"
    "v_mul_f32 v145, v145, v160\n"
    "v_mul_f32 v146, v146, v160\n"
    "v_mul_f32 v147, v147, v160\n"
    "v_mul_f32 v148, v148, v160\n"
    "v_mul_f32 v149, v149, v160\n"
    "v_mul_f32 v150, v150, v160\n"
    "v_mul_f32 v151, v151, v160\n"
    "buffer_load_dwordx4 a[12:15], v195, s[44:47], s56 offen sc1\n"
    "v_cvt_pk_bf16_f32 v144, v144, v145\n"
    "v_cvt_pk_bf16_f32 v145, v146, v147\n"
    "v_cvt_pk_bf16_f32 v146, v148, v149\n"
    "v_cvt_pk_bf16_f32 v147, v150, v151\n"
    "buffer_store_dwordx4 v[144:147], v240, s[44:47], s89 offen sc1\n"
    "v_mul_f32 v152, v152, v164\n"
    "v_mul_f32 v153, v153, v164\n"
    "v_mul_f32 v154, v154, v164\n"
    "v_mul_f32 v155, v155, v164\n"
    "v_mul_f32 v156, v156, v164\n"
    "v_mul_f32 v157, v157, v164\n"
    "v_mul_f32 v158, v158, v164\n"
    "v_mul_f32 v159, v159, v164\n"
    "buffer_load_dwordx4 a[16:19], v196, s[44:47], s56 offen sc1\n"
    "v_cvt_pk_bf16_f32 v152, v152, v153\n"
    "v_cvt_pk_bf16_f32 v153, v154, v155\n"
    "v_cvt_pk_bf16_f32 v154, v156, v157\n"
    "v_cvt_pk_bf16_f32 v155, v158, v159\n"
    "buffer_store_dwordx4 v[152:155], v240, s[44:47], s89 offen offset:1024 sc1\n"
    "v_mbcnt_lo_u32_b32 v251, -1, 0\n"
    "v_mbcnt_hi_u32_b32 v251, -1, v251\n"
    "v_lshrrev_b32 v249, 4, v251\n"
    "v_bfe_u32 v250, v251, 2, 2\n"
    "v_lshl_add_u32 v250, v249, 2, v250\n"
    "v_and_b32 v249, 7, v250\n"
    "v_lshlrev_b32 v250, 8, v250\n"
    "v_lshl_add_u32 v250, v249, 5, v250\n"
    "buffer_load_dwordx4 a[20:23], v197, s[44:47], s56 offen sc1\n"
    "v_and_b32 v249, 3, v251\n"
    "v_lshl_add_u32 v250, v249, 3, v250\n"
    "v_xor_b32 v224, 0, v250\n"
    "v_xor_b32 v225, 32, v250\n"
    "v_xor_b32 v226, 64, v250\n"
    "v_xor_b32 v227, 0x60, v250\n"
    "v_xor_b32 v204, 0x80, v250\n"
    "v_xor_b32 v241, 0xa0, v250\n"
    "v_xor_b32 v252, 0xc0, v250\n"
    "v_xor_b32 v253, 0xe0, v250\n"
    "v_cndmask_b32 v0, v64, v0, s[4:5]\n"
    "v_cndmask_b32 v1, v65, v1, s[6:7]\n"
    "v_cndmask_b32 v2, v66, v2, s[8:9]\n"
    "buffer_load_dwordx4 a[24:27], v198, s[44:47], s56 offen sc1\n"
    "v_cndmask_b32 v3, v67, v3, s[10:11]\n"
    "v_max3_f32 v168, v168, v0, v1\n"
    "v_max3_f32 v168, v168, v2, v3\n"
    "v_cndmask_b32 v76, v140, v76, s[4:5]\n"
    "v_cndmask_b32 v77, v141, v77, s[6:7]\n"
    "v_cndmask_b32 v78, v142, v78, s[8:9]\n"
    "v_cndmask_b32 v79, v143, v79, s[10:11]\n"
    "v_max3_f32 v170, v170, v76, v77\n"
    "v_max3_f32 v170, v170, v78, v79\n"
    "v_mov_b32 v169, v168\n"
    "v_mov_b32 v171, v170\n"
    "ds_read_b128 v[176:179], v228 offset:24576\n"
    "ds_read_b128 v[180:183], v228 offset:25600\n"
    "buffer_load_dwordx4 a[28:31], v199, s[44:47], s56 offen sc1\n"
    "v_permlane16_swap_b32 v168, v169\n"
    "v_permlane16_swap_b32 v170, v171\n"
    "v_max_f32 v168, v168, v169\n"
    "v_max_f32 v170, v170, v171\n"
    "v_mov_b32 v169, v168\n"
    "v_mov_b32 v171, v170\n"
    "ds_read_b128 v[232:235], v231\n"
    "v_permlane32_swap_b32 v168, v169\n"
    "v_permlane32_swap_b32 v170, v171\n"
    "v_max_f32 v168, v168, v169\n"
    "v_max_f32 v170, v170, v171\n"
    "v_pk_add_f32 v[0:1], v[0:1], v[168:169] op_sel_hi:[1,0] neg_lo:[0,1] neg_hi:[0,1]\n"
    "s_add_u32 m0, s85, 0x4000\n"
    "v_pk_add_f32 v[2:3], v[2:3], v[168:169] op_sel_hi:[1,0] neg_lo:[0,1] neg_hi:[0,1]\n"
    "v_pk_add_f32 v[4:5], v[4:5], v[168:169] op_sel_hi:[1,0] neg_lo:[0,1] neg_hi:[0,1]\n"
    "v_pk_add_f32 v[6:7], v[6:7], v[168:169] op_sel_hi:[1,0] neg_lo:[0,1] neg_hi:[0,1]\n"
    "v_pk_add_f32 v[76:77], v[76:77], v[170:171] op_sel_hi:[1,0] neg_lo:[0,1] neg_hi:[0,1]\n"
    "v_pk_add_f32 v[78:79], v[78:79], v[170:171] op_sel_hi:[1,0] neg_lo:[0,1] neg_hi:[0,1]\n"
    "v_exp_f32 v0, v0\n"
    "v_exp_f32 v1, v1\n"
    "v_exp_f32 v2, v2\n"
    "v_exp_f32 v3, v3\n"
    "v_exp_f32 v4, v4\n"
    "v_exp_f32 v5, v5\n"
    "v_exp_f32 v6, v6\n"
    "v_exp_f32 v7, v7\n"
    "s_lshl_b32 s98, s57, 4\n"
    "v_exp_f32 v76, v76\n"
    "v_exp_f32 v77, v77\n"
    "v_exp_f32 v78, v78\n"
    "v_exp_f32 v79, v79\n"
    "v_cvt_pk_bf16_f32 v172, v0, v1\n"
    "v_cvt_pk_bf16_f32 v173, v2, v3\n"
    "v_cvt_pk_bf16_f32 v2, v4, v5\n"
    "v_cvt_pk_bf16_f32 v3, v6, v7\n"
    "v_and_b32 v0, v172, v229\n"
    "v_and_b32 v1, v173, v230\n"
    "v_bfi_b32 v64, v229, 0, v172\n"
    "v_bfi_b32 v65, v230, 0, v173\n"
    "v_mov_b32 v66, 0\n"
    "buffer_load_dwordx4 v200, s[32:35], s98 offen lds\n"
    "v_mov_b32 v67, 0\n"
    "v_mov_b32 v72, 0\n"
    "v_mov_b32 v73, 0\n"
    "v_cvt_pk_bf16_f32 v174, v76, v77\n"
    "v_cvt_pk_bf16_f32 v175, v78, v79\n"
    "v_and_b32 v74, v174, v229\n"
    "v_and_b32 v75, v175, v230\n"
    "v_pk_add_f32 v[8:9], v[8:9], v[168:169] op_sel_hi:[1,0] neg_lo:[0,1] neg_hi:[0,1]\n"
    "v_pk_add_f32 v[10:11], v[10:11], v[168:169] op_sel_hi:[1,0] neg_lo:[0,1] neg_hi:[0,1]\n"
    "v_pk_add_f32 v[12:13], v[12:13], v[168:169] op_sel_hi:[1,0] neg_lo:[0,1] neg_hi:[0,1]\n"
    "v_pk_add_f32 v[14:15], v[14:15], v[168:169] op_sel_hi:[1,0] neg_lo:[0,1] neg_hi:[0,1]\n"
    "v_pk_add_f32 v[80:81], v[80:81], v[170:171] op_sel_hi:[1,0] neg_lo:[0,1] neg_hi:[0,1]\n"
    "v_pk_add_f32 v[82:83], v[82:83], v[170:171] op_sel_hi:[1,0] neg_lo:[0,1] neg_hi:[0,1]\n"
    "buffer_load_dwordx4 v201, s[32:35], s98 offen offset:1024 lds\n"
    "v_pk_add_f32 v[84:85], v[84:85], v[170:171] op_sel_hi:[1,0] neg_lo:[0,1] neg_hi:[0,1]\n"
    "v_pk_add_f32 v[86:87], v[86:87], v[170:171] op_sel_hi:[1,0] neg_lo:[0,1] neg_hi:[0,1]\n"
    "s_waitcnt lgkmcnt(0)\n"
    "v_mfma_f32_16x16x32_bf16 v[144:147], v[176:179], v[0:3], 0\n"
    "ds_read_b128 v[184:187], v228 offset:26624\n"
    "ds_read_b128 v[188:191], v228 offset:27648\n"
    "ds_read_b128 v[236:239], v231 offset:16\n"
    "v_mfma_f32_16x16x32_bf16 v[148:151], v[180:183], v[0:3], 0\n"
    "v_exp_f32 v8, v8\n"
    "v_exp_f32 v9, v9\n"
    "v_mfma_f32_16x16x32_bf16 v[160:163], v[232:235], v[0:3], 0\n"
    "v_exp_f32 v10, v10\n"
    "v_exp_f32 v11, v11\n"
    "buffer_load_dwordx4 v202, s[32:35], s98 offen offset:2048 lds\n"
    "v_mfma_f32_16x16x32_bf16 v[152:155], v[176:179], v[72:75], 0\n"
    "v_exp_f32 v12, v12\n"
    "v_exp_f32 v13, v13\n"
    "v_exp_f32 v14, v14\n"
    "v_mfma_f32_16x16x32_bf16 v[156:159], v[180:183], v[72:75], 0\n"
    "v_exp_f32 v15, v15\n"
    "v_exp_f32 v80, v80\n"
    "v_mfma_f32_16x16x32_bf16 v[164:167], v[232:235], v[72:75], 0\n"
    "v_exp_f32 v81, v81\n"
    "v_exp_f32 v82, v82\n"
    "v_exp_f32 v83, v83\n"
    "v_exp_f32 v84, v84\n"
    "v_exp_f32 v85, v85\n"
    "buffer_load_dwordx4 v203, s[32:35], s98 offen offset:3072 lds\n"
    "v_exp_f32 v86, v86\n"
    "v_exp_f32 v87, v87\n"
    "v_cvt_pk_bf16_f32 v8, v8, v9\n"
    "v_cvt_pk_bf16_f32 v9, v10, v11\n"
    "v_cvt_pk_bf16_f32 v10, v12, v13\n"
    "v_cvt_pk_bf16_f32 v11, v14, v15\n"
    "v_cvt_pk_bf16_f32 v80, v80, v81\n"
    "v_cvt_pk_bf16_f32 v81, v82, v83\n"
    "v_cvt_pk_bf16_f32 v82, v84, v85\n"
    "v_cvt_pk_bf16_f32 v83, v86, v87\n"
    "v_pk_add_f32 v[16:17], v[16:17], v[168:169] op_sel_hi:[1,0] neg_lo:[0,1] neg_hi:[0,1]\n"
    "v_pk_add_f32 v[18:19], v[18:19], v[168:169] op_sel_hi:[1,0] neg_lo:[0,1] neg_hi:[0,1]\n"
    "buffer_load_dwordx4 a[32:35], v192, s[44:47], s57 offen sc1\n"
    "v_pk_add_f32 v[20:21], v[20:21], v[168:169] op_sel_hi:[1,0] neg_lo:[0,1] neg_hi:[0,1]\n"
    "v_pk_add_f32 v[22:23], v[22:23], v[168:169] op_sel_hi:[1,0] neg_lo:[0,1] neg_hi:[0,1]\n"
    "v_pk_add_f32 v[88:89], v[88:89], v[170:171] op_sel_hi:[1,0] neg_lo:[0,1] neg_hi:[0,1]\n"
    "v_pk_add_f32 v[90:91], v[90:91], v[170:171] op_sel_hi:[1,0] neg_lo:[0,1] neg_hi:[0,1]\n"
    "v_pk_add_f32 v[92:93], v[92:93], v[170:171] op_sel_hi:[1,0] neg_lo:[0,1] neg_hi:[0,1]\n"
    "v_pk_add_f32 v[94:95], v[94:95], v[170:171] op_sel_hi:[1,0] neg_lo:[0,1] neg_hi:[0,1]\n"
    "s_waitcnt lgkmcnt(0)\n"
    "v_mfma_f32_16x16x32_bf16 v[144:147], v[184:187], v[8:11], v[144:147]\n"
    "ds_read_b128 v[176:179], v228 offset:28672\n"
    "ds_read_b128 v[180:183], v228 offset:29696\n"
    "ds_read_b128 v[232:235], v231 offset:32\n"
    "v_mfma_f32_16x16x32_bf16 v[148:151], v[188:191], v[8:11], v[148:151]\n"
    "v_exp_f32 v16, v16\n"
    "buffer_load_dwordx4 a[36:39], v193, s[44:47], s57 offen sc1\n"
    "v_exp_f32 v17, v17\n"
    "v_mfma_f32_16x16x32_bf16 v[160:163], v[236:239], v[8:11], v[160:163]\n"
    "v_exp_f32 v18, v18\n"
    "v_exp_f32 v19, v19\n"
    "v_mfma_f32_16x16x32_bf16 v[152:155], v[184:187], v[80:83], v[152:155]\n"
    "v_exp_f32 v20, v20\n"
    "v_exp_f32 v21, v21\n"
    "v_exp_f32 v22, v22\n"
    "v_mfma_f32_16x16x32_bf16 v[156:159], v[188:191], v[80:83], v[156:159]\n"
    "v_exp_f32 v23, v23\n"
    "v_exp_f32 v88, v88\n"
    "v_mfma_f32_16x16x32_bf16 v[164:167], v[236:239], v[80:83], v[164:167]\n"
    "v_exp_f32 v89, v89\n"
    "buffer_load_dwordx4 a[40:43], v194, s[44:47], s57 offen sc1\n"
    "v_exp_f32 v90, v90\n"
    "v_exp_f32 v91, v91\n"
    "v_exp_f32 v92, v92\n"
    "v_exp_f32 v93, v93\n"
    "v_exp_f32 v94, v94\n"
    "v_exp_f32 v95, v95\n"
    "v_cvt_pk_bf16_f32 v16, v16, v17\n"
    "v_cvt_pk_bf16_f32 v17, v18, v19\n"
    "v_cvt_pk_bf16_f32 v18, v20, v21\n"
    "v_cvt_pk_bf16_f32 v19, v22, v23\n"
    "v_cvt_pk_bf16_f32 v88, v88, v89\n"
    "v_cvt_pk_bf16_f32 v89, v90, v91\n"
    "v_cvt_pk_bf16_f32 v90, v92, v93\n"
    "buffer_load_dwordx4 a[44:47], v195, s[44:47], s57 offen sc1\n"
    "v_cvt_pk_bf16_f32 v91, v94, v95\n"
    "v_pk_add_f32 v[24:25], v[24:25], v[168:169] op_sel_hi:[1,0] neg_lo:[0,1] neg_hi:[0,1]\n"
    "v_pk_add_f32 v[26:27], v[26:27], v[168:169] op_sel_hi:[1,0] neg_lo:[0,1] neg_hi:[0,1]\n"
    "v_pk_add_f32 v[28:29], v[28:29], v[168:169] op_sel_hi:[1,0] neg_lo:[0,1] neg_hi:[0,1]\n"
    "v_pk_add_f32 v[30:31], v[30:31], v[168:169] op_sel_hi:[1,0] neg_lo:[0,1] neg_hi:[0,1]\n"
    "v_pk_add_f32 v[96:97], v[96:97], v[170:171] op_sel_hi:[1,0] neg_lo:[0,1] neg_hi:[0,1]\n"
    "v_pk_add_f32 v[98:99], v[98:99], v[170:171] op_sel_hi:[1,0] neg_lo:[0,1] neg_hi:[0,1]\n"
    "v_pk_add_f32 v[100:101], v[100:101], v[170:171] op_sel_hi:[1,0] neg_lo:[0,1] neg_hi:[0,1]\n"
    "v_pk_add_f32 v[102:103], v[102:103], v[170:171] op_sel_hi:[1,0] neg_lo:[0,1] neg_hi:[0,1]\n"
    "s_waitcnt lgkmcnt(0)\n"
    "v_mfma_f32_16x16x32_bf16 v[144:147], v[176:179], v[16:19], v[144:147]\n"
    "ds_read_b128 v[184:187], v228 offset:30720\n"
    "ds_read_b128 v[188:191], v228 offset:31744\n"
    "buffer_load_dwordx4 a[48:51], v196, s[44:47], s57 offen sc1\n"
    "ds_read_b128 v[236:239], v231 offset:48\n"
    "v_mfma_f32_16x16x32_bf16 v[148:151], v[180:183], v[16:19], v[148:151]\n"
    "v_exp_f32 v24, v24\n"
    "v_exp_f32 v25, v25\n"
    "v_mfma_f32_16x16x32_bf16 v[160:163], v[232:235], v[16:19], v[160:163]\n"
    "v_exp_f32 v26, v26\n"
    "v_exp_f32 v27, v27\n"
    "v_mfma_f32_16x16x32_bf16 v[152:155], v[176:179], v[88:91], v[152:155]\n"
    "v_exp_f32 v28, v28\n"
    "v_exp_f32 v29, v29\n"
    "v_exp_f32 v30, v30\n"
    "v_mfma_f32_16x16x32_bf16 v[156:159], v[180:183], v[88:91], v[156:159]\n"
    "v_exp_f32 v31, v31\n"
    "buffer_load_dwordx4 a[52:55], v197, s[44:47], s57 offen sc1\n"
    "v_exp_f32 v96, v96\n"
    "v_mfma_f32_16x16x32_bf16 v[164:167], v[232:235], v[88:91], v[164:167]\n"
    "v_exp_f32 v97, v97\n"
    "v_exp_f32 v98, v98\n"
    "v_exp_f32 v99, v99\n"
    "v_exp_f32 v100, v100\n"
    "v_exp_f32 v101, v101\n"
    "v_exp_f32 v102, v102\n"
    "v_exp_f32 v103, v103\n"
    "v_cvt_pk_bf16_f32 v24, v24, v25\n"
    "v_cvt_pk_bf16_f32 v25, v26, v27\n"
    "v_cvt_pk_bf16_f32 v26, v28, v29\n"
    "v_cvt_pk_bf16_f32 v27, v30, v31\n"
    "buffer_load_dwordx4 a[56:59], v198, s[44:47], s57 offen sc1\n"
    "v_cvt_pk_bf16_f32 v96, v96, v97\n"
    "v_cvt_pk_bf16_f32 v97, v98, v99\n"
    "v_cvt_pk_bf16_f32 v98, v100, v101\n"
    "v_cvt_pk_bf16_f32 v99, v102, v103\n"
    "v_pk_add_f32 v[32:33], v[32:33], v[168:169] op_sel_hi:[1,0] neg_lo:[0,1] neg_hi:[0,1]\n"
    "v_pk_add_f32 v[34:35], v[34:35], v[168:169] op_sel_hi:[1,0] neg_lo:[0,1] neg_hi:[0,1]\n"
    "v_pk_add_f32 v[36:37], v[36:37], v[168:169] op_sel_hi:[1,0] neg_lo:[0,1] neg_hi:[0,1]\n"
    "v_pk_add_f32 v[38:39], v[38:39], v[168:169] op_sel_hi:[1,0] neg_lo:[0,1] neg_hi:[0,1]\n"
    "v_pk_add_f32 v[104:105], v[104:105], v[170:171] op_sel_hi:[1,0] neg_lo:[0,1] neg_hi:[0,1]\n"
    "v_pk_add_f32 v[106:107], v[106:107], v[170:171] op_sel_hi:[1,0] neg_lo:[0,1] neg_hi:[0,1]\n"
    "v_pk_add_f32 v[108:109], v[108:109], v[170:171] op_sel_hi:[1,0] neg_lo:[0,1] neg_hi:[0,1]\n"
    "v_pk_add_f32 v[110:111], v[110:111], v[170:171] op_sel_hi:[1,0] neg_lo:[0,1] neg_hi:[0,1]\n"
    "buffer_load_dwordx4 a[60:63], v199, s[44:47], s57 offen sc1\n"
    "s_waitcnt lgkmcnt(0)\n"
    "v_mfma_f32_16x16x32_bf16 v[144:147], v[184:187], v[24:27], v[144:147]\n"
    "ds_read_b128 v[176:179], v228 offset:32768\n"
    "ds_read_b128 v[180:183], v228 offset:33792\n"
    "ds_read_b128 v[232:235], v231 offset:64\n"
    "v_mfma_f32_16x16x32_bf16 v[148:151], v[188:191], v[24:27], v[148:151]\n"
    "v_exp_f32 v32, v32\n"
    "v_exp_f32 v33, v33\n"
    "v_mfma_f32_16x16x32_bf16 v[160:163], v[236:239], v[24:27], v[160:163]\n"
    "v_exp_f32 v34, v34\n"
    "v_exp_f32 v35, v35\n"
    "v_mfma_f32_16x16x32_bf16 v[152:155], v[184:187], v[96:99], v[152:155]\n"
    "v_exp_f32 v36, v36\n"
    "s_add_u32 m0, s85, 0x8000\n"
    "v_exp_f32 v37, v37\n"
    "v_exp_f32 v38, v38\n"
    "v_mfma_f32_16x16x32_bf16 v[156:159], v[188:191], v[96:99], v[156:159]\n"
    "v_exp_f32 v39, v39\n"
    "v_exp_f32 v104, v104\n"
    "v_mfma_f32_16x16x32_bf16 v[164:167], v[236:239], v[96:99], v[164:167]\n"
    "v_exp_f32 v105, v105\n"
    "v_exp_f32 v106, v106\n"
    "v_exp_f32 v107, v107\n"
    "v_exp_f32 v108, v108\n"
    "v_exp_f32 v109, v109\n"
    "v_exp_f32 v110, v110\n"
    "v_exp_f32 v111, v111\n"
    "s_lshl_b32 s98, s58, 4\n"
    "v_cvt_pk_bf16_f32 v32, v32, v33\n"
    "v_cvt_pk_bf16_f32 v33, v34, v35\n"
    "v_cvt_pk_bf16_f32 v34, v36, v37\n"
    "v_cvt_pk_bf16_f32 v35, v38, v39\n"
    "v_cvt_pk_bf16_f32 v104, v104, v105\n"
    "v_cvt_pk_bf16_f32 v105, v106, v107\n"
    "v_cvt_pk_bf16_f32 v106, v108, v109\n"
    "v_cvt_pk_bf16_f32 v107, v110, v111\n"
    "v_pk_add_f32 v[40:41], v[40:41], v[168:169] op_sel_hi:[1,0] neg_lo:[0,1] neg_hi:[0,1]\n"
    "v_pk_add_f32 v[42:43], v[42:43], v[168:169] op_sel_hi:[1,0] neg_lo:[0,1] neg_hi:[0,1]\n"
    "v_pk_add_f32 v[44:45], v[44:45], v[168:169] op_sel_hi:[1,0] neg_lo:[0,1] neg_hi:[0,1]\n"
    "v_pk_add_f32 v[46:47], v[46:47], v[168:169] op_sel_hi:[1,0] neg_lo:[0,1] neg_hi:[0,1]\n"
    "v_pk_add_f32 v[112:113], v[112:113], v[170:171] op_sel_hi:[1,0] neg_lo:[0,1] neg_hi:[0,1]\n"
    "buffer_load_dwordx4 v200, s[32:35], s98 offen lds\n"
    "v_pk_add_f32 v[114:115], v[114:115], v[170:171] op_sel_hi:[1,0] neg_lo:[0,1] neg_hi:[0,1]\n"
    "v_pk_add_f32 v[116:117], v[116:117], v[170:171] op_sel_hi:[1,0] neg_lo:[0,1] neg_hi:[0,1]\n"
    "v_pk_add_f32 v[118:119], v[118:119], v[170:171] op_sel_hi:[1,0] neg_lo:[0,1] neg_hi:[0,1]\n"
    "s_waitcnt lgkmcnt(0)\n"
    "v_mfma_f32_16x16x32_bf16 v[144:147], v[176:179], v[32:35], v[144:147]\n"
    "ds_read_b128 v[184:187], v228 offset:34816\n"
    "ds_read_b128 v[188:191], v228 offset:35840\n"
    "ds_read_b128 v[236:239], v231 offset:80\n"
    "v_mfma_f32_16x16x32_bf16 v[148:151], v[180:183], v[32:35], v[148:151]\n"
    "v_exp_f32 v40, v40\n"
    "v_exp_f32 v41, v41\n"
    "v_mfma_f32_16x16x32_bf16 v[160:163], v[232:235], v[32:35], v[160:163]\n"
    "v_exp_f32 v42, v42\n"
    "buffer_load_dwordx4 v201, s[32:35], s98 offen offset:1024 lds\n"
    "v_exp_f32 v43, v43\n"
    "v_mfma_f32_16x16x32_bf16 v[152:155], v[176:179], v[104:107], v[152:155]\n"
    "v_exp_f32 v44, v44\n"
    "v_exp_f32 v45, v45\n"
    "v_exp_f32 v46, v46\n"
    "v_mfma_f32_16x16x32_bf16 v[156:159], v[180:183], v[104:107], v[156:159]\n"
    "v_exp_f32 v47, v47\n"
    "v_exp_f32 v112, v112\n"
    "v_mfma_f32_16x16x32_bf16 v[164:167], v[232:235], v[104:107], v[164:167]\n"
    "v_exp_f32 v113, v113\n"
    "v_exp_f32 v114, v114\n"
    "v_exp_f32 v115, v115\n"
    "v_exp_f32 v116, v116\n"
    "buffer_load_dwordx4 v202, s[32:35], s98 offen offset:2048 lds\n"
    "v_exp_f32 v117, v117\n"
    "v_exp_f32 v118, v118\n"
    "v_exp_f32 v119, v119\n"
    "v_cvt_pk_bf16_f32 v40, v40, v41\n"
    "v_cvt_pk_bf16_f32 v41, v42, v43\n"
    "v_cvt_pk_bf16_f32 v42, v44, v45\n"
    "v_cvt_pk_bf16_f32 v43, v46, v47\n"
    "v_cvt_pk_bf16_f32 v112, v112, v113\n"
    "v_cvt_pk_bf16_f32 v113, v114, v115\n"
    "v_cvt_pk_bf16_f32 v114, v116, v117\n"
    "v_cvt_pk_bf16_f32 v115, v118, v119\n"
    "v_pk_add_f32 v[48:49], v[48:49], v[168:169] op_sel_hi:[1,0] neg_lo:[0,1] neg_hi:[0,1]\n"
    "v_pk_add_f32 v[50:51], v[50:51], v[168:169] op_sel_hi:[1,0] neg_lo:[0,1] neg_hi:[0,1]\n"
    "buffer_load_dwordx4 v203, s[32:35], s98 offen offset:3072 lds\n"
    "v_pk_add_f32 v[52:53], v[52:53], v[168:169] op_sel_hi:[1,0] neg_lo:[0,1] neg_hi:[0,1]\n"
    "v_pk_add_f32 v[54:55], v[54:55], v[168:169] op_sel_hi:[1,0] neg_lo:[0,1] neg_hi:[0,1]\n"
    "v_pk_add_f32 v[120:121], v[120:121], v[170:171] op_sel_hi:[1,0] neg_lo:[0,1] neg_hi:[0,1]\n"
    "v_pk_add_f32 v[122:123], v[122:123], v[170:171] op_sel_hi:[1,0] neg_lo:[0,1] neg_hi:[0,1]\n"
    "v_pk_add_f32 v[124:125], v[124:125], v[170:171] op_sel_hi:[1,0] neg_lo:[0,1] neg_hi:[0,1]\n"
    "v_pk_add_f32 v[126:127], v[126:127], v[170:171] op_sel_hi:[1,0] neg_lo:[0,1] neg_hi:[0,1]\n"
    "s_waitcnt lgkmcnt(0)\n"
    "v_mfma_f32_16x16x32_bf16 v[144:147], v[184:187], v[40:43], v[144:147]\n"
    "ds_read_b128 v[176:179], v228 offset:36864\n"
    "ds_read_b128 v[180:183], v228 offset:37888\n"
    "ds_read_b128 v[232:235], v231 offset:96\n"
    "v_mfma_f32_16x16x32_bf16 v[148:151], v[188:191], v[40:43], v[148:151]\n"
    "s_add_u32 m0, s85, 0xc000\n"
    "v_exp_f32 v48, v48\n"
    "v_exp_f32 v49, v49\n"
    "v_mfma_f32_16x16x32_bf16 v[160:163], v[236:239], v[40:43], v[160:163]\n"
    "v_exp_f32 v50, v50\n"
    "v_exp_f32 v51, v51\n"
    "v_mfma_f32_16x16x32_bf16 v[152:155], v[184:187], v[112:115], v[152:155]\n"
    "v_exp_f32 v52, v52\n"
    "v_exp_f32 v53, v53\n"
    "v_exp_f32 v54, v54\n"
    "v_mfma_f32_16x16x32_bf16 v[156:159], v[188:191], v[112:115], v[156:159]\n"
    "v_exp_f32 v55, v55\n"
    "v_exp_f32 v120, v120\n"
    "v_mfma_f32_16x16x32_bf16 v[164:167], v[236:239], v[112:115], v[164:167]\n"
    "s_lshl_b32 s98, s59, 4\n"
    "v_exp_f32 v121, v121\n"
    "v_exp_f32 v122, v122\n"
    "v_exp_f32 v123, v123\n"
    "v_exp_f32 v124, v124\n"
    "v_exp_f32 v125, v125\n"
    "v_exp_f32 v126, v126\n"
    "v_exp_f32 v127, v127\n"
    "v_cvt_pk_bf16_f32 v48, v48, v49\n"
    "v_cvt_pk_bf16_f32 v49, v50, v51\n"
    "v_cvt_pk_bf16_f32 v50, v52, v53\n"
    "v_cvt_pk_bf16_f32 v51, v54, v55\n"
    "v_cvt_pk_bf16_f32 v120, v120, v121\n"
    "v_cvt_pk_bf16_f32 v121, v122, v123\n"
    "buffer_load_dwordx4 v200, s[32:35], s98 offen lds\n"
    "v_cvt_pk_bf16_f32 v122, v124, v125\n"
    "v_cvt_pk_bf16_f32 v123, v126, v127\n"
    "v_pk_add_f32 v[56:57], v[56:57], v[168:169] op_sel_hi:[1,0] neg_lo:[0,1] neg_hi:[0,1]\n"
    "v_pk_add_f32 v[58:59], v[58:59], v[168:169] op_sel_hi:[1,0] neg_lo:[0,1] neg_hi:[0,1]\n"
    "v_pk_add_f32 v[60:61], v[60:61], v[168:169] op_sel_hi:[1,0] neg_lo:[0,1] neg_hi:[0,1]\n"
    "v_pk_add_f32 v[62:63], v[62:63], v[168:169] op_sel_hi:[1,0] neg_lo:[0,1] neg_hi:[0,1]\n"
    "v_pk_add_f32 v[128:129], v[128:129], v[170:171] op_sel_hi:[1,0] neg_lo:[0,1] neg_hi:[0,1]\n"
    "v_pk_add_f32 v[130:131], v[130:131], v[170:171] op_sel_hi:[1,0] neg_lo:[0,1] neg_hi:[0,1]\n"
    "v_pk_add_f32 v[132:133], v[132:133], v[170:171] op_sel_hi:[1,0] neg_lo:[0,1] neg_hi:[0,1]\n"
    "v_pk_add_f32 v[134:135], v[134:135], v[170:171] op_sel_hi:[1,0] neg_lo:[0,1] neg_hi:[0,1]\n"
    "s_waitcnt lgkmcnt(0)\n"
    "v_mfma_f32_16x16x32_bf16 v[144:147], v[176:179], v[48:51], v[144:147]\n"
    "ds_read_b128 v[184:187], v228 offset:38912\n"
    "buffer_load_dwordx4 v201, s[32:35], s98 offen offset:1024 lds\n"
    "ds_read_b128 v[188:191], v228 offset:39936\n"
    "ds_read_b128 v[236:239], v231 offset:112\n"
    "v_mfma_f32_16x16x32_bf16 v[148:151], v[180:183], v[48:51], v[148:151]\n"
    "v_exp_f32 v56, v56\n"
    "v_exp_f32 v57, v57\n"
    "v_mfma_f32_16x16x32_bf16 v[160:163], v[232:235], v[48:51], v[160:163]\n"
    "v_exp_f32 v58, v58\n"
    "v_exp_f32 v59, v59\n"
    "v_mfma_f32_16x16x32_bf16 v[152:155], v[176:179], v[120:123], v[152:155]\n"
    "v_exp_f32 v60, v60\n"
    "v_exp_f32 v61, v61\n"
    "v_exp_f32 v62, v62\n"
    "v_mfma_f32_16x16x32_bf16 v[156:159], v[180:183], v[120:123], v[156:159]\n"
    "buffer_load_dwordx4 v202, s[32:35], s98 offen offset:2048 lds\n"
    "v_exp_f32 v63, v63\n"
    "v_exp_f32 v128, v128\n"
    "v_mfma_f32_16x16x32_bf16 v[164:167], v[232:235], v[120:123], v[164:167]\n"
    "v_exp_f32 v129, v129\n"
    "v_exp_f32 v130, v130\n"
    "v_exp_f32 v131, v131\n"
    "v_exp_f32 v132, v132\n"
    "v_exp_f32 v133, v133\n"
    "v_exp_f32 v134, v134\n"
    "v_exp_f32 v135, v135\n"
    "v_cvt_pk_bf16_f32 v56, v56, v57\n"
    "v_cvt_pk_bf16_f32 v57, v58, v59\n"
    "v_cvt_pk_bf16_f32 v58, v60, v61\n"
    "buffer_load_dwordx4 v203, s[32:35], s98 offen offset:3072 lds\n"
    "v_cvt_pk_bf16_f32 v59, v62, v63\n"
    "v_cvt_pk_bf16_f32 v128, v128, v129\n"
    "v_cvt_pk_bf16_f32 v129, v130, v131\n"
    "v_cvt_pk_bf16_f32 v130, v132, v133\n"
    "v_cvt_pk_bf16_f32 v131, v134, v135\n"
    "v_pk_add_f32 v[136:137], v[136:137], v[170:171] op_sel_hi:[1,0] neg_lo:[0,1] neg_hi:[0,1]\n"
    "v_pk_add_f32 v[138:139], v[138:139], v[170:171] op_sel_hi:[1,0] neg_lo:[0,1] neg_hi:[0,1]\n"
    "s_waitcnt lgkmcnt(0)\n"
    "v_mfma_f32_16x16x32_bf16 v[144:147], v[184:187], v[56:59], v[144:147]\n"
    "ds_read_b128 v[176:179], v228 offset:40960\n"
    "ds_read_b128 v[180:183], v228 offset:41984\n"
    "ds_read_b128 v[232:235], v231 offset:128\n"
    "v_mfma_f32_16x16x32_bf16 v[148:151], v[188:191], v[56:59], v[148:151]\n"
    "v_mfma_f32_16x16x32_bf16 v[160:163], v[236:239], v[56:59], v[160:163]\n"
    "v_exp_f32 v136, v136\n"
    "v_mfma_f32_16x16x32_bf16 v[152:155], v[184:187], v[128:131], v[152:155]\n"
    "v_exp_f32 v137, v137\n"
    "v_mfma_f32_16x16x32_bf16 v[156:159], v[188:191], v[128:131], v[156:159]\n"
    "v_exp_f32 v138, v138\n"
    "v_mfma_f32_16x16x32_bf16 v[164:167], v[236:239], v[128:131], v[164:167]\n"
    "v_exp_f32 v139, v139\n"
    "v_cvt_pk_bf16_f32 v136, v136, v137\n"
    "v_cvt_pk_bf16_f32 v137, v138, v139\n"
    "v_bfi_b32 v138, v229, 0, v174\n"
    "v_bfi_b32 v139, v230, 0, v175\n"
    "s_waitcnt vmcnt(16)\n"
    "s_barrier\n"
    "s_waitcnt lgkmcnt(0)\n"
    "v_mfma_f32_16x16x32_bf16 v[144:147], v[176:179], v[64:67], v[144:147]\n"
    "v_mfma_f32_16x16x32_bf16 v[148:151], v[180:183], v[64:67], v[148:151]\n"
    "ds_read_b64_tr_b16 v[208:209], v224\n"
    "v_mfma_f32_16x16x32_bf16 v[160:163], v[232:235], v[64:67], v[160:163]\n"
    "ds_read_b64_tr_b16 v[210:211], v224 offset:4096\n"
    "ds_read_b64_tr_b16 v[212:213], v225\n"
    "v_mfma_f32_16x16x32_bf16 v[152:155], v[176:179], v[136:139], v[152:155]\n"
    "ds_read_b64_tr_b16 v[214:215], v225 offset:4096\n"
    "v_mfma_f32_16x16x32_bf16 v[156:159], v[180:183], v[136:139], v[156:159]\n"
    "ds_read_b64_tr_b16 v[216:217], v226\n"
    "ds_read_b64_tr_b16 v[218:219], v226 offset:4096\n"
    "v_mfma_f32_16x16x32_bf16 v[164:167], v[232:235], v[136:139], v[164:167]\n"
    "ds_read_b64_tr_b16 v[220:221], v227\n"
    "ds_read_b64_tr_b16 v[222:223], v227 offset:4096\n"
    "s_waitcnt lgkmcnt(4)\n"
    "v_mfma_f32_16x16x32_bf16 v[32:35], v[208:211], a[4:7], 0\n"
    "buffer_load_dwordx4 a[64:67], v192, s[44:47], s58 offen sc1\n"
    "s_mul_i32 s89, s53, 0\n"
    "v_mfma_f32_16x16x32_bf16 v[64:67], v[208:211], a[8:11], 0\n"
    "s_and_b32 s89, s89, 12\n"
    "s_lshr_b32 s90, 0xc952c952, s89\n"
    "v_mfma_f32_16x16x32_bf16 v[96:99], v[208:211], a[12:15], 0\n"
    "ds_read_b64_tr_b16 v[128:129], v204\n"
    "s_lshr_b32 s91, 0x32103210, s89\n"
    "v_mfma_f32_16x16x32_bf16 v[0:3], v[208:211], a[0:3], 0\n"
    "ds_read_b64_tr_b16 v[130:131], v204 offset:4096\n"
    "s_and_b32 s89, s89, 4\n"
    "v_mfma_f32_16x16x32_bf16 v[4:7], v[212:215], a[0:3], 0\n"
    "s_lshr_b32 s89, 0x6d6, s89\n"
    "s_bfe_u32 s92, s90, 0x40000\n"
    "v_mfma_f32_16x16x32_bf16 v[100:103], v[212:215], a[12:15], 0\n"
    "s_lshl_b32 s64, s92, 13\n"
    "s_bfe_u32 s92, s90, 0x40004\n"
    "v_mfma_f32_16x16x32_bf16 v[68:71], v[212:215], a[8:11], 0\n"
    "ds_read_b64_tr_b16 v[132:133], v241\n"
    "s_lshl_b32 s65, s92, 13\n"
    "v_mfma_f32_16x16x32_bf16 v[36:39], v[212:215], a[4:7], 0\n"
    "s_waitcnt lgkmcnt(3)\n"
    "ds_read_b64_tr_b16 v[134:135], v241 offset:4096\n"
    "v_mfma_f32_16x16x32_bf16 v[40:43], v[216:219], a[4:7], 0\n"
    "buffer_load_dwordx4 a[68:71], v193, s[44:47], s58 offen sc1\n"
    "s_bfe_u32 s92, s90, 0x40008\n"
    "v_mfma_f32_16x16x32_bf16 v[72:75], v[216:219], a[8:11], 0\n"
    "s_lshl_b32 s66, s92, 13\n"
    "s_bfe_u32 s92, s90, 0x4000c\n"
    "v_mfma_f32_16x16x32_bf16 v[104:107], v[216:219], a[12:15], 0\n"
    "ds_read_b64_tr_b16 v[136:137], v252\n"
    "s_lshl_b32 s67, s92, 13\n"
    "v_mfma_f32_16x16x32_bf16 v[8:11], v[216:219], a[0:3], 0\n"
    "ds_read_b64_tr_b16 v[138:139], v252 offset:4096\n"
    "s_bfe_u32 s92, s89, 0x40000\n"
    "v_mfma_f32_16x16x32_bf16 v[12:15], v[220:223], a[0:3], 0\n"
    "s_lshl_b32 s68, s92, 13\n"
    "s_bfe_u32 s92, s89, 0x40004\n"
    "v_mfma_f32_16x16x32_bf16 v[108:111], v[220:223], a[12:15], 0\n"
    "s_lshl_b32 s69, s92, 13\n"
    "s_bfe_u32 s92, s91, 0x40000\n"
    "v_mfma_f32_16x16x32_bf16 v[76:79], v[220:223], a[8:11], 0\n"
    "ds_read_b64_tr_b16 v[140:141], v253\n"
    "buffer_load_dwordx4 a[72:75], v194, s[44:47], s58 offen sc1\n"
    "v_mfma_f32_16x16x32_bf16 v[44:47], v[220:223], a[4:7], 0\n"
    "s_waitcnt lgkmcnt(3)\n"
    "ds_read_b64_tr_b16 v[142:143], v253 offset:4096\n"
    "v_mfma_f32_16x16x32_bf16 v[48:51], v[128:131], a[4:7], 0\n"
    "s_add_u32 s92, s92, 14\n"
    "s_lshl_b32 s76, s92, 13\n"
    "v_mfma_f32_16x16x32_bf16 v[80:83], v[128:131], a[8:11], 0\n"
    "s_bfe_u32 s92, s91, 0x40004\n"
    "s_add_u32 s92, s92, 14\n"
    "v_mfma_f32_16x16x32_bf16 v[112:115], v[128:131], a[12:15], 0\n"
    "ds_read_b64_tr_b16 v[208:209], v224 offset:8192\n"
    "s_lshl_b32 s77, s92, 13\n"
    "v_mfma_f32_16x16x32_bf16 v[16:19], v[128:131], a[0:3], 0\n"
    "ds_read_b64_tr_b16 v[210:211], v224 offset:12288\n"
    "s_bfe_u32 s92, s91, 0x40008\n"
    "v_mfma_f32_16x16x32_bf16 v[20:23], v[132:135], a[0:3], 0\n"
    "s_add_u32 s92, s92, 14\n"
    "s_lshl_b32 s78, s92, 13\n"
    "v_mfma_f32_16x16x32_bf16 v[116:119], v[132:135], a[12:15], 0\n"
    "buffer_load_dwordx4 a[76:79], v195, s[44:47], s58 offen sc1\n"
    "s_bfe_u32 s92, s91, 0x4000c\n"
    "v_mfma_f32_16x16x32_bf16 v[84:87], v[132:135], a[8:11], 0\n"
    "ds_read_b64_tr_b16 v[212:213], v225 offset:8192\n"
    "s_add_u32 s92, s92, 14\n"
    "v_mfma_f32_16x16x32_bf16 v[52:55], v[132:135], a[4:7], 0\n"
    "s_waitcnt lgkmcnt(3)\n"
    "ds_read_b64_tr_b16 v[214:215], v225 offset:12288\n"
    "v_mfma_f32_16x16x32_bf16 v[56:59], v[136:139], a[4:7], 0\n"
    "s_lshl_b32 s79, s92, 13\n"
    "v_add_u32 v228, 0x10000, v224\n"
    "v_mfma_f32_16x16x32_bf16 v[88:91], v[136:139], a[8:11], 0\n"
    "v_add_u32 v229, 0x10000, v225\n"
    "v_add_u32 v230, 0x10000, v226\n"
    "v_mfma_f32_16x16x32_bf16 v[120:123], v[136:139], a[12:15], 0\n"
    "ds_read_b64_tr_b16 v[216:217], v226 offset:8192\n"
    "v_add_u32 v231, 0x10000, v227\n"
    "v_mfma_f32_16x16x32_bf16 v[24:27], v[136:139], a[0:3], 0\n"
    "ds_read_b64_tr_b16 v[218:219], v226 offset:12288\n"
    "v_add_u32 v232, 0x10000, v204\n"
    "v_mfma_f32_16x16x32_bf16 v[28:31], v[140:143], a[0:3], 0\n"
    "buffer_load_dwordx4 a[80:83], v196, s[44:47], s58 offen sc1\n"
    "v_add_u32 v233, 0x10000, v241\n"
    "v_mfma_f32_16x16x32_bf16 v[124:127], v[140:143], a[12:15], 0\n"
    "v_add_u32 v234, 0x10000, v252\n"
    "v_add_u32 v235, 0x10000, v253\n"
    "v_mfma_f32_16x16x32_bf16 v[92:95], v[140:143], a[8:11], 0\n"
    "ds_read_b64_tr_b16 v[220:221], v227 offset:8192\n"
    "s_lshl_b32 s89, s49, 13\n"
    "v_mfma_f32_16x16x32_bf16 v[60:63], v[140:143], a[4:7], 0\n"
    "s_waitcnt lgkmcnt(3)\n"
    "ds_read_b64_tr_b16 v[222:223], v227 offset:12288\n"
    "v_mfma_f32_16x16x32_bf16 v[32:35], v[208:211], a[20:23], v[32:35]\n"
    "s_and_b32 s89, s89, 0x6000\n"
    "s_add_u32 s89, s89, s88\n"
    "v_mfma_f32_16x16x32_bf16 v[64:67], v[208:211], a[24:27], v[64:67]\n"
    "s_add_u32 s89, s89, 0x1d000\n"
    "v_mfma_f32_16x16x32_bf16 v[96:99], v[208:211], a[28:31], v[96:99]\n"
    "ds_read_b64_tr_b16 v[128:129], v204 offset:8192\n"
    "v_mfma_f32_16x16x32_bf16 v[0:3], v[208:211], a[16:19], v[0:3]\n"
    "ds_read_b64_tr_b16 v[130:131], v204 offset:12288\n"
    "buffer_load_dwordx4 a[84:87], v197, s[44:47], s58 offen sc1\n"
    "v_mfma_f32_16x16x32_bf16 v[4:7], v[212:215], a[16:19], v[4:7]\n"
    "v_rcp_f32 v160, v160\n"
    "v_mfma_f32_16x16x32_bf16 v[100:103], v[212:215], a[28:31], v[100:103]\n"
    "v_rcp_f32 v164, v164\n"
    "v_mfma_f32_16x16x32_bf16 v[68:71], v[212:215], a[24:27], v[68:71]\n"
    "ds_read_b64_tr_b16 v[132:133], v241 offset:8192\n"
    "v_mul_f32 v144, v144, v160\n"
    "v_mfma_f32_16x16x32_bf16 v[36:39], v[212:215], a[20:23], v[36:39]\n"
    "s_waitcnt lgkmcnt(3)\n"
    "ds_read_b64_tr_b16 v[134:135], v241 offset:12288\n"
    "v_mfma_f32_16x16x32_bf16 v[40:43], v[216:219], a[20:23], v[40:43]\n"
    "v_mul_f32 v145, v145, v160\n"
    "v_mul_f32 v146, v146, v160\n"
    "v_mfma_f32_16x16x32_bf16 v[72:75], v[216:219], a[24:27], v[72:75]\n"
    "v_mul_f32 v147, v147, v160\n"
    "v_mul_f32 v148, v148, v160\n"
    "v_mfma_f32_16x16x32_bf16 v[104:107], v[216:219], a[28:31], v[104:107]\n"
    "ds_read_b64_tr_b16 v[136:137], v252 offset:8192\n"
    "buffer_load_dwordx4 a[88:91], v198, s[44:47], s58 offen sc1\n"
    "v_mfma_f32_16x16x32_bf16 v[8:11], v[216:219], a[16:19], v[8:11]\n"
    "ds_read_b64_tr_b16 v[138:139], v252 offset:12288\n"
    "v_mul_f32 v149, v149, v160\n"
    "v_mfma_f32_16x16x32_bf16 v[12:15], v[220:223], a[16:19], v[12:15]\n"
    "v_mul_f32 v150, v150, v160\n"
    "v_mul_f32 v151, v151, v160\n"
    "v_mfma_f32_16x16x32_bf16 v[108:111], v[220:223], a[28:31], v[108:111]\n"
    "v_cvt_pk_bf16_f32 v144, v144, v145\n"
    "v_cvt_pk_bf16_f32 v145, v146, v147\n"
    "v_mfma_f32_16x16x32_bf16 v[76:79], v[220:223], a[24:27], v[76:79]\n"
    "ds_read_b64_tr_b16 v[140:141], v253 offset:8192\n"
    "v_cvt_pk_bf16_f32 v146, v148, v149\n"
    "v_mfma_f32_16x16x32_bf16 v[44:47], v[220:223], a[20:23], v[44:47]\n"
    "s_waitcnt lgkmcnt(3)\n"
    "ds_read_b64_tr_b16 v[142:143], v253 offset:12288\n"
    "v_mfma_f32_16x16x32_bf16 v[48:51], v[128:131], a[20:23], v[48:51]\n"
    "v_cvt_pk_bf16_f32 v147, v150, v151\n"
    "buffer_store_dwordx4 v[144:147], v240, s[44:47], s89 offen sc1\n"
    "v_mfma_f32_16x16x32_bf16 v[80:83], v[128:131], a[24:27], v[80:83]\n"
    "buffer_load_dwordx4 a[92:95], v199, s[44:47], s58 offen sc1\n"
    "v_mul_f32 v152, v152, v164\n"
    "v_mfma_f32_16x16x32_bf16 v[112:115], v[128:131], a[28:31], v[112:115]\n"
    "ds_read_b64_tr_b16 v[208:209], v224 offset:16384\n"
    "v_mul_f32 v153, v153, v164\n"
    "v_mfma_f32_16x16x32_bf16 v[16:19], v[128:131], a[16:19], v[16:19]\n"
    "ds_read_b64_tr_b16 v[210:211], v224 offset:20480\n"
    "v_mul_f32 v154, v154, v164\n"
    "v_mfma_f32_16x16x32_bf16 v[20:23], v[132:135], a[16:19], v[20:23]\n"
    "v_mul_f32 v155, v155, v164\n"
    "v_mul_f32 v156, v156, v164\n"
    "v_mfma_f32_16x16x32_bf16 v[116:119], v[132:135], a[28:31], v[116:119]\n"
    "v_mul_f32 v157, v157, v164\n"
    "v_mul_f32 v158, v158, v164\n"
    "v_mfma_f32_16x16x32_bf16 v[84:87], v[132:135], a[24:27], v[84:87]\n"
    "ds_read_b64_tr_b16 v[212:213], v225 offset:16384\n"
    "v_mul_f32 v159, v159, v164\n"
    "v_mfma_f32_16x16x32_bf16 v[52:55], v[132:135], a[20:23], v[52:55]\n"
    "s_waitcnt lgkmcnt(3)\n"
    "ds_read_b64_tr_b16 v[214:215], v225 offset:20480\n"
    "v_mfma_f32_16x16x32_bf16 v[56:59], v[136:139], a[20:23], v[56:59]\n"
    "buffer_load_dwordx4 a[96:99], v192, s[44:47], s59 offen sc1\n"
    "v_cvt_pk_bf16_f32 v152, v152, v153\n"
    "v_mfma_f32_16x16x32_bf16 v[88:91], v[136:139], a[24:27], v[88:91]\n"
    "v_cvt_pk_bf16_f32 v153, v154, v155\n"
    "v_cvt_pk_bf16_f32 v154, v156, v157\n"
    "v_mfma_f32_16x16x32_bf16 v[120:123], v[136:139], a[28:31], v[120:123]\n"
    "ds_read_b64_tr_b16 v[216:217], v226 offset:16384\n"
    "v_cvt_pk_bf16_f32 v155, v158, v159\n"
    "v_mfma_f32_16x16x32_bf16 v[24:27], v[136:139], a[16:19], v[24:27]\n"
    "ds_read_b64_tr_b16 v[218:219], v226 offset:20480\n"
    "buffer_store_dwordx4 v[152:155], v240, s[44:47], s89 offen offset:1024 sc1\n"
    "v_mfma_f32_16x16x32_bf16 v[28:31], v[140:143], a[16:19], v[28:31]\n"
    "v_mfma_f32_16x16x32_bf16 v[124:127], v[140:143], a[28:31], v[124:127]\n"
    "v_mfma_f32_16x16x32_bf16 v[92:95], v[140:143], a[24:27], v[92:95]\n"
    "ds_read_b64_tr_b16 v[220:221], v227 offset:16384\n"
    "v_mfma_f32_16x16x32_bf16 v[60:63], v[140:143], a[20:23], v[60:63]\n"
    "s_waitcnt vmcnt(23) lgkmcnt(3)\n"
    "ds_read_b64_tr_b16 v[222:223], v227 offset:20480\n"
    "v_mfma_f32_16x16x32_bf16 v[32:35], v[208:211], a[36:39], v[32:35]\n"
    "buffer_load_dword v246, v245, s[44:47], 0 offen sc1\n"
    "buffer_load_dwordx4 a[100:103], v193, s[44:47], s59 offen sc1\n"
    "v_mfma_f32_16x16x32_bf16 v[64:67], v[208:211], a[40:43], v[64:67]\n"
    "v_mfma_f32_16x16x32_bf16 v[96:99], v[208:211], a[44:47], v[96:99]\n"
    "ds_read_b64_tr_b16 v[128:129], v204 offset:16384\n"
    "v_mfma_f32_16x16x32_bf16 v[0:3], v[208:211], a[32:35], v[0:3]\n"
    "ds_read_b64_tr_b16 v[130:131], v204 offset:20480\n"
    "v_mfma_f32_16x16x32_bf16 v[4:7], v[212:215], a[32:35], v[4:7]\n"
    "v_mfma_f32_16x16x32_bf16 v[100:103], v[212:215], a[44:47], v[100:103]\n"
    "v_mfma_f32_16x16x32_bf16 v[68:71], v[212:215], a[40:43], v[68:71]\n"
    "ds_read_b64_tr_b16 v[132:133], v241 offset:16384\n"
    "buffer_load_dwordx4 a[104:107], v194, s[44:47], s59 offen sc1\n"
    "v_mfma_f32_16x16x32_bf16 v[36:39], v[212:215], a[36:39], v[36:39]\n"
    "s_waitcnt lgkmcnt(3)\n"
    "ds_read_b64_tr_b16 v[134:135], v241 offset:20480\n"
    "v_mfma_f32_16x16x32_bf16 v[40:43], v[216:219], a[36:39], v[40:43]\n"
    "v_mfma_f32_16x16x32_bf16 v[72:75], v[216:219], a[40:43], v[72:75]\n"
    "v_mfma_f32_16x16x32_bf16 v[104:107], v[216:219], a[44:47], v[104:107]\n"
    "ds_read_b64_tr_b16 v[136:137], v252 offset:16384\n"
    "v_mfma_f32_16x16x32_bf16 v[8:11], v[216:219], a[32:35], v[8:11]\n"
    "ds_read_b64_tr_b16 v[138:139], v252 offset:20480\n"
    "v_mfma_f32_16x16x32_bf16 v[12:15], v[220:223], a[32:35], v[12:15]\n"
    "v_mfma_f32_16x16x32_bf16 v[108:111], v[220:223], a[44:47], v[108:111]\n"
    "buffer_load_dwordx4 a[108:111], v195, s[44:47], s59 offen sc1\n"
    "v_mfma_f32_16x16x32_bf16 v[76:79], v[220:223], a[40:43], v[76:79]\n"
    "ds_read_b64_tr_b16 v[140:141], v253 offset:16384\n"
    "v_mfma_f32_16x16x32_bf16 v[44:47], v[220:223], a[36:39], v[44:47]\n"
    "s_waitcnt lgkmcnt(3)\n"
    "ds_read_b64_tr_b16 v[142:143], v253 offset:20480\n"
    "v_mfma_f32_16x16x32_bf16 v[48:51], v[128:131], a[36:39], v[48:51]\n"
    "v_mfma_f32_16x16x32_bf16 v[80:83], v[128:131], a[40:43], v[80:83]\n"
    "v_mfma_f32_16x16x32_bf16 v[112:115], v[128:131], a[44:47], v[112:115]\n"
    "ds_read_b64_tr_b16 v[208:209], v224 offset:24576\n"
    "v_mfma_f32_16x16x32_bf16 v[16:19], v[128:131], a[32:35], v[16:19]\n"
    "ds_read_b64_tr_b16 v[210:211], v224 offset:28672\n"
    "v_mfma_f32_16x16x32_bf16 v[20:23], v[132:135], a[32:35], v[20:23]\n"
    "buffer_load_dwordx4 a[112:115], v196, s[44:47], s59 offen sc1\n"
    "v_mfma_f32_16x16x32_bf16 v[116:119], v[132:135], a[44:47], v[116:119]\n"
    "v_mfma_f32_16x16x32_bf16 v[84:87], v[132:135], a[40:43], v[84:87]\n"
    "ds_read_b64_tr_b16 v[212:213], v225 offset:24576\n"
    "v_mfma_f32_16x16x32_bf16 v[52:55], v[132:135], a[36:39], v[52:55]\n"
    "s_waitcnt lgkmcnt(3)\n"
    "ds_read_b64_tr_b16 v[214:215], v225 offset:28672\n"
    "v_mfma_f32_16x16x32_bf16 v[56:59], v[136:139], a[36:39], v[56:59]\n"
    "v_mfma_f32_16x16x32_bf16 v[88:91], v[136:139], a[40:43], v[88:91]\n"
    "v_mfma_f32_16x16x32_bf16 v[120:123], v[136:139], a[44:47], v[120:123]\n"
    "ds_read_b64_tr_b16 v[216:217], v226 offset:24576\n"
    "v_mfma_f32_16x16x32_bf16 v[24:27], v[136:139], a[32:35], v[24:27]\n"
    "ds_read_b64_tr_b16 v[218:219], v226 offset:28672\n"
    "buffer_load_dwordx4 a[116:119], v197, s[44:47], s59 offen sc1\n"
    "v_mfma_f32_16x16x32_bf16 v[28:31], v[140:143], a[32:35], v[28:31]\n"
    "v_mfma_f32_16x16x32_bf16 v[124:127], v[140:143], a[44:47], v[124:127]\n"
    "v_mfma_f32_16x16x32_bf16 v[92:95], v[140:143], a[40:43], v[92:95]\n"
    "ds_read_b64_tr_b16 v[220:221], v227 offset:24576\n"
    "v_mfma_f32_16x16x32_bf16 v[60:63], v[140:143], a[36:39], v[60:63]\n"
    "s_waitcnt vmcnt(25) lgkmcnt(3)\n"
    "ds_read_b64_tr_b16 v[222:223], v227 offset:28672\n"
    "v_mfma_f32_16x16x32_bf16 v[32:35], v[208:211], a[52:55], v[32:35]\n"
    "v_mfma_f32_16x16x32_bf16 v[64:67], v[208:211], a[56:59], v[64:67]\n"
    "v_mfma_f32_16x16x32_bf16 v[96:99], v[208:211], a[60:63], v[96:99]\n"
    "ds_read_b64_tr_b16 v[128:129], v204 offset:24576\n"
    "buffer_load_dwordx4 a[120:123], v198, s[44:47], s59 offen sc1\n"
    "v_mfma_f32_16x16x32_bf16 v[0:3], v[208:211], a[48:51], v[0:3]\n"
    "ds_read_b64_tr_b16 v[130:131], v204 offset:28672\n"
    "s_waitcnt vmcnt(6)\n"
    "v_mfma_f32_16x16x32_bf16 v[4:7], v[212:215], a[48:51], v[4:7]\n"
    "v_xor_b32 v249, v246, v244\n"
    "v_and_b32 v250, 63, v246\n"
    "v_mfma_f32_16x16x32_bf16 v[100:103], v[212:215], a[60:63], v[100:103]\n"
    "v_cmp_lt_u32 vcc, 0xff, v249\n"
    "v_cmp_le_u32 s[96:97], 4, v250\n"
    "v_mfma_f32_16x16x32_bf16 v[68:71], v[212:215], a[56:59], v[68:71]\n"
    "ds_read_b64_tr_b16 v[132:133], v241 offset:24576\n"
    "s_and_b32 s96, vcc_lo, s96\n"
    "v_mfma_f32_16x16x32_bf16 v[36:39], v[212:215], a[52:55], v[36:39]\n"
    "s_waitcnt lgkmcnt(3)\n"
    "ds_read_b64_tr_b16 v[134:135], v241 offset:28672\n"
    "v_mfma_f32_16x16x32_bf16 v[40:43], v[216:219], a[52:55], v[40:43]\n"
    "s_andn2_b32 s96, s83, s96\n"
    "s_andn2_b32 s97, s80, vcc_lo\n"
    "v_mfma_f32_16x16x32_bf16 v[72:75], v[216:219], a[56:59], v[72:75]\n"
    "s_or_b32 s96, s96, s97\n"
    "s_cbranch_scc1 .Lslow_h23\n"
    ".Lret_h23:\n"
    "v_mfma_f32_16x16x32_bf16 v[104:107], v[216:219], a[60:63], v[104:107]\n"
    "ds_read_b64_tr_b16 v[136:137], v252 offset:24576\n"
    "buffer_load_dwordx4 a[124:127], v199, s[44:47], s59 offen sc1\n"
    "v_mfma_f32_16x16x32_bf16 v[8:11], v[216:219], a[48:51], v[8:11]\n"
    "ds_read_b64_tr_b16 v[138:139], v252 offset:28672\n"
    "s_add_u32 m0, s85, 0x10000\n"
    "v_mfma_f32_16x16x32_bf16 v[12:15], v[220:223], a[48:51], v[12:15]\n"
    "s_lshl_b32 s98, s60, 4\n"
    "v_mfma_f32_16x16x32_bf16 v[108:111], v[220:223], a[60:63], v[108:111]\n"
    "v_mfma_f32_16x16x32_bf16 v[76:79], v[220:223], a[56:59], v[76:79]\n"
    "ds_read_b64_tr_b16 v[140:141], v253 offset:24576\n"
    "v_mfma_f32_16x16x32_bf16 v[44:47], v[220:223], a[52:55], v[44:47]\n"
    "s_waitcnt lgkmcnt(3)\n"
    "v_mfma_f32_16x16x32_bf16 v[48:51], v[128:131], a[52:55], v[48:51]\n"
    "s_barrier\n"
    "ds_read_b64_tr_b16 v[142:143], v253 offset:28672\n"
    "v_mfma_f32_16x16x32_bf16 v[80:83], v[128:131], a[56:59], v[80:83]\n"
    "buffer_load_dwordx4 a[128:131], v192, s[44:47], s60 offen sc1\n"
    "v_mfma_f32_16x16x32_bf16 v[112:115], v[128:131], a[60:63], v[112:115]\n"
    "ds_read_b64_tr_b16 v[208:209], v224 offset:32768\n"
    "v_mfma_f32_16x16x32_bf16 v[16:19], v[128:131], a[48:51], v[16:19]\n"
    "ds_read_b64_tr_b16 v[210:211], v224 offset:36864\n"
    "v_mfma_f32_16x16x32_bf16 v[20:23], v[132:135], a[48:51], v[20:23]\n"
    "v_mfma_f32_16x16x32_bf16 v[116:119], v[132:135], a[60:63], v[116:119]\n"
    "buffer_load_dwordx4 v200, s[32:35], s98 offen lds\n"
    "v_mfma_f32_16x16x32_bf16 v[84:87], v[132:135], a[56:59], v[84:87]\n"
    "ds_read_b64_tr_b16 v[212:213], v225 offset:32768\n"
    "v_mfma_f32_16x16x32_bf16 v[52:55], v[132:135], a[52:55], v[52:55]\n"
    "s_waitcnt lgkmcnt(3)\n"
    "ds_read_b64_tr_b16 v[214:215], v225 offset:36864\n"
    "v_mfma_f32_16x16x32_bf16 v[56:59], v[136:139], a[52:55], v[56:59]\n"
    "buffer_load_dwordx4 a[132:135], v193, s[44:47], s60 offen sc1\n"
    "v_mfma_f32_16x16x32_bf16 v[88:91], v[136:139], a[56:59], v[88:91]\n"
    "v_mfma_f32_16x16x32_bf16 v[120:123], v[136:139], a[60:63], v[120:123]\n"
    "ds_read_b64_tr_b16 v[216:217], v226 offset:32768\n"
    "v_mfma_f32_16x16x32_bf16 v[24:27], v[136:139], a[48:51], v[24:27]\n"
    "ds_read_b64_tr_b16 v[218:219], v226 offset:36864\n"
    "v_mfma_f32_16x16x32_bf16 v[28:31], v[140:143], a[48:51], v[28:31]\n"
    "v_mfma_f32_16x16x32_bf16 v[124:127], v[140:143], a[60:63], v[124:127]\n"
    "buffer_load_dwordx4 v201, s[32:35], s98 offen offset:1024 lds\n"
    "v_mfma_f32_16x16x32_bf16 v[92:95], v[140:143], a[56:59], v[92:95]\n"
    "ds_read_b64_tr_b16 v[220:221], v227 offset:32768\n"
    "buffer_load_dwordx4 a[136:139], v194, s[44:47], s60 offen sc1\n"
    "v_mfma_f32_16x16x32_bf16 v[60:63], v[140:143], a[52:55], v[60:63]\n"
    "s_waitcnt lgkmcnt(3)\n"
    "ds_read_b64_tr_b16 v[222:223], v227 offset:36864\n"
    "v_mfma_f32_16x16x32_bf16 v[32:35], v[208:211], a[68:71], v[32:35]\n"
    "s_getpc_b64 s[92:93]\n"
    ".Lct6:\n"
    "s_and_b32 s93, s93, 0xffff\n"
    "v_mfma_f32_16x16x32_bf16 v[64:67], v[208:211], a[72:75], v[64:67]\n"
    "s_mov_b32 s94, .Lend-.Lct6\n"
    "s_min_u32 s94, s94, 0x8000\n"
    "v_mfma_f32_16x16x32_bf16 v[96:99], v[208:211], a[76:79], v[96:99]\n"
    "ds_read_b64_tr_b16 v[128:129], v204 offset:32768\n"
    "s_mov_b32 s95, 0x20000\n"
    "v_mfma_f32_16x16x32_bf16 v[0:3], v[208:211], a[64:67], v[0:3]\n"
    "ds_read_b64_tr_b16 v[130:131], v204 offset:36864\n"
    "s_and_b32 s91, s13, 0x7f\n"
    "v_mfma_f32_16x16x32_bf16 v[4:7], v[212:215], a[64:67], v[4:7]\n"
    "s_lshl_b32 s91, s91, 10\n"
    "buffer_load_dword v206, v240, s[92:95], s91 offen\n"
    "v_mfma_f32_16x16x32_bf16 v[100:103], v[212:215], a[76:79], v[100:103]\n"
    "buffer_load_dwordx4 v202, s[32:35], s98 offen offset:2048 lds\n"
    "buffer_load_dwordx4 a[140:143], v195, s[44:47], s60 offen sc1\n"
    "v_mfma_f32_16x16x32_bf16 v[68:71], v[212:215], a[72:75], v[68:71]\n"
    "ds_read_b64_tr_b16 v[132:133], v241 offset:32768\n"
    "v_mfma_f32_16x16x32_bf16 v[36:39], v[212:215], a[68:71], v[36:39]\n"
    "s_waitcnt lgkmcnt(3)\n"
    "ds_read_b64_tr_b16 v[134:135], v241 offset:36864\n"
    "v_mfma_f32_16x16x32_bf16 v[40:43], v[216:219], a[68:71], v[40:43]\n"
    "v_mfma_f32_16x16x32_bf16 v[72:75], v[216:219], a[72:75], v[72:75]\n"
    "v_mfma_f32_16x16x32_bf16 v[104:107], v[216:219], a[76:79], v[104:107]\n"
    "ds_read_b64_tr_b16 v[136:137], v252 offset:32768\n"
    "v_mfma_f32_16x16x32_bf16 v[8:11], v[216:219], a[64:67], v[8:11]\n"
    "ds_read_b64_tr_b16 v[138:139], v252 offset:36864\n"
    "v_mfma_f32_16x16x32_bf16 v[12:15], v[220:223], a[64:67], v[12:15]\n"
    "buffer_load_dwordx4 a[144:147], v196, s[44:47], s60 offen sc1\n"
    "v_mfma_f32_16x16x32_bf16 v[108:111], v[220:223], a[76:79], v[108:111]\n"
    "buffer_load_dwordx4 v203, s[32:35], s98 offen offset:3072 lds\n"
    "s_add_u32 m0, s85, 0x14000\n"
    "v_mfma_f32_16x16x32_bf16 v[76:79], v[220:223], a[72:75], v[76:79]\n"
    "ds_read_b64_tr_b16 v[140:141], v253 offset:32768\n"
    "s_lshl_b32 s98, s61, 4\n"
    "v_mfma_f32_16x16x32_bf16 v[44:47], v[220:223], a[68:71], v[44:47]\n"
    "s_waitcnt lgkmcnt(3)\n"
    "ds_read_b64_tr_b16 v[142:143], v253 offset:36864\n"
    "v_mfma_f32_16x16x32_bf16 v[48:51], v[128:131], a[68:71], v[48:51]\n"
    "v_mfma_f32_16x16x32_bf16 v[80:83], v[128:131], a[72:75], v[80:83]\n"
    "v_mfma_f32_16x16x32_bf16 v[112:115], v[128:131], a[76:79], v[112:115]\n"
    "ds_read_b64_tr_b16 v[208:209], v224 offset:40960\n"
    "v_mfma_f32_16x16x32_bf16 v[16:19], v[128:131], a[64:67], v[16:19]\n"
    "ds_read_b64_tr_b16 v[210:211], v224 offset:45056\n"
    "buffer_load_dwordx4 a[148:151], v197, s[44:47], s60 offen sc1\n"
    "v_mfma_f32_16x16x32_bf16 v[20:23], v[132:135], a[64:67], v[20:23]\n"
    "v_mfma_f32_16x16x32_bf16 v[116:119], v[132:135], a[76:79], v[116:119]\n"
    "buffer_load_dwordx4 v200, s[32:35], s98 offen lds\n"
    "v_mfma_f32_16x16x32_bf16 v[84:87], v[132:135], a[72:75], v[84:87]\n"
    "ds_read_b64_tr_b16 v[212:213], v225 offset:40960\n"
    "v_mfma_f32_16x16x32_bf16 v[52:55], v[132:135], a[68:71], v[52:55]\n"
    "s_waitcnt lgkmcnt(3)\n"
    "ds_read_b64_tr_b16 v[214:215], v225 offset:45056\n"
    "v_mfma_f32_16x16x32_bf16 v[56:59], v[136:139], a[68:71], v[56:59]\n"
    "v_mfma_f32_16x16x32_bf16 v[88:91], v[136:139], a[72:75], v[88:91]\n"
    "v_mfma_f32_16x16x32_bf16 v[120:123], v[136:139], a[76:79], v[120:123]\n"
    "ds_read_b64_tr_b16 v[216:217], v226 offset:40960\n"
    "buffer_load_dwordx4 a[152:155], v198, s[44:47], s60 offen sc1\n"
    "v_mfma_f32_16x16x32_bf16 v[24:27], v[136:139], a[64:67], v[24:27]\n"
    "ds_read_b64_tr_b16 v[218:219], v226 offset:45056\n"
    "v_mfma_f32_16x16x32_bf16 v[28:31], v[140:143], a[64:67], v[28:31]\n"
    "v_mfma_f32_16x16x32_bf16 v[124:127], v[140:143], a[76:79], v[124:127]\n"
    "buffer_load_dwordx4 v201, s[32:35], s98 offen offset:1024 lds\n"
    "v_mfma_f32_16x16x32_bf16 v[92:95], v[140:143], a[72:75], v[92:95]\n"
    "ds_read_b64_tr_b16 v[220:221], v227 offset:40960\n"
    "v_mfma_f32_16x16x32_bf16 v[60:63], v[140:143], a[68:71], v[60:63]\n"
    "s_waitcnt lgkmcnt(3)\n"
    "ds_read_b64_tr_b16 v[222:223], v227 offset:45056\n"
    "v_mfma_f32_16x16x32_bf16 v[32:35], v[208:211], a[84:87], v[32:35]\n"
    "v_mfma_f32_16x16x32_bf16 v[64:67], v[208:211], a[88:91], v[64:67]\n"
    "buffer_load_dwordx4 a[156:159], v199, s[44:47], s60 offen sc1\n"
    "v_mfma_f32_16x16x32_bf16 v[96:99], v[208:211], a[92:95], v[96:99]\n"
    "ds_read_b64_tr_b16 v[128:129], v204 offset:40960\n"
    "v_mfma_f32_16x16x32_bf16 v[0:3], v[208:211], a[80:83], v[0:3]\n"
    "ds_read_b64_tr_b16 v[130:131], v204 offset:45056\n"
    "v_mfma_f32_16x16x32_bf16 v[4:7], v[212:215], a[80:83], v[4:7]\n"
    "v_mfma_f32_16x16x32_bf16 v[100:103], v[212:215], a[92:95], v[100:103]\n"
    "buffer_load_dwordx4 v202, s[32:35], s98 offen offset:2048 lds\n"
    "v_mfma_f32_16x16x32_bf16 v[68:71], v[212:215], a[88:91], v[68:71]\n"
    "ds_read_b64_tr_b16 v[132:133], v241 offset:40960\n"
    "v_mfma_f32_16x16x32_bf16 v[36:39], v[212:215], a[84:87], v[36:39]\n"
    "s_waitcnt lgkmcnt(3)\n"
    "ds_read_b64_tr_b16 v[134:135], v241 offset:45056\n"
    "v_mfma_f32_16x16x32_bf16 v[40:43], v[216:219], a[84:87], v[40:43]\n"
    "buffer_load_dwordx4 a[160:163], v192, s[44:47], s61 offen sc1\n"
    "v_mfma_f32_16x16x32_bf16 v[72:75], v[216:219], a[88:91], v[72:75]\n"
    "v_mfma_f32_16x16x32_bf16 v[104:107], v[216:219], a[92:95], v[104:107]\n"
    "ds_read_b64_tr_b16 v[136:137], v252 offset:40960\n"
    "v_mfma_f32_16x16x32_bf16 v[8:11], v[216:219], a[80:83], v[8:11]\n"
    "ds_read_b64_tr_b16 v[138:139], v252 offset:45056\n"
    "v_mfma_f32_16x16x32_bf16 v[12:15], v[220:223], a[80:83], v[12:15]\n"
    "v_mfma_f32_16x16x32_bf16 v[108:111], v[220:223], a[92:95], v[108:111]\n"
    "buffer_load_dwordx4 v203, s[32:35], s98 offen offset:3072 lds\n"
    "s_add_u32 m0, s85, 0x18000\n"
    "v_mfma_f32_16x16x32_bf16 v[76:79], v[220:223], a[88:91], v[76:79]\n"
    "ds_read_b64_tr_b16 v[140:141], v253 offset:40960\n"
    "s_lshl_b32 s98, s62, 4\n"
    "v_mfma_f32_16x16x32_bf16 v[44:47], v[220:223], a[84:87], v[44:47]\n"
    "s_waitcnt lgkmcnt(3)\n"
    "ds_read_b64_tr_b16 v[142:143], v253 offset:45056\n"
    "v_mfma_f32_16x16x32_bf16 v[48:51], v[128:131], a[84:87], v[48:51]\n"
    "buffer_load_dwordx4 a[164:167], v193, s[44:47], s61 offen sc1\n"
    "v_mfma_f32_16x16x32_bf16 v[80:83], v[128:131], a[88:91], v[80:83]\n"
    "v_mfma_f32_16x16x32_bf16 v[112:115], v[128:131], a[92:95], v[112:115]\n"
    "ds_read_b64_tr_b16 v[208:209], v224 offset:49152\n"
    "v_mfma_f32_16x16x32_bf16 v[16:19], v[128:131], a[80:83], v[16:19]\n"
    "ds_read_b64_tr_b16 v[210:211], v224 offset:53248\n"
    "v_mfma_f32_16x16x32_bf16 v[20:23], v[132:135], a[80:83], v[20:23]\n"
    "v_mfma_f32_16x16x32_bf16 v[116:119], v[132:135], a[92:95], v[116:119]\n"
    "buffer_load_dwordx4 v200, s[32:35], s98 offen lds\n"
    "v_mfma_f32_16x16x32_bf16 v[84:87], v[132:135], a[88:91], v[84:87]\n"
    "ds_read_b64_tr_b16 v[212:213], v225 offset:49152\n"
    "buffer_load_dwordx4 a[168:171], v194, s[44:47], s61 offen sc1\n"
    "v_mfma_f32_16x16x32_bf16 v[52:55], v[132:135], a[84:87], v[52:55]\n"
    "s_waitcnt lgkmcnt(3)\n"
    "ds_read_b64_tr_b16 v[214:215], v225 offset:53248\n"
    "v_mfma_f32_16x16x32_bf16 v[56:59], v[136:139], a[84:87], v[56:59]\n"
    "v_mfma_f32_16x16x32_bf16 v[88:91], v[136:139], a[88:91], v[88:91]\n"
    "v_mfma_f32_16x16x32_bf16 v[120:123], v[136:139], a[92:95], v[120:123]\n"
    "ds_read_b64_tr_b16 v[216:217], v226 offset:49152\n"
    "v_mfma_f32_16x16x32_bf16 v[24:27], v[136:139], a[80:83], v[24:27]\n"
    "ds_read_b64_tr_b16 v[218:219], v226 offset:53248\n"
    "v_mfma_f32_16x16x32_bf16 v[28:31], v[140:143], a[80:83], v[28:31]\n"
    "v_mfma_f32_16x16x32_bf16 v[124:127], v[140:143], a[92:95], v[124:127]\n"
    "buffer_load_dwordx4 v201, s[32:35], s98 offen offset:1024 lds\n"
    "buffer_load_dwordx4 a[172:175], v195, s[44:47], s61 offen sc1\n"
    "v_mfma_f32_16x16x32_bf16 v[92:95], v[140:143], a[88:91], v[92:95]\n"
    "ds_read_b64_tr_b16 v[220:221], v227 offset:49152\n"
    "v_mfma_f32_16x16x32_bf16 v[60:63], v[140:143], a[84:87], v[60:63]\n"
    "s_waitcnt vmcnt(27) lgkmcnt(3)\n"
    "ds_read_b64_tr_b16 v[222:223], v227 offset:53248\n"
    "v_mfma_f32_16x16x32_bf16 v[32:35], v[208:211], a[100:103], v[32:35]\n"
    "v_mfma_f32_16x16x32_bf16 v[64:67], v[208:211], a[104:107], v[64:67]\n"
    "v_mfma_f32_16x16x32_bf16 v[96:99], v[208:211], a[108:111], v[96:99]\n"
    "ds_read_b64_tr_b16 v[128:129], v204 offset:49152\n"
    "v_mfma_f32_16x16x32_bf16 v[0:3], v[208:211], a[96:99], v[0:3]\n"
    "ds_read_b64_tr_b16 v[130:131], v204 offset:53248\n"
    "v_mfma_f32_16x16x32_bf16 v[4:7], v[212:215], a[96:99], v[4:7]\n"
    "buffer_load_dwordx4 a[176:179], v196, s[44:47], s61 offen sc1\n"
    "v_mfma_f32_16x16x32_bf16 v[100:103], v[212:215], a[108:111], v[100:103]\n"
    "buffer_load_dwordx4 v202, s[32:35], s98 offen offset:2048 lds\n"
    "v_mfma_f32_16x16x32_bf16 v[68:71], v[212:215], a[104:107], v[68:71]\n"
    "ds_read_b64_tr_b16 v[132:133], v241 offset:49152\n"
    "v_mfma_f32_16x16x32_bf16 v[36:39], v[212:215], a[100:103], v[36:39]\n"
    "s_waitcnt lgkmcnt(3)\n"
    "ds_read_b64_tr_b16 v[134:135], v241 offset:53248\n"
    "v_mfma_f32_16x16x32_bf16 v[40:43], v[216:219], a[100:103], v[40:43]\n"
    "v_mfma_f32_16x16x32_bf16 v[72:75], v[216:219], a[104:107], v[72:75]\n"
    "v_mfma_f32_16x16x32_bf16 v[104:107], v[216:219], a[108:111], v[104:107]\n"
    "ds_read_b64_tr_b16 v[136:137], v252 offset:49152\n"
    "v_mfma_f32_16x16x32_bf16 v[8:11], v[216:219], a[96:99], v[8:11]\n"
    "ds_read_b64_tr_b16 v[138:139], v252 offset:53248\n"
    "buffer_load_dwordx4 a[180:183], v197, s[44:47], s61 offen sc1\n"
    "v_mfma_f32_16x16x32_bf16 v[12:15], v[220:223], a[96:99], v[12:15]\n"
    "v_mfma_f32_16x16x32_bf16 v[108:111], v[220:223], a[108:111], v[108:111]\n"
    "buffer_load_dwordx4 v203, s[32:35], s98 offen offset:3072 lds\n"
    "s_add_u32 m0, s85, 0x1c000\n"
    "v_mfma_f32_16x16x32_bf16 v[76:79], v[220:223], a[104:107], v[76:79]\n"
    "ds_read_b64_tr_b16 v[140:141], v253 offset:49152\n"
    "s_lshl_b32 s98, s63, 4\n"
    "v_mfma_f32_16x16x32_bf16 v[44:47], v[220:223], a[100:103], v[44:47]\n"
    "s_waitcnt lgkmcnt(3)\n"
    "ds_read_b64_tr_b16 v[142:143], v253 offset:53248\n"
    "v_mfma_f32_16x16x32_bf16 v[48:51], v[128:131], a[100:103], v[48:51]\n"
    "v_mfma_f32_16x16x32_bf16 v[80:83], v[128:131], a[104:107], v[80:83]\n"
    "v_mfma_f32_16x16x32_bf16 v[112:115], v[128:131], a[108:111], v[112:115]\n"
    "ds_read_b64_tr_b16 v[208:209], v224 offset:57344\n"
    "buffer_load_dwordx4 a[184:187], v198, s[44:47], s61 offen sc1\n"
    "v_mfma_f32_16x16x32_bf16 v[16:19], v[128:131], a[96:99], v[16:19]\n"
    "ds_read_b64_tr_b16 v[210:211], v224 offset:61440\n"
    "v_mfma_f32_16x16x32_bf16 v[20:23], v[132:135], a[96:99], v[20:23]\n"
    "v_mfma_f32_16x16x32_bf16 v[116:119], v[132:135], a[108:111], v[116:119]\n"
    "buffer_load_dwordx4 v200, s[32:35], s98 offen lds\n"
    "v_mfma_f32_16x16x32_bf16 v[84:87], v[132:135], a[104:107], v[84:87]\n"
    "ds_read_b64_tr_b16 v[212:213], v225 offset:57344\n"
    "v_mfma_f32_16x16x32_bf16 v[52:55], v[132:135], a[100:103], v[52:55]\n"
    "s_waitcnt lgkmcnt(3)\n"
    "ds_read_b64_tr_b16 v[214:215], v225 offset:61440\n"
    "v_mfma_f32_16x16x32_bf16 v[56:59], v[136:139], a[100:103], v[56:59]\n"
    "v_mfma_f32_16x16x32_bf16 v[88:91], v[136:139], a[104:107], v[88:91]\n"
    "buffer_load_dwordx4 a[188:191], v199, s[44:47], s61 offen sc1\n"
    "v_mfma_f32_16x16x32_bf16 v[120:123], v[136:139], a[108:111], v[120:123]\n"
    "ds_read_b64_tr_b16 v[216:217], v226 offset:57344\n"
    "v_mfma_f32_16x16x32_bf16 v[24:27], v[136:139], a[96:99], v[24:27]\n"
    "ds_read_b64_tr_b16 v[218:219], v226 offset:61440\n"
    "v_mfma_f32_16x16x32_bf16 v[28:31], v[140:143], a[96:99], v[28:31]\n"
    "v_mfma_f32_16x16x32_bf16 v[124:127], v[140:143], a[108:111], v[124:127]\n"
    "v_mfma_f32_16x16x32_bf16 v[92:95], v[140:143], a[104:107], v[92:95]\n"
    "ds_read_b64_tr_b16 v[220:221], v227 offset:57344\n"
    "v_mfma_f32_16x16x32_bf16 v[60:63], v[140:143], a[100:103], v[60:63]\n"
    "s_waitcnt vmcnt(30) lgkmcnt(3)\n"
    "ds_read_b64_tr_b16 v[222:223], v227 offset:61440\n"
    "v_mfma_f32_16x16x32_bf16 v[32:35], v[208:211], a[116:119], v[32:35]\n"
    "buffer_load_dwordx4 a[192:195], v192, s[44:47], s62 offen sc1\n"
    "v_mfma_f32_16x16x32_bf16 v[64:67], v[208:211], a[120:123], v[64:67]\n"
    "v_mfma_f32_16x16x32_bf16 v[96:99], v[208:211], a[124:127], v[96:99]\n"
    "ds_read_b64_tr_b16 v[128:129], v204 offset:57344\n"
    "v_mfma_f32_16x16x32_bf16 v[0:3], v[208:211], a[112:115], v[0:3]\n"
    "ds_read_b64_tr_b16 v[130:131], v204 offset:61440\n"
    "buffer_load_dwordx4 v201, s[32:35], s98 offen offset:1024 lds\n"
    "v_mfma_f32_16x16x32_bf16 v[4:7], v[212:215], a[112:115], v[4:7]\n"
    "v_mfma_f32_16x16x32_bf16 v[100:103], v[212:215], a[124:127], v[100:103]\n"
    "v_mfma_f32_16x16x32_bf16 v[68:71], v[212:215], a[120:123], v[68:71]\n"
    "ds_read_b64_tr_b16 v[132:133], v241 offset:57344\n"
    "v_mfma_f32_16x16x32_bf16 v[36:39], v[212:215], a[116:119], v[36:39]\n"
    "s_waitcnt lgkmcnt(3)\n"
    "ds_read_b64_tr_b16 v[134:135], v241 offset:61440\n"
    "v_mfma_f32_16x16x32_bf16 v[40:43], v[216:219], a[116:119], v[40:43]\n"
    "buffer_load_dwordx4 a[196:199], v193, s[44:47], s62 offen sc1\n"
    "v_mfma_f32_16x16x32_bf16 v[72:75], v[216:219], a[120:123], v[72:75]\n"
    "v_mfma_f32_16x16x32_bf16 v[104:107], v[216:219], a[124:127], v[104:107]\n"
    "ds_read_b64_tr_b16 v[136:137], v252 offset:57344\n"
    "v_mfma_f32_16x16x32_bf16 v[8:11], v[216:219], a[112:115], v[8:11]\n"
    "ds_read_b64_tr_b16 v[138:139], v252 offset:61440\n"
    "v_mfma_f32_16x16x32_bf16 v[12:15], v[220:223], a[112:115], v[12:15]\n"
    "v_mfma_f32_16x16x32_bf16 v[108:111], v[220:223], a[124:127], v[108:111]\n"
    "v_mfma_f32_16x16x32_bf16 v[76:79], v[220:223], a[120:123], v[76:79]\n"
    "ds_read_b64_tr_b16 v[140:141], v253 offset:57344\n"
    "buffer_load_dwordx4 a[200:203], v194, s[44:47], s62 offen sc1\n"
    "v_mfma_f32_16x16x32_bf16 v[44:47], v[220:223], a[116:119], v[44:47]\n"
    "s_waitcnt lgkmcnt(3)\n"
    "s_waitcnt vmcnt(16)\n"
    "v_mfma_f32_16x16x32_bf16 v[48:51], v[128:131], a[116:119], v[48:51]\n"
    "s_barrier\n"
    "ds_read_b64_tr_b16 v[142:143], v253 offset:61440\n"
    "v_mfma_f32_16x16x32_bf16 v[80:83], v[128:131], a[120:123], v[80:83]\n"
    "v_mov_b32 v248, 0x181\n"
    "buffer_atomic_add v248, v247, s[44:47], 0 offen\n"
    "v_mfma_f32_16x16x32_bf16 v[112:115], v[128:131], a[124:127], v[112:115]\n"
    "ds_read_b64_tr_b16 v[208:209], v228\n"
    "v_mov_b32 v248, 0x101\n"
    "v_mfma_f32_16x16x32_bf16 v[16:19], v[128:131], a[112:115], v[16:19]\n"
    "ds_read_b64_tr_b16 v[210:211], v228 offset:4096\n"
    "buffer_load_dwordx4 v202, s[32:35], s98 offen offset:2048 lds\n"
    "v_mfma_f32_16x16x32_bf16 v[20:23], v[132:135], a[112:115], v[20:23]\n"
    "v_mfma_f32_16x16x32_bf16 v[116:119], v[132:135], a[124:127], v[116:119]\n"
    "buffer_load_dwordx4 a[204:207], v195, s[44:47], s62 offen sc1\n"
    "v_mfma_f32_16x16x32_bf16 v[84:87], v[132:135], a[120:123], v[84:87]\n"
    "ds_read_b64_tr_b16 v[212:213], v229\n"
    "v_mfma_f32_16x16x32_bf16 v[52:55], v[132:135], a[116:119], v[52:55]\n"
    "s_waitcnt lgkmcnt(3)\n"
    "ds_read_b64_tr_b16 v[214:215], v229 offset:4096\n"
    "v_mfma_f32_16x16x32_bf16 v[56:59], v[136:139], a[116:119], v[56:59]\n"
    "v_mfma_f32_16x16x32_bf16 v[88:91], v[136:139], a[120:123], v[88:91]\n"
    "v_mfma_f32_16x16x32_bf16 v[120:123], v[136:139], a[124:127], v[120:123]\n"
    "ds_read_b64_tr_b16 v[216:217], v230\n"
    "v_mfma_f32_16x16x32_bf16 v[24:27], v[136:139], a[112:115], v[24:27]\n"
    "ds_read_b64_tr_b16 v[218:219], v230 offset:4096\n"
    "v_mfma_f32_16x16x32_bf16 v[28:31], v[140:143], a[112:115], v[28:31]\n"
    "buffer_load_dwordx4 a[208:211], v196, s[44:47], s62 offen sc1\n"
    "v_mfma_f32_16x16x32_bf16 v[124:127], v[140:143], a[124:127], v[124:127]\n"
    "v_mfma_f32_16x16x32_bf16 v[92:95], v[140:143], a[120:123], v[92:95]\n"
    "ds_read_b64_tr_b16 v[220:221], v231\n"
    "v_mfma_f32_16x16x32_bf16 v[60:63], v[140:143], a[116:119], v[60:63]\n"
    "s_waitcnt lgkmcnt(3)\n"
    "ds_read_b64_tr_b16 v[222:223], v231 offset:4096\n"
    "v_mfma_f32_16x16x32_bf16 v[32:35], v[208:211], a[132:135], v[32:35]\n"
    "buffer_load_dwordx4 v203, s[32:35], s98 offen offset:3072 lds\n"
    "v_mfma_f32_16x16x32_bf16 v[64:67], v[208:211], a[136:139], v[64:67]\n"
    "s_add_u32 m0, s85, 0\n"
    "s_lshl_b32 s98, s64, 4\n"
    "v_mfma_f32_16x16x32_bf16 v[96:99], v[208:211], a[140:143], v[96:99]\n"
    "ds_read_b64_tr_b16 v[128:129], v232\n"
    "v_mfma_f32_16x16x32_bf16 v[0:3], v[208:211], a[128:131], v[0:3]\n"
    "ds_read_b64_tr_b16 v[130:131], v232 offset:4096\n"
    "buffer_load_dwordx4 a[212:215], v197, s[44:47], s62 offen sc1\n"
    "v_mfma_f32_16x16x32_bf16 v[4:7], v[212:215], a[128:131], v[4:7]\n"
    "v_mfma_f32_16x16x32_bf16 v[100:103], v[212:215], a[140:143], v[100:103]\n"
    "v_mfma_f32_16x16x32_bf16 v[68:71], v[212:215], a[136:139], v[68:71]\n"
    "ds_read_b64_tr_b16 v[132:133], v233\n"
    "v_mfma_f32_16x16x32_bf16 v[36:39], v[212:215], a[132:135], v[36:39]\n"
    "s_waitcnt lgkmcnt(3)\n"
    "ds_read_b64_tr_b16 v[134:135], v233 offset:4096\n"
    "v_mfma_f32_16x16x32_bf16 v[40:43], v[216:219], a[132:135], v[40:43]\n"
    "v_mfma_f32_16x16x32_bf16 v[72:75], v[216:219], a[136:139], v[72:75]\n"
    "v_mfma_f32_16x16x32_bf16 v[104:107], v[216:219], a[140:143], v[104:107]\n"
    "ds_read_b64_tr_b16 v[136:137], v234\n"
    "buffer_load_dwordx4 a[216:219], v198, s[44:47], s62 offen sc1\n"
    "v_mfma_f32_16x16x32_bf16 v[8:11], v[216:219], a[128:131], v[8:11]\n"
    "ds_read_b64_tr_b16 v[138:139], v234 offset:4096\n"
    "v_mfma_f32_16x16x32_bf16 v[12:15], v[220:223], a[128:131], v[12:15]\n"
    "v_mfma_f32_16x16x32_bf16 v[108:111], v[220:223], a[140:143], v[108:111]\n"
    "buffer_load_dwordx4 v200, s[32:35], s98 offen lds\n"
    "v_mfma_f32_16x16x32_bf16 v[76:79], v[220:223], a[136:139], v[76:79]\n"
    "ds_read_b64_tr_b16 v[140:141], v235\n"
    "v_mfma_f32_16x16x32_bf16 v[44:47], v[220:223], a[132:135], v[44:47]\n"
    "s_waitcnt lgkmcnt(3)\n"
    "ds_read_b64_tr_b16 v[142:143], v235 offset:4096\n"
    "v_mfma_f32_16x16x32_bf16 v[48:51], v[128:131], a[132:135], v[48:51]\n"
    "v_mfma_f32_16x16x32_bf16 v[80:83], v[128:131], a[136:139], v[80:83]\n"
    "buffer_load_dwordx4 a[220:223], v199, s[44:47], s62 offen sc1\n"
    "v_mfma_f32_16x16x32_bf16 v[112:115], v[128:131], a[140:143], v[112:115]\n"
    "ds_read_b64_tr_b16 v[208:209], v228 offset:8192\n"
    "v_mfma_f32_16x16x32_bf16 v[16:19], v[128:131], a[128:131], v[16:19]\n"
    "ds_read_b64_tr_b16 v[210:211], v228 offset:12288\n"
    "v_mfma_f32_16x16x32_bf16 v[20:23], v[132:135], a[128:131], v[20:23]\n"
    "v_mfma_f32_16x16x32_bf16 v[116:119], v[132:135], a[140:143], v[116:119]\n"
    "v_mfma_f32_16x16x32_bf16 v[84:87], v[132:135], a[136:139], v[84:87]\n"
    "ds_read_b64_tr_b16 v[212:213], v229 offset:8192\n"
    "v_mfma_f32_16x16x32_bf16 v[52:55], v[132:135], a[132:135], v[52:55]\n"
    "s_waitcnt lgkmcnt(3)\n"
    "ds_read_b64_tr_b16 v[214:215], v229 offset:12288\n"
    "v_mfma_f32_16x16x32_bf16 v[56:59], v[136:139], a[132:135], v[56:59]\n"
    "buffer_load_dwordx4 a[224:227], v192, s[44:47], s63 offen sc1\n"
    "v_mfma_f32_16x16x32_bf16 v[88:91], v[136:139], a[136:139], v[88:91]\n"
    "v_mfma_f32_16x16x32_bf16 v[120:123], v[136:139], a[140:143], v[120:123]\n"
    "ds_read_b64_tr_b16 v[216:217], v230 offset:8192\n"
    "v_mfma_f32_16x16x32_bf16 v[24:27], v[136:139], a[128:131], v[24:27]\n"
    "ds_read_b64_tr_b16 v[218:219], v230 offset:12288\n"
    "buffer_load_dwordx4 v201, s[32:35], s98 offen offset:1024 lds\n"
    "v_mfma_f32_16x16x32_bf16 v[28:31], v[140:143], a[128:131], v[28:31]\n"
    "v_mfma_f32_16x16x32_bf16 v[124:127], v[140:143], a[140:143], v[124:127]\n"
    "v_mfma_f32_16x16x32_bf16 v[92:95], v[140:143], a[136:139], v[92:95]\n"
    "ds_read_b64_tr_b16 v[220:221], v231 offset:8192\n"
    "v_mfma_f32_16x16x32_bf16 v[60:63], v[140:143], a[132:135], v[60:63]\n"
    "s_waitcnt lgkmcnt(3)\n"
    "ds_read_b64_tr_b16 v[222:223], v231 offset:12288\n"
    "v_mfma_f32_16x16x32_bf16 v[32:35], v[208:211], a[148:151], v[32:35]\n"
    "buffer_load_dwordx4 a[228:231], v193, s[44:47], s63 offen sc1\n"
    "v_mfma_f32_16x16x32_bf16 v[64:67], v[208:211], a[152:155], v[64:67]\n"
    "v_mfma_f32_16x16x32_bf16 v[96:99], v[208:211], a[156:159], v[96:99]\n"
    "ds_read_b64_tr_b16 v[128:129], v232 offset:8192\n"
    "v_mfma_f32_16x16x32_bf16 v[0:3], v[208:211], a[144:147], v[0:3]\n"
    "ds_read_b64_tr_b16 v[130:131], v232 offset:12288\n"
    "v_mfma_f32_16x16x32_bf16 v[4:7], v[212:215], a[144:147], v[4:7]\n"
    "v_mfma_f32_16x16x32_bf16 v[100:103], v[212:215], a[156:159], v[100:103]\n"
    "v_mfma_f32_16x16x32_bf16 v[68:71], v[212:215], a[152:155], v[68:71]\n"
    "ds_read_b64_tr_b16 v[132:133], v233 offset:8192\n"
    "buffer_load_dwordx4 a[232:235], v194, s[44:47], s63 offen sc1\n"
    "v_mfma_f32_16x16x32_bf16 v[36:39], v[212:215], a[148:151], v[36:39]\n"
    "s_waitcnt lgkmcnt(3)\n"
    "ds_read_b64_tr_b16 v[134:135], v233 offset:12288\n"
    "v_mfma_f32_16x16x32_bf16 v[40:43], v[216:219], a[148:151], v[40:43]\n"
    "v_mfma_f32_16x16x32_bf16 v[72:75], v[216:219], a[152:155], v[72:75]\n"
    "buffer_load_dwordx4 v202, s[32:35], s98 offen offset:2048 lds\n"
    "v_mfma_f32_16x16x32_bf16 v[104:107], v[216:219], a[156:159], v[104:107]\n"
    "ds_read_b64_tr_b16 v[136:137], v234 offset:8192\n"
    "v_mfma_f32_16x16x32_bf16 v[8:11], v[216:219], a[144:147], v[8:11]\n"
    "ds_read_b64_tr_b16 v[138:139], v234 offset:12288\n"
    "v_mfma_f32_16x16x32_bf16 v[12:15], v[220:223], a[144:147], v[12:15]\n"
    "v_mfma_f32_16x16x32_bf16 v[108:111], v[220:223], a[156:159], v[108:111]\n"
    "buffer_load_dwordx4 a[236:239], v195, s[44:47], s63 offen sc1\n"
    "v_mfma_f32_16x16x32_bf16 v[76:79], v[220:223], a[152:155], v[76:79]\n"
    "ds_read_b64_tr_b16 v[140:141], v235 offset:8192\n"
    "v_mfma_f32_16x16x32_bf16 v[44:47], v[220:223], a[148:151], v[44:47]\n"
    "s_waitcnt lgkmcnt(3)\n"
    "ds_read_b64_tr_b16 v[142:143], v235 offset:12288\n"
    "v_mfma_f32_16x16x32_bf16 v[48:51], v[128:131], a[148:151], v[48:51]\n"
    "v_mfma_f32_16x16x32_bf16 v[80:83], v[128:131], a[152:155], v[80:83]\n"
    "v_mfma_f32_16x16x32_bf16 v[112:115], v[128:131], a[156:159], v[112:115]\n"
    "ds_read_b64_tr_b16 v[208:209], v228 offset:16384\n"
    "v_mfma_f32_16x16x32_bf16 v[16:19], v[128:131], a[144:147], v[16:19]\n"
    "ds_read_b64_tr_b16 v[210:211], v228 offset:20480\n"
    "v_mfma_f32_16x16x32_bf16 v[20:23], v[132:135], a[144:147], v[20:23]\n"
    "buffer_load_dwordx4 a[240:243], v196, s[44:47], s63 offen sc1\n"
    "v_mfma_f32_16x16x32_bf16 v[116:119], v[132:135], a[156:159], v[116:119]\n"
    "v_mfma_f32_16x16x32_bf16 v[84:87], v[132:135], a[152:155], v[84:87]\n"
    "ds_read_b64_tr_b16 v[212:213], v229 offset:16384\n"
    "v_mfma_f32_16x16x32_bf16 v[52:55], v[132:135], a[148:151], v[52:55]\n"
    "s_waitcnt lgkmcnt(3)\n"
    "ds_read_b64_tr_b16 v[214:215], v229 offset:20480\n"
    "v_mfma_f32_16x16x32_bf16 v[56:59], v[136:139], a[148:151], v[56:59]\n"
    "buffer_load_dwordx4 v203, s[32:35], s98 offen offset:3072 lds\n"
    "v_mfma_f32_16x16x32_bf16 v[88:91], v[136:139], a[152:155], v[88:91]\n"
    "s_add_u32 m0, s85, 0x4000\n"
    "s_lshl_b32 s98, s65, 4\n"
    "v_mfma_f32_16x16x32_bf16 v[120:123], v[136:139], a[156:159], v[120:123]\n"
    "ds_read_b64_tr_b16 v[216:217], v230 offset:16384\n"
    "v_mfma_f32_16x16x32_bf16 v[24:27], v[136:139], a[144:147], v[24:27]\n"
    "ds_read_b64_tr_b16 v[218:219], v230 offset:20480\n"
    "buffer_load_dwordx4 a[244:247], v197, s[44:47], s63 offen sc1\n"
    "v_mfma_f32_16x16x32_bf16 v[28:31], v[140:143], a[144:147], v[28:31]\n"
    "v_mfma_f32_16x16x32_bf16 v[124:127], v[140:143], a[156:159], v[124:127]\n"
    "v_mfma_f32_16x16x32_bf16 v[92:95], v[140:143], a[152:155], v[92:95]\n"
    "ds_read_b64_tr_b16 v[220:221], v231 offset:16384\n"
    "v_mfma_f32_16x16x32_bf16 v[60:63], v[140:143], a[148:151], v[60:63]\n"
    "s_waitcnt vmcnt(29) lgkmcnt(3)\n"
    "ds_read_b64_tr_b16 v[222:223], v231 offset:20480\n"
    "v_mfma_f32_16x16x32_bf16 v[32:35], v[208:211], a[164:167], v[32:35]\n"
    "v_mfma_f32_16x16x32_bf16 v[64:67], v[208:211], a[168:171], v[64:67]\n"
    "v_mfma_f32_16x16x32_bf16 v[96:99], v[208:211], a[172:175], v[96:99]\n"
    "ds_read_b64_tr_b16 v[128:129], v232 offset:16384\n"
    "buffer_load_dwordx4 a[248:251], v198, s[44:47], s63 offen sc1\n"
    "v_mfma_f32_16x16x32_bf16 v[0:3], v[208:211], a[160:163], v[0:3]\n"
    "ds_read_b64_tr_b16 v[130:131], v232 offset:20480\n"
    "v_mfma_f32_16x16x32_bf16 v[4:7], v[212:215], a[160:163], v[4:7]\n"
    "v_mfma_f32_16x16x32_bf16 v[100:103], v[212:215], a[172:175], v[100:103]\n"
    "buffer_load_dwordx4 v200, s[32:35], s98 offen lds\n"
    "v_mfma_f32_16x16x32_bf16 v[68:71], v[212:215], a[168:171], v[68:71]\n"
    "ds_read_b64_tr_b16 v[132:133], v233 offset:16384\n"
    "v_mfma_f32_16x16x32_bf16 v[36:39], v[212:215], a[164:167], v[36:39]\n"
    "s_waitcnt lgkmcnt(3)\n"
    "ds_read_b64_tr_b16 v[134:135], v233 offset:20480\n"
    "v_mfma_f32_16x16x32_bf16 v[40:43], v[216:219], a[164:167], v[40:43]\n"
    "v_mfma_f32_16x16x32_bf16 v[72:75], v[216:219], a[168:171], v[72:75]\n"
    "buffer_load_dwordx4 a[252:255], v199, s[44:47], s63 offen sc1\n"
    "v_mfma_f32_16x16x32_bf16 v[104:107], v[216:219], a[172:175], v[104:107]\n"
    "ds_read_b64_tr_b16 v[136:137], v234 offset:16384\n"
    "v_mfma_f32_16x16x32_bf16 v[8:11], v[216:219], a[160:163], v[8:11]\n"
    "ds_read_b64_tr_b16 v[138:139], v234 offset:20480\n"
    "v_mfma_f32_16x16x32_bf16 v[12:15], v[220:223], a[160:163], v[12:15]\n"
    "v_mfma_f32_16x16x32_bf16 v[108:111], v[220:223], a[172:175], v[108:111]\n"
    "v_mfma_f32_16x16x32_bf16 v[76:79], v[220:223], a[168:171], v[76:79]\n"
    "ds_read_b64_tr_b16 v[140:141], v235 offset:16384\n"
    "v_mfma_f32_16x16x32_bf16 v[44:47], v[220:223], a[164:167], v[44:47]\n"
    "s_waitcnt lgkmcnt(3)\n"
    "ds_read_b64_tr_b16 v[142:143], v235 offset:20480\n"
    "v_mfma_f32_16x16x32_bf16 v[48:51], v[128:131], a[164:167], v[48:51]\n"
    "buffer_load_dwordx4 a[0:3], v192, s[44:47], s64 offen sc1\n"
    "v_mfma_f32_16x16x32_bf16 v[80:83], v[128:131], a[168:171], v[80:83]\n"
    "v_mfma_f32_16x16x32_bf16 v[112:115], v[128:131], a[172:175], v[112:115]\n"
    "ds_read_b64_tr_b16 v[208:209], v228 offset:24576\n"
    "v_mfma_f32_16x16x32_bf16 v[16:19], v[128:131], a[160:163], v[16:19]\n"
    "ds_read_b64_tr_b16 v[210:211], v228 offset:28672\n"
    "buffer_load_dwordx4 v201, s[32:35], s98 offen offset:1024 lds\n"
    "v_mfma_f32_16x16x32_bf16 v[20:23], v[132:135], a[160:163], v[20:23]\n"
    "v_mfma_f32_16x16x32_bf16 v[116:119], v[132:135], a[172:175], v[116:119]\n"
    "v_mfma_f32_16x16x32_bf16 v[84:87], v[132:135], a[168:171], v[84:87]\n"
    "ds_read_b64_tr_b16 v[212:213], v229 offset:24576\n"
    "v_mfma_f32_16x16x32_bf16 v[52:55], v[132:135], a[164:167], v[52:55]\n"
    "s_waitcnt lgkmcnt(3)\n"
    "ds_read_b64_tr_b16 v[214:215], v229 offset:28672\n"
    "v_mfma_f32_16x16x32_bf16 v[56:59], v[136:139], a[164:167], v[56:59]\n"
    "buffer_load_dwordx4 a[4:7], v193, s[44:47], s64 offen sc1\n"
    "v_mfma_f32_16x16x32_bf16 v[88:91], v[136:139], a[168:171], v[88:91]\n"
    "v_mfma_f32_16x16x32_bf16 v[120:123], v[136:139], a[172:175], v[120:123]\n"
    "ds_read_b64_tr_b16 v[216:217], v230 offset:24576\n"
    "v_mfma_f32_16x16x32_bf16 v[24:27], v[136:139], a[160:163], v[24:27]\n"
    "ds_read_b64_tr_b16 v[218:219], v230 offset:28672\n"
    "v_mfma_f32_16x16x32_bf16 v[28:31], v[140:143], a[160:163], v[28:31]\n"
    "v_mfma_f32_16x16x32_bf16 v[124:127], v[140:143], a[172:175], v[124:127]\n"
    "v_mfma_f32_16x16x32_bf16 v[92:95], v[140:143], a[168:171], v[92:95]\n"
    "ds_read_b64_tr_b16 v[220:221], v231 offset:24576\n"
    "buffer_load_dwordx4 a[8:11], v194, s[44:47], s64 offen sc1\n"
    "v_mfma_f32_16x16x32_bf16 v[60:63], v[140:143], a[164:167], v[60:63]\n"
    "s_waitcnt vmcnt(29) lgkmcnt(3)\n"
    "ds_read_b64_tr_b16 v[222:223], v231 offset:28672\n"
    "v_mfma_f32_16x16x32_bf16 v[32:35], v[208:211], a[180:183], v[32:35]\n"
    "v_mfma_f32_16x16x32_bf16 v[64:67], v[208:211], a[184:187], v[64:67]\n"
    "buffer_load_dwordx4 v202, s[32:35], s98 offen offset:2048 lds\n"
    "v_mfma_f32_16x16x32_bf16 v[96:99], v[208:211], a[188:191], v[96:99]\n"
    "ds_read_b64_tr_b16 v[128:129], v232 offset:24576\n"
    "v_mfma_f32_16x16x32_bf16 v[0:3], v[208:211], a[176:179], v[0:3]\n"
    "ds_read_b64_tr_b16 v[130:131], v232 offset:28672\n"
    "v_mfma_f32_16x16x32_bf16 v[4:7], v[212:215], a[176:179], v[4:7]\n"
    "v_mfma_f32_16x16x32_bf16 v[100:103], v[212:215], a[188:191], v[100:103]\n"
    "buffer_load_dwordx4 a[12:15], v195, s[44:47], s64 offen sc1\n"
    "v_mfma_f32_16x16x32_bf16 v[68:71], v[212:215], a[184:187], v[68:71]\n"
    "ds_read_b64_tr_b16 v[132:133], v233 offset:24576\n"
    "v_mfma_f32_16x16x32_bf16 v[36:39], v[212:215], a[180:183], v[36:39]\n"
    "s_waitcnt lgkmcnt(3)\n"
    "ds_read_b64_tr_b16 v[134:135], v233 offset:28672\n"
    "v_mfma_f32_16x16x32_bf16 v[40:43], v[216:219], a[180:183], v[40:43]\n"
    "v_mfma_f32_16x16x32_bf16 v[72:75], v[216:219], a[184:187], v[72:75]\n"
    "v_mfma_f32_16x16x32_bf16 v[104:107], v[216:219], a[188:191], v[104:107]\n"
    "ds_read_b64_tr_b16 v[136:137], v234 offset:24576\n"
    "v_mfma_f32_16x16x32_bf16 v[8:11], v[216:219], a[176:179], v[8:11]\n"
    "ds_read_b64_tr_b16 v[138:139], v234 offset:28672\n"
    "v_mfma_f32_16x16x32_bf16 v[12:15], v[220:223], a[176:179], v[12:15]\n"
    "buffer_load_dwordx4 a[16:19], v196, s[44:47], s64 offen sc1\n"
    "v_mfma_f32_16x16x32_bf16 v[108:111], v[220:223], a[188:191], v[108:111]\n"
    "v_mfma_f32_16x16x32_bf16 v[76:79], v[220:223], a[184:187], v[76:79]\n"
    "ds_read_b64_tr_b16 v[140:141], v235 offset:24576\n"
    "v_mfma_f32_16x16x32_bf16 v[44:47], v[220:223], a[180:183], v[44:47]\n"
    "s_waitcnt lgkmcnt(3)\n"
    "s_waitcnt vmcnt(23)\n"
    "v_mfma_f32_16x16x32_bf16 v[48:51], v[128:131], a[180:183], v[48:51]\n"
    "s_barrier\n"
    "ds_read_b64_tr_b16 v[142:143], v235 offset:28672\n"
    "v_mfma_f32_16x16x32_bf16 v[80:83], v[128:131], a[184:187], v[80:83]\n"
    "buffer_load_dwordx4 v203, s[32:35], s98 offen offset:3072 lds\n"
    "s_add_u32 m0, s85, 0x8000\n"
    "v_mfma_f32_16x16x32_bf16 v[112:115], v[128:131], a[188:191], v[112:115]\n"
    "ds_read_b64_tr_b16 v[208:209], v228 offset:32768\n"
    "s_lshl_b32 s98, s66, 4\n"
    "v_mfma_f32_16x16x32_bf16 v[16:19], v[128:131], a[176:179], v[16:19]\n"
    "ds_read_b64_tr_b16 v[210:211], v228 offset:36864\n"
    "buffer_load_dwordx4 a[20:23], v197, s[44:47], s64 offen sc1\n"
    "v_mfma_f32_16x16x32_bf16 v[20:23], v[132:135], a[176:179], v[20:23]\n"
    "v_mfma_f32_16x16x32_bf16 v[116:119], v[132:135], a[188:191], v[116:119]\n"
    "v_mfma_f32_16x16x32_bf16 v[84:87], v[132:135], a[184:187], v[84:87]\n"
    "ds_read_b64_tr_b16 v[212:213], v229 offset:32768\n"
    "v_mfma_f32_16x16x32_bf16 v[52:55], v[132:135], a[180:183], v[52:55]\n"
    "s_waitcnt lgkmcnt(3)\n"
    "ds_read_b64_tr_b16 v[214:215], v229 offset:36864\n"
    "v_mfma_f32_16x16x32_bf16 v[56:59], v[136:139], a[180:183], v[56:59]\n"
    "v_mfma_f32_16x16x32_bf16 v[88:91], v[136:139], a[184:187], v[88:91]\n"
    "v_mfma_f32_16x16x32_bf16 v[120:123], v[136:139], a[188:191], v[120:123]\n"
    "ds_read_b64_tr_b16 v[216:217], v230 offset:32768\n"
    "buffer_load_dwordx4 a[24:27], v198, s[44:47], s64 offen sc1\n"
    "v_mfma_f32_16x16x32_bf16 v[24:27], v[136:139], a[176:179], v[24:27]\n"
    "ds_read_b64_tr_b16 v[218:219], v230 offset:36864\n"
    "v_mfma_f32_16x16x32_bf16 v[28:31], v[140:143], a[176:179], v[28:31]\n"
    "v_mfma_f32_16x16x32_bf16 v[124:127], v[140:143], a[188:191], v[124:127]\n"
    "buffer_load_dwordx4 v200, s[32:35], s98 offen lds\n"
    "v_mfma_f32_16x16x32_bf16 v[92:95], v[140:143], a[184:187], v[92:95]\n"
    "ds_read_b64_tr_b16 v[220:221], v231 offset:32768\n"
    "v_mfma_f32_16x16x32_bf16 v[60:63], v[140:143], a[180:183], v[60:63]\n"
    "s_waitcnt lgkmcnt(3)\n"
    "ds_read_b64_tr_b16 v[222:223], v231 offset:36864\n"
    "v_mfma_f32_16x16x32_bf16 v[32:35], v[208:211], a[196:199], v[32:35]\n"
    "v_mfma_f32_16x16x32_bf16 v[64:67], v[208:211], a[200:203], v[64:67]\n"
    "buffer_load_dwordx4 a[28:31], v199, s[44:47], s64 offen sc1\n"
    "v_mfma_f32_16x16x32_bf16 v[96:99], v[208:211], a[204:207], v[96:99]\n"
    "ds_read_b64_tr_b16 v[128:129], v232 offset:32768\n"
    "v_mfma_f32_16x16x32_bf16 v[0:3], v[208:211], a[192:195], v[0:3]\n"
    "ds_read_b64_tr_b16 v[130:131], v232 offset:36864\n"
    "v_mfma_f32_16x16x32_bf16 v[4:7], v[212:215], a[192:195], v[4:7]\n"
    "v_mfma_f32_16x16x32_bf16 v[100:103], v[212:215], a[204:207], v[100:103]\n"
    "v_mfma_f32_16x16x32_bf16 v[68:71], v[212:215], a[200:203], v[68:71]\n"
    "ds_read_b64_tr_b16 v[132:133], v233 offset:32768\n"
    "v_mfma_f32_16x16x32_bf16 v[36:39], v[212:215], a[196:199], v[36:39]\n"
    "s_waitcnt lgkmcnt(3)\n"
    "ds_read_b64_tr_b16 v[134:135], v233 offset:36864\n"
    "v_mfma_f32_16x16x32_bf16 v[40:43], v[216:219], a[196:199], v[40:43]\n"
    "buffer_load_dwordx4 a[32:35], v192, s[44:47], s65 offen sc1\n"
    "v_mfma_f32_16x16x32_bf16 v[72:75], v[216:219], a[200:203], v[72:75]\n"
    "v_mfma_f32_16x16x32_bf16 v[104:107], v[216:219], a[204:207], v[104:107]\n"
    "ds_read_b64_tr_b16 v[136:137], v234 offset:32768\n"
    "v_mfma_f32_16x16x32_bf16 v[8:11], v[216:219], a[192:195], v[8:11]\n"
    "ds_read_b64_tr_b16 v[138:139], v234 offset:36864\n"
    "buffer_load_dwordx4 v201, s[32:35], s98 offen offset:1024 lds\n"
    "v_mfma_f32_16x16x32_bf16 v[12:15], v[220:223], a[192:195], v[12:15]\n"
    "v_mfma_f32_16x16x32_bf16 v[108:111], v[220:223], a[204:207], v[108:111]\n"
    "v_mfma_f32_16x16x32_bf16 v[76:79], v[220:223], a[200:203], v[76:79]\n"
    "ds_read_b64_tr_b16 v[140:141], v235 offset:32768\n"
    "v_mfma_f32_16x16x32_bf16 v[44:47], v[220:223], a[196:199], v[44:47]\n"
    "s_waitcnt lgkmcnt(3)\n"
    "ds_read_b64_tr_b16 v[142:143], v235 offset:36864\n"
    "v_mfma_f32_16x16x32_bf16 v[48:51], v[128:131], a[196:199], v[48:51]\n"
    "buffer_load_dwordx4 a[36:39], v193, s[44:47], s65 offen sc1\n"
    "v_mfma_f32_16x16x32_bf16 v[80:83], v[128:131], a[200:203], v[80:83]\n"
    "v_mfma_f32_16x16x32_bf16 v[112:115], v[128:131], a[204:207], v[112:115]\n"
    "ds_read_b64_tr_b16 v[208:209], v228 offset:40960\n"
    "v_mfma_f32_16x16x32_bf16 v[16:19], v[128:131], a[192:195], v[16:19]\n"
    "ds_read_b64_tr_b16 v[210:211], v228 offset:45056\n"
    "v_mfma_f32_16x16x32_bf16 v[20:23], v[132:135], a[192:195], v[20:23]\n"
    "v_mfma_f32_16x16x32_bf16 v[116:119], v[132:135], a[204:207], v[116:119]\n"
    "v_mfma_f32_16x16x32_bf16 v[84:87], v[132:135], a[200:203], v[84:87]\n"
    "ds_read_b64_tr_b16 v[212:213], v229 offset:40960\n"
    "buffer_load_dwordx4 a[40:43], v194, s[44:47], s65 offen sc1\n"
    "v_mfma_f32_16x16x32_bf16 v[52:55], v[132:135], a[196:199], v[52:55]\n"
    "s_waitcnt lgkmcnt(3)\n"
    "ds_read_b64_tr_b16 v[214:215], v229 offset:45056\n"
    "v_mfma_f32_16x16x32_bf16 v[56:59], v[136:139], a[196:199], v[56:59]\n"
    "v_mfma_f32_16x16x32_bf16 v[88:91], v[136:139], a[200:203], v[88:91]\n"
    "buffer_load_dwordx4 v202, s[32:35], s98 offen offset:2048 lds\n"
    "v_mfma_f32_16x16x32_bf16 v[120:123], v[136:139], a[204:207], v[120:123]\n"
    "ds_read_b64_tr_b16 v[216:217], v230 offset:40960\n"
    "v_mfma_f32_16x16x32_bf16 v[24:27], v[136:139], a[192:195], v[24:27]\n"
    "ds_read_b64_tr_b16 v[218:219], v230 offset:45056\n"
    "v_mfma_f32_16x16x32_bf16 v[28:31], v[140:143], a[192:195], v[28:31]\n"
    "v_mfma_f32_16x16x32_bf16 v[124:127], v[140:143], a[204:207], v[124:127]\n"
    "buffer_load_dwordx4 a[44:47], v195, s[44:47], s65 offen sc1\n"
    "v_mfma_f32_16x16x32_bf16 v[92:95], v[140:143], a[200:203], v[92:95]\n"
    "ds_read_b64_tr_b16 v[220:221], v231 offset:40960\n"
    "v_mfma_f32_16x16x32_bf16 v[60:63], v[140:143], a[196:199], v[60:63]\n"
    "s_waitcnt vmcnt(30) lgkmcnt(3)\n"
    "ds_read_b64_tr_b16 v[222:223], v231 offset:45056\n"
    "v_mfma_f32_16x16x32_bf16 v[32:35], v[208:211], a[212:215], v[32:35]\n"
    "buffer_load_dword v246, v245, s[44:47], 0 offen sc1\n"
    "v_mfma_f32_16x16x32_bf16 v[64:67], v[208:211], a[216:219], v[64:67]\n"
    "v_mfma_f32_16x16x32_bf16 v[96:99], v[208:211], a[220:223], v[96:99]\n"
    "ds_read_b64_tr_b16 v[128:129], v232 offset:40960\n"
    "v_mfma_f32_16x16x32_bf16 v[0:3], v[208:211], a[208:211], v[0:3]\n"
    "ds_read_b64_tr_b16 v[130:131], v232 offset:45056\n"
    "v_mfma_f32_16x16x32_bf16 v[4:7], v[212:215], a[208:211], v[4:7]\n"
    "buffer_load_dwordx4 a[48:51], v196, s[44:47], s65 offen sc1\n"
    "v_mfma_f32_16x16x32_bf16 v[100:103], v[212:215], a[220:223], v[100:103]\n"
    "v_mfma_f32_16x16x32_bf16 v[68:71], v[212:215], a[216:219], v[68:71]\n"
    "ds_read_b64_tr_b16 v[132:133], v233 offset:40960\n"
    "v_mfma_f32_16x16x32_bf16 v[36:39], v[212:215], a[212:215], v[36:39]\n"
    "s_waitcnt lgkmcnt(3)\n"
    "ds_read_b64_tr_b16 v[134:135], v233 offset:45056\n"
    "v_mfma_f32_16x16x32_bf16 v[40:43], v[216:219], a[212:215], v[40:43]\n"
    "buffer_load_dwordx4 v203, s[32:35], s98 offen offset:3072 lds\n"
    "v_mfma_f32_16x16x32_bf16 v[72:75], v[216:219], a[216:219], v[72:75]\n"
    "s_add_u32 m0, s85, 0xc000\n"
    "s_lshl_b32 s98, s67, 4\n"
    "v_mfma_f32_16x16x32_bf16 v[104:107], v[216:219], a[220:223], v[104:107]\n"
    "ds_read_b64_tr_b16 v[136:137], v234 offset:40960\n"
    "v_mfma_f32_16x16x32_bf16 v[8:11], v[216:219], a[208:211], v[8:11]\n"
    "ds_read_b64_tr_b16 v[138:139], v234 offset:45056\n"
    "buffer_load_dwordx4 a[52:55], v197, s[44:47], s65 offen sc1\n"
    "v_mfma_f32_16x16x32_bf16 v[12:15], v[220:223], a[208:211], v[12:15]\n"
    "v_mfma_f32_16x16x32_bf16 v[108:111], v[220:223], a[220:223], v[108:111]\n"
    "v_mfma_f32_16x16x32_bf16 v[76:79], v[220:223], a[216:219], v[76:79]\n"
    "ds_read_b64_tr_b16 v[140:141], v235 offset:40960\n"
    "v_mfma_f32_16x16x32_bf16 v[44:47], v[220:223], a[212:215], v[44:47]\n"
    "s_waitcnt lgkmcnt(3)\n"
    "ds_read_b64_tr_b16 v[142:143], v235 offset:45056\n"
    "v_mfma_f32_16x16x32_bf16 v[48:51], v[128:131], a[212:215], v[48:51]\n"
    "v_mfma_f32_16x16x32_bf16 v[80:83], v[128:131], a[216:219], v[80:83]\n"
    "v_mfma_f32_16x16x32_bf16 v[112:115], v[128:131], a[220:223], v[112:115]\n"
    "ds_read_b64_tr_b16 v[208:209], v228 offset:49152\n"
    "buffer_load_dwordx4 a[56:59], v198, s[44:47], s65 offen sc1\n"
    "v_mfma_f32_16x16x32_bf16 v[16:19], v[128:131], a[208:211], v[16:19]\n"
    "ds_read_b64_tr_b16 v[210:211], v228 offset:53248\n"
    "v_mfma_f32_16x16x32_bf16 v[20:23], v[132:135], a[208:211], v[20:23]\n"
    "v_mfma_f32_16x16x32_bf16 v[116:119], v[132:135], a[220:223], v[116:119]\n"
    "buffer_load_dwordx4 v200, s[32:35], s98 offen lds\n"
    "v_mfma_f32_16x16x32_bf16 v[84:87], v[132:135], a[216:219], v[84:87]\n"
    "ds_read_b64_tr_b16 v[212:213], v229 offset:49152\n"
    "v_mfma_f32_16x16x32_bf16 v[52:55], v[132:135], a[212:215], v[52:55]\n"
    "s_waitcnt lgkmcnt(3)\n"
    "ds_read_b64_tr_b16 v[214:215], v229 offset:53248\n"
    "v_mfma_f32_16x16x32_bf16 v[56:59], v[136:139], a[212:215], v[56:59]\n"
    "v_mfma_f32_16x16x32_bf16 v[88:91], v[136:139], a[216:219], v[88:91]\n"
    "buffer_load_dwordx4 a[60:63], v199, s[44:47], s65 offen sc1\n"
    "v_mfma_f32_16x16x32_bf16 v[120:123], v[136:139], a[220:223], v[120:123]\n"
    "ds_read_b64_tr_b16 v[216:217], v230 offset:49152\n"
    "v_mfma_f32_16x16x32_bf16 v[24:27], v[136:139], a[208:211], v[24:27]\n"
    "ds_read_b64_tr_b16 v[218:219], v230 offset:53248\n"
    "v_mfma_f32_16x16x32_bf16 v[28:31], v[140:143], a[208:211], v[28:31]\n"
    "v_mfma_f32_16x16x32_bf16 v[124:127], v[140:143], a[220:223], v[124:127]\n"
    "v_mfma_f32_16x16x32_bf16 v[92:95], v[140:143], a[216:219], v[92:95]\n"
    "ds_read_b64_tr_b16 v[220:221], v231 offset:49152\n"
    "v_mfma_f32_16x16x32_bf16 v[60:63], v[140:143], a[212:215], v[60:63]\n"
    "s_waitcnt vmcnt(31) lgkmcnt(3)\n"
    "ds_read_b64_tr_b16 v[222:223], v231 offset:53248\n"
    "v_mfma_f32_16x16x32_bf16 v[32:35], v[208:211], a[228:231], v[32:35]\n"
    "buffer_load_dwordx4 a[64:67], v192, s[44:47], s66 offen sc1\n"
    "v_mfma_f32_16x16x32_bf16 v[64:67], v[208:211], a[232:235], v[64:67]\n"
    "v_mfma_f32_16x16x32_bf16 v[96:99], v[208:211], a[236:239], v[96:99]\n"
    "ds_read_b64_tr_b16 v[128:129], v232 offset:49152\n"
    "v_mfma_f32_16x16x32_bf16 v[0:3], v[208:211], a[224:227], v[0:3]\n"
    "ds_read_b64_tr_b16 v[130:131], v232 offset:53248\n"
    "buffer_load_dwordx4 v201, s[32:35], s98 offen offset:1024 lds\n"
    "v_mfma_f32_16x16x32_bf16 v[4:7], v[212:215], a[224:227], v[4:7]\n"
    "v_mfma_f32_16x16x32_bf16 v[100:103], v[212:215], a[236:239], v[100:103]\n"
    "v_mfma_f32_16x16x32_bf16 v[68:71], v[212:215], a[232:235], v[68:71]\n"
    "ds_read_b64_tr_b16 v[132:133], v233 offset:49152\n"
    "v_mfma_f32_16x16x32_bf16 v[36:39], v[212:215], a[228:231], v[36:39]\n"
    "s_waitcnt lgkmcnt(3)\n"
    "ds_read_b64_tr_b16 v[134:135], v233 offset:53248\n"
    "v_mfma_f32_16x16x32_bf16 v[40:43], v[216:219], a[228:231], v[40:43]\n"
    "buffer_load_dwordx4 a[68:71], v193, s[44:47], s66 offen sc1\n"
    "v_mfma_f32_16x16x32_bf16 v[72:75], v[216:219], a[232:235], v[72:75]\n"
    "v_mfma_f32_16x16x32_bf16 v[104:107], v[216:219], a[236:239], v[104:107]\n"
    "ds_read_b64_tr_b16 v[136:137], v234 offset:49152\n"
    "v_mfma_f32_16x16x32_bf16 v[8:11], v[216:219], a[224:227], v[8:11]\n"
    "ds_read_b64_tr_b16 v[138:139], v234 offset:53248\n"
    "v_mfma_f32_16x16x32_bf16 v[12:15], v[220:223], a[224:227], v[12:15]\n"
    "v_mfma_f32_16x16x32_bf16 v[108:111], v[220:223], a[236:239], v[108:111]\n"
    "v_mfma_f32_16x16x32_bf16 v[76:79], v[220:223], a[232:235], v[76:79]\n"
    "ds_read_b64_tr_b16 v[140:141], v235 offset:49152\n"
    "buffer_load_dwordx4 a[72:75], v194, s[44:47], s66 offen sc1\n"
    "v_mfma_f32_16x16x32_bf16 v[44:47], v[220:223], a[228:231], v[44:47]\n"
    "s_waitcnt lgkmcnt(3)\n"
    "ds_read_b64_tr_b16 v[142:143], v235 offset:53248\n"
    "v_mfma_f32_16x16x32_bf16 v[48:51], v[128:131], a[228:231], v[48:51]\n"
    "v_mfma_f32_16x16x32_bf16 v[80:83], v[128:131], a[232:235], v[80:83]\n"
    "buffer_load_dwordx4 v202, s[32:35], s98 offen offset:2048 lds\n"
    "v_mfma_f32_16x16x32_bf16 v[112:115], v[128:131], a[236:239], v[112:115]\n"
    "ds_read_b64_tr_b16 v[208:209], v228 offset:57344\n"
    "v_mfma_f32_16x16x32_bf16 v[16:19], v[128:131], a[224:227], v[16:19]\n"
    "ds_read_b64_tr_b16 v[210:211], v228 offset:61440\n"
    "v_mfma_f32_16x16x32_bf16 v[20:23], v[132:135], a[224:227], v[20:23]\n"
    "v_mfma_f32_16x16x32_bf16 v[116:119], v[132:135], a[236:239], v[116:119]\n"
    "buffer_load_dwordx4 a[76:79], v195, s[44:47], s66 offen sc1\n"
    "v_mfma_f32_16x16x32_bf16 v[84:87], v[132:135], a[232:235], v[84:87]\n"
    "ds_read_b64_tr_b16 v[212:213], v229 offset:57344\n"
    "v_mfma_f32_16x16x32_bf16 v[52:55], v[132:135], a[228:231], v[52:55]\n"
    "s_waitcnt lgkmcnt(3)\n"
    "ds_read_b64_tr_b16 v[214:215], v229 offset:61440\n"
    "v_mfma_f32_16x16x32_bf16 v[56:59], v[136:139], a[228:231], v[56:59]\n"
    "v_mfma_f32_16x16x32_bf16 v[88:91], v[136:139], a[232:235], v[88:91]\n"
    "v_mfma_f32_16x16x32_bf16 v[120:123], v[136:139], a[236:239], v[120:123]\n"
    "ds_read_b64_tr_b16 v[216:217], v230 offset:57344\n"
    "v_mfma_f32_16x16x32_bf16 v[24:27], v[136:139], a[224:227], v[24:27]\n"
    "ds_read_b64_tr_b16 v[218:219], v230 offset:61440\n"
    "v_mfma_f32_16x16x32_bf16 v[28:31], v[140:143], a[224:227], v[28:31]\n"
    "buffer_load_dwordx4 a[80:83], v196, s[44:47], s66 offen sc1\n"
    "v_mfma_f32_16x16x32_bf16 v[124:127], v[140:143], a[236:239], v[124:127]\n"
    "v_mfma_f32_16x16x32_bf16 v[92:95], v[140:143], a[232:235], v[92:95]\n"
    "ds_read_b64_tr_b16 v[220:221], v231 offset:57344\n"
    "v_mfma_f32_16x16x32_bf16 v[60:63], v[140:143], a[228:231], v[60:63]\n"
    "s_waitcnt vmcnt(32) lgkmcnt(3)\n"
    "ds_read_b64_tr_b16 v[222:223], v231 offset:61440\n"
    "v_mfma_f32_16x16x32_bf16 v[32:35], v[208:211], a[244:247], v[32:35]\n"
    "buffer_load_dwordx4 v203, s[32:35], s98 offen offset:3072 lds\n"
    "v_mfma_f32_16x16x32_bf16 v[64:67], v[208:211], a[248:251], v[64:67]\n"
    "s_add_u32 m0, s85, 0x10000\n"
    "s_lshl_b32 s98, s68, 4\n"
    "v_mfma_f32_16x16x32_bf16 v[96:99], v[208:211], a[252:255], v[96:99]\n"
    "ds_read_b64_tr_b16 v[128:129], v232 offset:57344\n"
    "s_waitcnt vmcnt(14)\n"
    "v_mfma_f32_16x16x32_bf16 v[0:3], v[208:211], a[240:243], v[0:3]\n"
    "ds_read_b64_tr_b16 v[130:131], v232 offset:61440\n"
    "buffer_load_dwordx4 a[84:87], v197, s[44:47], s66 offen sc1\n"
    "v_mfma_f32_16x16x32_bf16 v[4:7], v[212:215], a[240:243], v[4:7]\n"
    "v_xor_b32 v249, v246, v244\n"
    "v_and_b32 v250, 63, v246\n"
    "v_mfma_f32_16x16x32_bf16 v[100:103], v[212:215], a[252:255], v[100:103]\n"
    "v_cmp_lt_u32 vcc, 0xff, v249\n"
    "v_cmp_le_u32 s[96:97], 5, v250\n"
    "v_mfma_f32_16x16x32_bf16 v[68:71], v[212:215], a[248:251], v[68:71]\n"
    "ds_read_b64_tr_b16 v[132:133], v233 offset:57344\n"
    "s_and_b32 s96, vcc_lo, s96\n"
    "v_mfma_f32_16x16x32_bf16 v[36:39], v[212:215], a[244:247], v[36:39]\n"
    "s_waitcnt lgkmcnt(3)\n"
    "ds_read_b64_tr_b16 v[134:135], v233 offset:61440\n"
    "v_mfma_f32_16x16x32_bf16 v[40:43], v[216:219], a[244:247], v[40:43]\n"
    "s_andn2_b32 s96, s84, s96\n"
    "s_cbranch_scc1 .Lslow_o\n"
    ".Lret_o:\n"
    "v_mfma_f32_16x16x32_bf16 v[72:75], v[216:219], a[248:251], v[72:75]\n"
    "v_mfma_f32_16x16x32_bf16 v[104:107], v[216:219], a[252:255], v[104:107]\n"
    "ds_read_b64_tr_b16 v[136:137], v234 offset:57344\n"
    "buffer_load_dwordx4 a[88:91], v198, s[44:47], s66 offen sc1\n"
    "v_mfma_f32_16x16x32_bf16 v[8:11], v[216:219], a[240:243], v[8:11]\n"
    "ds_read_b64_tr_b16 v[138:139], v234 offset:61440\n"
    "v_mfma_f32_16x16x32_bf16 v[12:15], v[220:223], a[240:243], v[12:15]\n"
    "v_mfma_f32_16x16x32_bf16 v[108:111], v[220:223], a[252:255], v[108:111]\n"
    "buffer_load_dwordx4 v200, s[32:35], s98 offen lds\n"
    "v_mfma_f32_16x16x32_bf16 v[76:79], v[220:223], a[248:251], v[76:79]\n"
    "ds_read_b64_tr_b16 v[140:141], v235 offset:57344\n"
    "v_mfma_f32_16x16x32_bf16 v[44:47], v[220:223], a[244:247], v[44:47]\n"
    "s_waitcnt lgkmcnt(3)\n"
    "v_mfma_f32_16x16x32_bf16 v[48:51], v[128:131], a[244:247], v[48:51]\n"
    "s_barrier\n"
    "ds_read_b64_tr_b16 v[142:143], v235 offset:61440\n"
    "v_mfma_f32_16x16x32_bf16 v[80:83], v[128:131], a[248:251], v[80:83]\n"
    "buffer_load_dwordx4 a[92:95], v199, s[44:47], s66 offen sc1\n"
    "v_mfma_f32_16x16x32_bf16 v[112:115], v[128:131], a[252:255], v[112:115]\n"
    "ds_read_b64_tr_b16 v[208:209], v224\n"
    "v_mfma_f32_16x16x32_bf16 v[16:19], v[128:131], a[240:243], v[16:19]\n"
    "ds_read_b64_tr_b16 v[210:211], v224 offset:4096\n"
    "v_mfma_f32_16x16x32_bf16 v[20:23], v[132:135], a[240:243], v[20:23]\n"
    "v_mfma_f32_16x16x32_bf16 v[116:119], v[132:135], a[252:255], v[116:119]\n"
    "buffer_load_dwordx4 v201, s[32:35], s98 offen offset:1024 lds\n"
    "v_mfma_f32_16x16x32_bf16 v[84:87], v[132:135], a[248:251], v[84:87]\n"
    "ds_read_b64_tr_b16 v[212:213], v225\n"
    "v_mfma_f32_16x16x32_bf16 v[52:55], v[132:135], a[244:247], v[52:55]\n"
    "s_waitcnt lgkmcnt(3)\n"
    "ds_read_b64_tr_b16 v[214:215], v225 offset:4096\n"
    "v_mfma_f32_16x16x32_bf16 v[56:59], v[136:139], a[244:247], v[56:59]\n"
    "buffer_load_dwordx4 a[96:99], v192, s[44:47], s67 offen sc1\n"
    "v_mfma_f32_16x16x32_bf16 v[88:91], v[136:139], a[248:251], v[88:91]\n"
    "v_mfma_f32_16x16x32_bf16 v[120:123], v[136:139], a[252:255], v[120:123]\n"
    "ds_read_b64_tr_b16 v[216:217], v226\n"
    "v_mfma_f32_16x16x32_bf16 v[24:27], v[136:139], a[240:243], v[24:27]\n"
    "ds_read_b64_tr_b16 v[218:219], v226 offset:4096\n"
    "v_mfma_f32_16x16x32_bf16 v[28:31], v[140:143], a[240:243], v[28:31]\n"
    "v_mfma_f32_16x16x32_bf16 v[124:127], v[140:143], a[252:255], v[124:127]\n"
    "buffer_load_dwordx4 v202, s[32:35], s98 offen offset:2048 lds\n"
    "v_mfma_f32_16x16x32_bf16 v[92:95], v[140:143], a[248:251], v[92:95]\n"
    "ds_read_b64_tr_b16 v[220:221], v227\n"
    "v_mfma_f32_16x16x32_bf16 v[60:63], v[140:143], a[244:247], v[60:63]\n"
    "s_waitcnt lgkmcnt(3)\n"
    "ds_read_b64_tr_b16 v[222:223], v227 offset:4096\n"
    "v_mfma_f32_16x16x32_bf16 v[32:35], v[208:211], a[4:7], v[32:35]\n"
    "buffer_load_dwordx4 a[100:103], v193, s[44:47], s67 offen sc1\n"
    "s_getpc_b64 s[92:93]\n"
    ".Lct7:\n"
    "v_mfma_f32_16x16x32_bf16 v[64:67], v[208:211], a[8:11], v[64:67]\n"
    "s_and_b32 s93, s93, 0xffff\n"
    "s_mov_b32 s94, .Lend-.Lct7\n"
    "v_mfma_f32_16x16x32_bf16 v[96:99], v[208:211], a[12:15], v[96:99]\n"
    "ds_read_b64_tr_b16 v[128:129], v204\n"
    "s_min_u32 s94, s94, 0x8000\n"
    "v_mfma_f32_16x16x32_bf16 v[0:3], v[208:211], a[0:3], v[0:3]\n"
    "ds_read_b64_tr_b16 v[130:131], v204 offset:4096\n"
    "s_mov_b32 s95, 0x20000\n"
    "v_mfma_f32_16x16x32_bf16 v[4:7], v[212:215], a[0:3], v[4:7]\n"
    "s_and_b32 s91, s13, 0x7f\n"
    "s_lshl_b32 s91, s91, 10\n"
    "v_mfma_f32_16x16x32_bf16 v[100:103], v[212:215], a[12:15], v[100:103]\n"
    "buffer_load_dwordx4 v203, s[32:35], s98 offen offset:3072 lds\n"
    "s_add_u32 m0, s85, 0x14000\n"
    "v_mfma_f32_16x16x32_bf16 v[68:71], v[212:215], a[8:11], v[68:71]\n"
    "ds_read_b64_tr_b16 v[132:133], v241\n"
    "buffer_load_dwordx4 a[104:107], v194, s[44:47], s67 offen sc1\n"
    "v_mfma_f32_16x16x32_bf16 v[36:39], v[212:215], a[4:7], v[36:39]\n"
    "s_waitcnt lgkmcnt(3)\n"
    "ds_read_b64_tr_b16 v[134:135], v241 offset:4096\n"
    "v_mfma_f32_16x16x32_bf16 v[40:43], v[216:219], a[4:7], v[40:43]\n"
    "s_lshl_b32 s98, s69, 4\n"
    "buffer_load_dword v206, v240, s[92:95], s91 offen\n"
    "v_mfma_f32_16x16x32_bf16 v[72:75], v[216:219], a[8:11], v[72:75]\n"
    "v_mfma_f32_16x16x32_bf16 v[104:107], v[216:219], a[12:15], v[104:107]\n"
    "ds_read_b64_tr_b16 v[136:137], v252\n"
    "v_mfma_f32_16x16x32_bf16 v[8:11], v[216:219], a[0:3], v[8:11]\n"
    "ds_read_b64_tr_b16 v[138:139], v252 offset:4096\n"
    "v_mfma_f32_16x16x32_bf16 v[12:15], v[220:223], a[0:3], v[12:15]\n"
    "v_mfma_f32_16x16x32_bf16 v[108:111], v[220:223], a[12:15], v[108:111]\n"
    "buffer_load_dwordx4 v200, s[32:35], s98 offen lds\n"
    "buffer_load_dwordx4 a[108:111], v195, s[44:47], s67 offen sc1\n"
    "v_mfma_f32_16x16x32_bf16 v[76:79], v[220:223], a[8:11], v[76:79]\n"
    "ds_read_b64_tr_b16 v[140:141], v253\n"
    "v_mfma_f32_16x16x32_bf16 v[44:47], v[220:223], a[4:7], v[44:47]\n"
    "s_waitcnt lgkmcnt(3)\n"
    "ds_read_b64_tr_b16 v[142:143], v253 offset:4096\n"
    "v_mfma_f32_16x16x32_bf16 v[48:51], v[128:131], a[4:7], v[48:51]\n"
    "v_mfma_f32_16x16x32_bf16 v[80:83], v[128:131], a[8:11], v[80:83]\n"
    "v_mfma_f32_16x16x32_bf16 v[112:115], v[128:131], a[12:15], v[112:115]\n"
    "ds_read_b64_tr_b16 v[208:209], v224 offset:8192\n"
    "v_mfma_f32_16x16x32_bf16 v[16:19], v[128:131], a[0:3], v[16:19]\n"
    "ds_read_b64_tr_b16 v[210:211], v224 offset:12288\n"
    "v_mfma_f32_16x16x32_bf16 v[20:23], v[132:135], a[0:3], v[20:23]\n"
    "buffer_load_dwordx4 a[112:115], v196, s[44:47], s67 offen sc1\n"
    "v_mfma_f32_16x16x32_bf16 v[116:119], v[132:135], a[12:15], v[116:119]\n"
    "buffer_load_dwordx4 v201, s[32:35], s98 offen offset:1024 lds\n"
    "v_mfma_f32_16x16x32_bf16 v[84:87], v[132:135], a[8:11], v[84:87]\n"
    "ds_read_b64_tr_b16 v[212:213], v225 offset:8192\n"
    "v_mfma_f32_16x16x32_bf16 v[52:55], v[132:135], a[4:7], v[52:55]\n"
    "s_waitcnt lgkmcnt(3)\n"
    "ds_read_b64_tr_b16 v[214:215], v225 offset:12288\n"
    "v_mfma_f32_16x16x32_bf16 v[56:59], v[136:139], a[4:7], v[56:59]\n"
    "v_mfma_f32_16x16x32_bf16 v[88:91], v[136:139], a[8:11], v[88:91]\n"
    "v_mfma_f32_16x16x32_bf16 v[120:123], v[136:139], a[12:15], v[120:123]\n"
    "ds_read_b64_tr_b16 v[216:217], v226 offset:8192\n"
    "v_mfma_f32_16x16x32_bf16 v[24:27], v[136:139], a[0:3], v[24:27]\n"
    "ds_read_b64_tr_b16 v[218:219], v226 offset:12288\n"
    "buffer_load_dwordx4 a[116:119], v197, s[44:47], s67 offen sc1\n"
    "v_mfma_f32_16x16x32_bf16 v[28:31], v[140:143], a[0:3], v[28:31]\n"
    "v_mfma_f32_16x16x32_bf16 v[124:127], v[140:143], a[12:15], v[124:127]\n"
    "buffer_load_dwordx4 v202, s[32:35], s98 offen offset:2048 lds\n"
    "v_mfma_f32_16x16x32_bf16 v[92:95], v[140:143], a[8:11], v[92:95]\n"
    "ds_read_b64_tr_b16 v[220:221], v227 offset:8192\n"
    "v_mfma_f32_16x16x32_bf16 v[60:63], v[140:143], a[4:7], v[60:63]\n"
    "s_waitcnt lgkmcnt(3)\n"
    "ds_read_b64_tr_b16 v[222:223], v227 offset:12288\n"
    "v_mfma_f32_16x16x32_bf16 v[32:35], v[208:211], a[20:23], v[32:35]\n"
    "v_mfma_f32_16x16x32_bf16 v[64:67], v[208:211], a[24:27], v[64:67]\n"
    "v_mfma_f32_16x16x32_bf16 v[96:99], v[208:211], a[28:31], v[96:99]\n"
    "ds_read_b64_tr_b16 v[128:129], v204 offset:8192\n"
    "buffer_load_dwordx4 a[120:123], v198, s[44:47], s67 offen sc1\n"
    "v_mfma_f32_16x16x32_bf16 v[0:3], v[208:211], a[16:19], v[0:3]\n"
    "ds_read_b64_tr_b16 v[130:131], v204 offset:12288\n"
    "v_mfma_f32_16x16x32_bf16 v[4:7], v[212:215], a[16:19], v[4:7]\n"
    "v_mfma_f32_16x16x32_bf16 v[100:103], v[212:215], a[28:31], v[100:103]\n"
    "buffer_load_dwordx4 v203, s[32:35], s98 offen offset:3072 lds\n"
    "s_mul_i32 s98, s51, 0x140000\n"
    "v_mfma_f32_16x16x32_bf16 v[68:71], v[212:215], a[24:27], v[68:71]\n"
    "ds_read_b64_tr_b16 v[132:133], v241 offset:8192\n"
    "v_subrev_u32 v200, s98, v200\n"
    "v_mfma_f32_16x16x32_bf16 v[36:39], v[212:215], a[20:23], v[36:39]\n"
    "s_waitcnt lgkmcnt(3)\n"
    "ds_read_b64_tr_b16 v[134:135], v241 offset:12288\n"
    "v_mfma_f32_16x16x32_bf16 v[40:43], v[216:219], a[20:23], v[40:43]\n"
    "v_subrev_u32 v201, s98, v201\n"
    "v_subrev_u32 v202, s98, v202\n"
    "v_mfma_f32_16x16x32_bf16 v[72:75], v[216:219], a[24:27], v[72:75]\n"
    "v_subrev_u32 v203, s98, v203\n"
    "s_add_u32 m0, s85, 0x18000\n"
    "v_mfma_f32_16x16x32_bf16 v[104:107], v[216:219], a[28:31], v[104:107]\n"
    "ds_read_b64_tr_b16 v[136:137], v252 offset:8192\n"
    "s_lshl_b32 s98, s76, 4\n"
    "v_mfma_f32_16x16x32_bf16 v[8:11], v[216:219], a[16:19], v[8:11]\n"
    "ds_read_b64_tr_b16 v[138:139], v252 offset:12288\n"
    "s_sub_u32 s98, s98, 0x1c0000\n"
    "v_mfma_f32_16x16x32_bf16 v[12:15], v[220:223], a[16:19], v[12:15]\n"
    "buffer_load_dwordx4 a[124:127], v199, s[44:47], s67 offen sc1\n"
    "v_mfma_f32_16x16x32_bf16 v[108:111], v[220:223], a[28:31], v[108:111]\n"
    "buffer_load_dwordx4 v200, s[36:39], s98 offen lds\n"
    "v_mfma_f32_16x16x32_bf16 v[76:79], v[220:223], a[24:27], v[76:79]\n"
    "ds_read_b64_tr_b16 v[140:141], v253 offset:8192\n"
    "v_mfma_f32_16x16x32_bf16 v[44:47], v[220:223], a[20:23], v[44:47]\n"
    "s_waitcnt lgkmcnt(3)\n"
    "ds_read_b64_tr_b16 v[142:143], v253 offset:12288\n"
    "v_mfma_f32_16x16x32_bf16 v[48:51], v[128:131], a[20:23], v[48:51]\n"
    "buffer_load_dwordx4 a[132:135], v193, s[44:47], s68 offen sc1\n"
    "v_mfma_f32_16x16x32_bf16 v[80:83], v[128:131], a[24:27], v[80:83]\n"
    "v_mfma_f32_16x16x32_bf16 v[112:115], v[128:131], a[28:31], v[112:115]\n"
    "ds_read_b64_tr_b16 v[208:209], v224 offset:16384\n"
    "v_mfma_f32_16x16x32_bf16 v[16:19], v[128:131], a[16:19], v[16:19]\n"
    "ds_read_b64_tr_b16 v[210:211], v224 offset:20480\n"
    "v_mfma_f32_16x16x32_bf16 v[20:23], v[132:135], a[16:19], v[20:23]\n"
    "v_mfma_f32_16x16x32_bf16 v[116:119], v[132:135], a[28:31], v[116:119]\n"
    "v_mfma_f32_16x16x32_bf16 v[84:87], v[132:135], a[24:27], v[84:87]\n"
    "ds_read_b64_tr_b16 v[212:213], v225 offset:16384\n"
    "v_mfma_f32_16x16x32_bf16 v[52:55], v[132:135], a[20:23], v[52:55]\n"
    "s_waitcnt lgkmcnt(3)\n"
    "ds_read_b64_tr_b16 v[214:215], v225 offset:20480\n"
    "v_mfma_f32_16x16x32_bf16 v[56:59], v[136:139], a[20:23], v[56:59]\n"
    "buffer_load_dwordx4 a[136:139], v194, s[44:47], s68 offen sc1\n"
    "v_mfma_f32_16x16x32_bf16 v[88:91], v[136:139], a[24:27], v[88:91]\n"
    "v_mfma_f32_16x16x32_bf16 v[120:123], v[136:139], a[28:31], v[120:123]\n"
    "ds_read_b64_tr_b16 v[216:217], v226 offset:16384\n"
    "v_mfma_f32_16x16x32_bf16 v[24:27], v[136:139], a[16:19], v[24:27]\n"
    "ds_read_b64_tr_b16 v[218:219], v226 offset:20480\n"
    "buffer_load_dwordx4 v201, s[36:39], s98 offen offset:1024 lds\n"
    "v_mfma_f32_16x16x32_bf16 v[28:31], v[140:143], a[16:19], v[28:31]\n"
    "v_mfma_f32_16x16x32_bf16 v[124:127], v[140:143], a[28:31], v[124:127]\n"
    "v_mfma_f32_16x16x32_bf16 v[92:95], v[140:143], a[24:27], v[92:95]\n"
    "ds_read_b64_tr_b16 v[220:221], v227 offset:16384\n"
    "buffer_load_dwordx4 a[148:151], v197, s[44:47], s68 offen sc1\n"
    "v_mfma_f32_16x16x32_bf16 v[60:63], v[140:143], a[20:23], v[60:63]\n"
    "s_waitcnt lgkmcnt(3)\n"
    "ds_read_b64_tr_b16 v[222:223], v227 offset:20480\n"
    "v_mfma_f32_16x16x32_bf16 v[32:35], v[208:211], a[36:39], v[32:35]\n"
    "v_mov_b32 v236, 0x3f80\n"
    "v_mov_b32 v237, 0x3f800000\n"
    "v_mfma_f32_16x16x32_bf16 v[64:67], v[208:211], a[40:43], v[64:67]\n"
    "v_mbcnt_lo_u32_b32 v251, -1, 0\n"
    "v_mbcnt_hi_u32_b32 v251, -1, v251\n"
    "v_mfma_f32_16x16x32_bf16 v[96:99], v[208:211], a[44:47], v[96:99]\n"
    "ds_read_b64_tr_b16 v[128:129], v204 offset:16384\n"
    "v_and_b32 v249, 15, v251\n"
    "v_mfma_f32_16x16x32_bf16 v[0:3], v[208:211], a[32:35], v[0:3]\n"
    "ds_read_b64_tr_b16 v[130:131], v204 offset:20480\n"
    "v_lshlrev_b32 v249, 11, v249\n"
    "v_mfma_f32_16x16x32_bf16 v[4:7], v[212:215], a[32:35], v[4:7]\n"
    "v_bfe_u32 v250, v251, 4, 1\n"
    "v_lshl_add_u32 v249, v250, 5, v249\n"
    "v_mfma_f32_16x16x32_bf16 v[100:103], v[212:215], a[44:47], v[100:103]\n"
    "buffer_load_dwordx4 a[152:155], v198, s[44:47], s68 offen sc1\n"
    "v_lshrrev_b32 v250, 5, v251\n"
    "v_mfma_f32_16x16x32_bf16 v[68:71], v[212:215], a[40:43], v[68:71]\n"
    "ds_read_b64_tr_b16 v[132:133], v241 offset:16384\n"
    "v_lshl_add_u32 v242, v250, 4, v249\n"
    "v_mfma_f32_16x16x32_bf16 v[36:39], v[212:215], a[36:39], v[36:39]\n"
    "s_waitcnt lgkmcnt(3)\n"
    "ds_read_b64_tr_b16 v[134:135], v241 offset:20480\n"
    "v_mfma_f32_16x16x32_bf16 v[40:43], v[216:219], a[36:39], v[40:43]\n"
    "s_lshl_b32 s89, s52, 2\n"
    "s_add_u32 s89, s89, s50\n"
    "v_mfma_f32_16x16x32_bf16 v[72:75], v[216:219], a[40:43], v[72:75]\n"
    "buffer_load_dwordx4 v202, s[36:39], s98 offen offset:2048 lds\n"
    "s_lshl_b32 s89, s89, 2\n"
    "v_mfma_f32_16x16x32_bf16 v[104:107], v[216:219], a[44:47], v[104:107]\n"
    "ds_read_b64_tr_b16 v[136:137], v252 offset:16384\n"
    "s_add_u32 s89, s89, s51\n"
    "v_mfma_f32_16x16x32_bf16 v[8:11], v[216:219], a[32:35], v[8:11]\n"
    "ds_read_b64_tr_b16 v[138:139], v252 offset:20480\n"
    "s_lshl_b32 s89, s89, 15\n"
    "v_mfma_f32_16x16x32_bf16 v[12:15], v[220:223], a[32:35], v[12:15]\n"
    "buffer_load_dwordx4 a[164:167], v193, s[44:47], s69 offen sc1\n"
    "s_lshl_b32 s90, s53, 8\n"
    "v_mfma_f32_16x16x32_bf16 v[108:111], v[220:223], a[44:47], v[108:111]\n"
    "s_add_u32 s16, s89, s90\n"
    "v_mfma_f32_16x16x32_bf16 v[76:79], v[220:223], a[40:43], v[76:79]\n"
    "ds_read_b64_tr_b16 v[140:141], v253 offset:16384\n"
    "v_mfma_f32_16x16x32_bf16 v[44:47], v[220:223], a[36:39], v[44:47]\n"
    "s_waitcnt lgkmcnt(3)\n"
    "ds_read_b64_tr_b16 v[142:143], v253 offset:20480\n"
    "v_mfma_f32_16x16x32_bf16 v[48:51], v[128:131], a[36:39], v[48:51]\n"
    "v_mfma_f32_16x16x32_bf16 v[80:83], v[128:131], a[40:43], v[80:83]\n"
    "v_mfma_f32_16x16x32_bf16 v[112:115], v[128:131], a[44:47], v[112:115]\n"
    "ds_read_b64_tr_b16 v[208:209], v224 offset:24576\n"
    "v_mfma_f32_16x16x32_bf16 v[16:19], v[128:131], a[32:35], v[16:19]\n"
    "ds_read_b64_tr_b16 v[210:211], v224 offset:28672\n"
    "buffer_load_dwordx4 a[168:171], v194, s[44:47], s69 offen sc1\n"
    "v_mfma_f32_16x16x32_bf16 v[20:23], v[132:135], a[32:35], v[20:23]\n"
    "v_mfma_f32_16x16x32_bf16 v[116:119], v[132:135], a[44:47], v[116:119]\n"
    "v_mfma_f32_16x16x32_bf16 v[84:87], v[132:135], a[40:43], v[84:87]\n"
    "ds_read_b64_tr_b16 v[212:213], v225 offset:24576\n"
    "v_mfma_f32_16x16x32_bf16 v[52:55], v[132:135], a[36:39], v[52:55]\n"
    "s_waitcnt lgkmcnt(3)\n"
    "ds_read_b64_tr_b16 v[214:215], v225 offset:28672\n"
    "v_mfma_f32_16x16x32_bf16 v[56:59], v[136:139], a[36:39], v[56:59]\n"
    "buffer_load_dwordx4 v203, s[36:39], s98 offen offset:3072 lds\n"
    "v_mfma_f32_16x16x32_bf16 v[88:91], v[136:139], a[40:43], v[88:91]\n"
    "v_mfma_f32_16x16x32_bf16 v[120:123], v[136:139], a[44:47], v[120:123]\n"
    "ds_read_b64_tr_b16 v[216:217], v226 offset:24576\n"
    "buffer_load_dwordx4 a[180:183], v197, s[44:47], s69 offen sc1\n"
    "v_mfma_f32_16x16x32_bf16 v[24:27], v[136:139], a[32:35], v[24:27]\n"
    "ds_read_b64_tr_b16 v[218:219], v226 offset:28672\n"
    "v_mfma_f32_16x16x32_bf16 v[28:31], v[140:143], a[32:35], v[28:31]\n"
    "v_mfma_f32_16x16x32_bf16 v[124:127], v[140:143], a[44:47], v[124:127]\n"
    "v_mfma_f32_16x16x32_bf16 v[92:95], v[140:143], a[40:43], v[92:95]\n"
    "ds_read_b64_tr_b16 v[220:221], v227 offset:24576\n"
    "v_mfma_f32_16x16x32_bf16 v[60:63], v[140:143], a[36:39], v[60:63]\n"
    "s_waitcnt vmcnt(39) lgkmcnt(3)\n"
    "ds_read_b64_tr_b16 v[222:223], v227 offset:28672\n"
    "v_mfma_f32_16x16x32_bf16 v[32:35], v[208:211], a[52:55], v[32:35]\n"
    "v_mfma_f32_16x16x32_bf16 v[64:67], v[208:211], a[56:59], v[64:67]\n"
    "buffer_load_dwordx4 a[184:187], v198, s[44:47], s69 offen sc1\n"
    "v_mfma_f32_16x16x32_bf16 v[96:99], v[208:211], a[60:63], v[96:99]\n"
    "ds_read_b64_tr_b16 v[128:129], v204 offset:24576\n"
    "v_mfma_f32_16x16x32_bf16 v[0:3], v[208:211], a[48:51], v[0:3]\n"
    "ds_read_b64_tr_b16 v[130:131], v204 offset:28672\n"
    "v_mfma_f32_16x16x32_bf16 v[4:7], v[212:215], a[48:51], v[4:7]\n"
    "v_mfma_f32_16x16x32_bf16 v[100:103], v[212:215], a[60:63], v[100:103]\n"
    "v_mfma_f32_16x16x32_bf16 v[68:71], v[212:215], a[56:59], v[68:71]\n"
    "ds_read_b64_tr_b16 v[132:133], v241 offset:24576\n"
    "v_mfma_f32_16x16x32_bf16 v[36:39], v[212:215], a[52:55], v[36:39]\n"
    "s_waitcnt lgkmcnt(3)\n"
    "ds_read_b64_tr_b16 v[134:135], v241 offset:28672\n"
    "v_mfma_f32_16x16x32_bf16 v[40:43], v[216:219], a[52:55], v[40:43]\n"
    "buffer_load_dwordx4 a[196:199], v193, s[44:47], s76 offen sc1\n"
    "v_mfma_f32_16x16x32_bf16 v[72:75], v[216:219], a[56:59], v[72:75]\n"
    "s_add_u32 m0, s85, 0x1c000\n"
    "s_lshl_b32 s98, s77, 4\n"
    "v_mfma_f32_16x16x32_bf16 v[104:107], v[216:219], a[60:63], v[104:107]\n"
    "ds_read_b64_tr_b16 v[136:137], v252 offset:24576\n"
    "s_sub_u32 s98, s98, 0x1c0000\n"
    "v_mfma_f32_16x16x32_bf16 v[8:11], v[216:219], a[48:51], v[8:11]\n"
    "ds_read_b64_tr_b16 v[138:139], v252 offset:28672\n"
    "v_mfma_f32_16x16x32_bf16 v[12:15], v[220:223], a[48:51], v[12:15]\n"
    "v_mfma_f32_16x16x32_bf16 v[108:111], v[220:223], a[60:63], v[108:111]\n"
    "v_mfma_f32_16x16x32_bf16 v[76:79], v[220:223], a[56:59], v[76:79]\n"
    "ds_read_b64_tr_b16 v[140:141], v253 offset:24576\n"
    "v_mfma_f32_16x16x32_bf16 v[44:47], v[220:223], a[52:55], v[44:47]\n"
    "s_waitcnt lgkmcnt(3)\n"
    "s_waitcnt vmcnt(33)\n"
    "v_mfma_f32_16x16x32_bf16 v[48:51], v[128:131], a[52:55], v[48:51]\n"
    "s_barrier\n"
    "ds_read_b64_tr_b16 v[142:143], v253 offset:28672\n"
    "v_mfma_f32_16x16x32_bf16 v[80:83], v[128:131], a[56:59], v[80:83]\n"
    "buffer_load_dwordx4 a[200:203], v194, s[44:47], s76 offen sc1\n"
    "v_mfma_f32_16x16x32_bf16 v[112:115], v[128:131], a[60:63], v[112:115]\n"
    "ds_read_b64_tr_b16 v[208:209], v224 offset:32768\n"
    "v_mfma_f32_16x16x32_bf16 v[16:19], v[128:131], a[48:51], v[16:19]\n"
    "ds_read_b64_tr_b16 v[210:211], v224 offset:36864\n"
    "v_mfma_f32_16x16x32_bf16 v[20:23], v[132:135], a[48:51], v[20:23]\n"
    "v_mfma_f32_16x16x32_bf16 v[116:119], v[132:135], a[60:63], v[116:119]\n"
    "buffer_load_dwordx4 v200, s[36:39], s98 offen lds\n"
    "v_mfma_f32_16x16x32_bf16 v[84:87], v[132:135], a[56:59], v[84:87]\n"
    "ds_read_b64_tr_b16 v[212:213], v225 offset:32768\n"
    "buffer_load_dwordx4 a[212:215], v197, s[44:47], s76 offen sc1\n"
    "v_mfma_f32_16x16x32_bf16 v[52:55], v[132:135], a[52:55], v[52:55]\n"
    "s_waitcnt lgkmcnt(3)\n"
    "ds_read_b64_tr_b16 v[214:215], v225 offset:36864\n"
    "v_mfma_f32_16x16x32_bf16 v[56:59], v[136:139], a[52:55], v[56:59]\n"
    "v_mfma_f32_16x16x32_bf16 v[88:91], v[136:139], a[56:59], v[88:91]\n"
    "v_mfma_f32_16x16x32_bf16 v[120:123], v[136:139], a[60:63], v[120:123]\n"
    "ds_read_b64_tr_b16 v[216:217], v226 offset:32768\n"
    "v_mfma_f32_16x16x32_bf16 v[24:27], v[136:139], a[48:51], v[24:27]\n"
    "ds_read_b64_tr_b16 v[218:219], v226 offset:36864\n"
    "v_mfma_f32_16x16x32_bf16 v[28:31], v[140:143], a[48:51], v[28:31]\n"
    "v_mfma_f32_16x16x32_bf16 v[124:127], v[140:143], a[60:63], v[124:127]\n"
    "buffer_load_dwordx4 v201, s[36:39], s98 offen offset:1024 lds\n"
    "buffer_load_dwordx4 a[216:219], v198, s[44:47], s76 offen sc1\n"
    "v_mfma_f32_16x16x32_bf16 v[92:95], v[140:143], a[56:59], v[92:95]\n"
    "ds_read_b64_tr_b16 v[220:221], v227 offset:32768\n"
    "v_mfma_f32_16x16x32_bf16 v[60:63], v[140:143], a[52:55], v[60:63]\n"
    "s_waitcnt lgkmcnt(3)\n"
    "ds_read_b64_tr_b16 v[222:223], v227 offset:36864\n"
    "v_mfma_f32_16x16x32_bf16 v[32:35], v[208:211], a[68:71], v[32:35]\n"
    "v_mfma_f32_16x16x32_bf16 v[64:67], v[208:211], a[72:75], v[64:67]\n"
    "v_mfma_f32_16x16x32_bf16 v[96:99], v[208:211], a[76:79], v[96:99]\n"
    "ds_read_b64_tr_b16 v[128:129], v204 offset:32768\n"
    "v_mfma_f32_16x16x32_bf16 v[0:3], v[208:211], a[64:67], v[0:3]\n"
    "ds_read_b64_tr_b16 v[130:131], v204 offset:36864\n"
    "v_mfma_f32_16x16x32_bf16 v[4:7], v[212:215], a[64:67], v[4:7]\n"
    "buffer_load_dwordx4 a[228:231], v193, s[44:47], s77 offen sc1\n"
    "v_mfma_f32_16x16x32_bf16 v[100:103], v[212:215], a[76:79], v[100:103]\n"
    "buffer_load_dwordx4 v202, s[36:39], s98 offen offset:2048 lds\n"
    "v_mfma_f32_16x16x32_bf16 v[68:71], v[212:215], a[72:75], v[68:71]\n"
    "ds_read_b64_tr_b16 v[132:133], v241 offset:32768\n"
    "v_mfma_f32_16x16x32_bf16 v[36:39], v[212:215], a[68:71], v[36:39]\n"
    "s_waitcnt lgkmcnt(3)\n"
    "ds_read_b64_tr_b16 v[134:135], v241 offset:36864\n"
    "v_mfma_f32_16x16x32_bf16 v[40:43], v[216:219], a[68:71], v[40:43]\n"
    "buffer_load_dwordx4 v[176:179], v242, s[20:23], s16 offen\n"
    "v_mfma_f32_16x16x32_bf16 v[72:75], v[216:219], a[72:75], v[72:75]\n"
    "v_mfma_f32_16x16x32_bf16 v[104:107], v[216:219], a[76:79], v[104:107]\n"
    "ds_read_b64_tr_b16 v[136:137], v252 offset:32768\n"
    "v_mfma_f32_16x16x32_bf16 v[8:11], v[216:219], a[64:67], v[8:11]\n"
    "ds_read_b64_tr_b16 v[138:139], v252 offset:36864\n"
    "v_mfma_f32_16x16x32_bf16 v[12:15], v[220:223], a[64:67], v[12:15]\n"
    "buffer_load_dwordx4 a[232:235], v194, s[44:47], s77 offen sc1\n"
    "v_mfma_f32_16x16x32_bf16 v[108:111], v[220:223], a[76:79], v[108:111]\n"
    "buffer_load_dwordx4 v203, s[36:39], s98 offen offset:3072 lds\n"
    "s_add_u32 m0, s85, 0\n"
    "v_mfma_f32_16x16x32_bf16 v[76:79], v[220:223], a[72:75], v[76:79]\n"
    "ds_read_b64_tr_b16 v[140:141], v253 offset:32768\n"
    "s_lshl_b32 s98, s78, 4\n"
    "v_mfma_f32_16x16x32_bf16 v[44:47], v[220:223], a[68:71], v[44:47]\n"
    "s_waitcnt lgkmcnt(3)\n"
    "ds_read_b64_tr_b16 v[142:143], v253 offset:36864\n"
    "v_mfma_f32_16x16x32_bf16 v[48:51], v[128:131], a[68:71], v[48:51]\n"
    "s_sub_u32 s98, s98, 0x1c0000\n"
    "v_mfma_f32_16x16x32_bf16 v[80:83], v[128:131], a[72:75], v[80:83]\n"
    "v_mfma_f32_16x16x32_bf16 v[112:115], v[128:131], a[76:79], v[112:115]\n"
    "ds_read_b64_tr_b16 v[208:209], v224 offset:40960\n"
    "v_mfma_f32_16x16x32_bf16 v[16:19], v[128:131], a[64:67], v[16:19]\n"
    "ds_read_b64_tr_b16 v[210:211], v224 offset:45056\n"
    "v_mfma_f32_16x16x32_bf16 v[20:23], v[132:135], a[64:67], v[20:23]\n"
    "buffer_load_dwordx4 a[244:247], v197, s[44:47], s77 offen sc1\n"
    "buffer_load_dwordx4 v[180:183], v242, s[20:23], s16 offen offset:64\n"
    "v_mfma_f32_16x16x32_bf16 v[116:119], v[132:135], a[76:79], v[116:119]\n"
    "buffer_load_dwordx4 v200, s[36:39], s98 offen lds\n"
    "v_mfma_f32_16x16x32_bf16 v[84:87], v[132:135], a[72:75], v[84:87]\n"
    "ds_read_b64_tr_b16 v[212:213], v225 offset:40960\n"
    "v_mfma_f32_16x16x32_bf16 v[52:55], v[132:135], a[68:71], v[52:55]\n"
    "s_waitcnt lgkmcnt(3)\n"
    "ds_read_b64_tr_b16 v[214:215], v225 offset:45056\n"
    "v_mfma_f32_16x16x32_bf16 v[56:59], v[136:139], a[68:71], v[56:59]\n"
    "v_mfma_f32_16x16x32_bf16 v[88:91], v[136:139], a[72:75], v[88:91]\n"
    "v_mfma_f32_16x16x32_bf16 v[120:123], v[136:139], a[76:79], v[120:123]\n"
    "ds_read_b64_tr_b16 v[216:217], v226 offset:40960\n"
    "v_mfma_f32_16x16x32_bf16 v[24:27], v[136:139], a[64:67], v[24:27]\n"
    "ds_read_b64_tr_b16 v[218:219], v226 offset:45056\n"
    "v_mfma_f32_16x16x32_bf16 v[28:31], v[140:143], a[64:67], v[28:31]\n"
    "buffer_load_dwordx4 a[248:251], v198, s[44:47], s77 offen sc1\n"
    "v_mfma_f32_16x16x32_bf16 v[124:127], v[140:143], a[76:79], v[124:127]\n"
    "v_mfma_f32_16x16x32_bf16 v[92:95], v[140:143], a[72:75], v[92:95]\n"
    "ds_read_b64_tr_b16 v[220:221], v227 offset:40960\n"
    "v_mfma_f32_16x16x32_bf16 v[60:63], v[140:143], a[68:71], v[60:63]\n"
    "s_waitcnt vmcnt(43) lgkmcnt(3)\n"
    "ds_read_b64_tr_b16 v[222:223], v227 offset:45056\n"
    "v_mfma_f32_16x16x32_bf16 v[32:35], v[208:211], a[84:87], v[32:35]\n"
    "buffer_load_dwordx4 v[184:187], v242, s[20:23], s16 offen offset:128\n"
    "v_mfma_f32_16x16x32_bf16 v[64:67], v[208:211], a[88:91], v[64:67]\n"
    "v_mfma_f32_16x16x32_bf16 v[96:99], v[208:211], a[92:95], v[96:99]\n"
    "ds_read_b64_tr_b16 v[128:129], v204 offset:40960\n"
    "v_mfma_f32_16x16x32_bf16 v[0:3], v[208:211], a[80:83], v[0:3]\n"
    "ds_read_b64_tr_b16 v[130:131], v204 offset:45056\n"
    "buffer_load_dwordx4 v201, s[36:39], s98 offen offset:1024 lds\n"
    "v_mfma_f32_16x16x32_bf16 v[4:7], v[212:215], a[80:83], v[4:7]\n"
    "buffer_load_dwordx4 a[4:7], v193, s[44:47], s78 offen sc1\n"
    "v_mfma_f32_16x16x32_bf16 v[100:103], v[212:215], a[92:95], v[100:103]\n"
    "v_mfma_f32_16x16x32_bf16 v[68:71], v[212:215], a[88:91], v[68:71]\n"
    "ds_read_b64_tr_b16 v[132:133], v241 offset:40960\n"
    "v_mfma_f32_16x16x32_bf16 v[36:39], v[212:215], a[84:87], v[36:39]\n"
    "s_waitcnt lgkmcnt(3)\n"
    "ds_read_b64_tr_b16 v[134:135], v241 offset:45056\n"
    "v_mfma_f32_16x16x32_bf16 v[40:43], v[216:219], a[84:87], v[40:43]\n"
    "v_mfma_f32_16x16x32_bf16 v[72:75], v[216:219], a[88:91], v[72:75]\n"
    "v_mfma_f32_16x16x32_bf16 v[104:107], v[216:219], a[92:95], v[104:107]\n"
    "ds_read_b64_tr_b16 v[136:137], v252 offset:40960\n"
    "v_mfma_f32_16x16x32_bf16 v[8:11], v[216:219], a[80:83], v[8:11]\n"
    "ds_read_b64_tr_b16 v[138:139], v252 offset:45056\n"
    "v_mfma_f32_16x16x32_bf16 v[12:15], v[220:223], a[80:83], v[12:15]\n"
    "buffer_load_dwordx4 a[12:15], v195, s[44:47], s78 offen sc1\n"
    "buffer_load_dwordx4 v[188:191], v242, s[20:23], s16 offen offset:192\n"
    "v_mfma_f32_16x16x32_bf16 v[108:111], v[220:223], a[92:95], v[108:111]\n"
    "v_mfma_f32_16x16x32_bf16 v[76:79], v[220:223], a[88:91], v[76:79]\n"
    "ds_read_b64_tr_b16 v[140:141], v253 offset:40960\n"
    "v_mfma_f32_16x16x32_bf16 v[44:47], v[220:223], a[84:87], v[44:47]\n"
    "s_waitcnt lgkmcnt(3)\n"
    "ds_read_b64_tr_b16 v[142:143], v253 offset:45056\n"
    "v_mfma_f32_16x16x32_bf16 v[48:51], v[128:131], a[84:87], v[48:51]\n"
    "v_mfma_f32_16x16x32_bf16 v[80:83], v[128:131], a[88:91], v[80:83]\n"
    "buffer_load_dwordx4 v202, s[36:39], s98 offen offset:2048 lds\n"
    "v_mfma_f32_16x16x32_bf16 v[112:115], v[128:131], a[92:95], v[112:115]\n"
    "ds_read_b64_tr_b16 v[208:209], v224 offset:49152\n"
    "v_mfma_f32_16x16x32_bf16 v[16:19], v[128:131], a[80:83], v[16:19]\n"
    "ds_read_b64_tr_b16 v[210:211], v224 offset:53248\n"
    "v_mfma_f32_16x16x32_bf16 v[20:23], v[132:135], a[80:83], v[20:23]\n"
    "buffer_load_dwordx4 a[20:23], v197, s[44:47], s78 offen sc1\n"
    "v_mfma_f32_16x16x32_bf16 v[116:119], v[132:135], a[92:95], v[116:119]\n"
    "v_mfma_f32_16x16x32_bf16 v[84:87], v[132:135], a[88:91], v[84:87]\n"
    "ds_read_b64_tr_b16 v[212:213], v225 offset:49152\n"
    "v_mfma_f32_16x16x32_bf16 v[52:55], v[132:135], a[84:87], v[52:55]\n"
    "s_waitcnt lgkmcnt(3)\n"
    "ds_read_b64_tr_b16 v[214:215], v225 offset:53248\n"
    "v_mfma_f32_16x16x32_bf16 v[56:59], v[136:139], a[84:87], v[56:59]\n"
    "v_mfma_f32_16x16x32_bf16 v[88:91], v[136:139], a[88:91], v[88:91]\n"
    "v_mfma_f32_16x16x32_bf16 v[120:123], v[136:139], a[92:95], v[120:123]\n"
    "ds_read_b64_tr_b16 v[216:217], v226 offset:49152\n"
    "v_mfma_f32_16x16x32_bf16 v[24:27], v[136:139], a[80:83], v[24:27]\n"
    "ds_read_b64_tr_b16 v[218:219], v226 offset:53248\n"
    "v_mfma_f32_16x16x32_bf16 v[28:31], v[140:143], a[80:83], v[28:31]\n"
    "buffer_load_dwordx4 a[28:31], v199, s[44:47], s78 offen sc1\n"
    "v_mfma_f32_16x16x32_bf16 v[124:127], v[140:143], a[92:95], v[124:127]\n"
    "v_mfma_f32_16x16x32_bf16 v[92:95], v[140:143], a[88:91], v[92:95]\n"
    "ds_read_b64_tr_b16 v[220:221], v227 offset:49152\n"
    "v_mfma_f32_16x16x32_bf16 v[60:63], v[140:143], a[84:87], v[60:63]\n"
    "s_waitcnt vmcnt(42) lgkmcnt(3)\n"
    "ds_read_b64_tr_b16 v[222:223], v227 offset:53248\n"
    "v_mfma_f32_16x16x32_bf16 v[32:35], v[208:211], a[100:103], v[32:35]\n"
    "buffer_load_dwordx4 v203, s[36:39], s98 offen offset:3072 lds\n"
    "v_mfma_f32_16x16x32_bf16 v[64:67], v[208:211], a[104:107], v[64:67]\n"
    "v_mfma_f32_16x16x32_bf16 v[96:99], v[208:211], a[108:111], v[96:99]\n"
    "ds_read_b64_tr_b16 v[128:129], v204 offset:49152\n"
    "v_mfma_f32_16x16x32_bf16 v[0:3], v[208:211], a[96:99], v[0:3]\n"
    "ds_read_b64_tr_b16 v[130:131], v204 offset:53248\n"
    "v_mfma_f32_16x16x32_bf16 v[4:7], v[212:215], a[96:99], v[4:7]\n"
    "buffer_load_dwordx4 a[36:39], v193, s[44:47], s79 offen sc1\n"
    "v_mfma_f32_16x16x32_bf16 v[100:103], v[212:215], a[108:111], v[100:103]\n"
    "v_mfma_f32_16x16x32_bf16 v[68:71], v[212:215], a[104:107], v[68:71]\n"
    "ds_read_b64_tr_b16 v[132:133], v241 offset:49152\n"
    "v_mfma_f32_16x16x32_bf16 v[36:39], v[212:215], a[100:103], v[36:39]\n"
    "s_waitcnt lgkmcnt(3)\n"
    "ds_read_b64_tr_b16 v[134:135], v241 offset:53248\n"
    "v_mfma_f32_16x16x32_bf16 v[40:43], v[216:219], a[100:103], v[40:43]\n"
    "v_mfma_f32_16x16x32_bf16 v[72:75], v[216:219], a[104:107], v[72:75]\n"
    "v_mfma_f32_16x16x32_bf16 v[104:107], v[216:219], a[108:111], v[104:107]\n"
    "ds_read_b64_tr_b16 v[136:137], v252 offset:49152\n"
    "v_mfma_f32_16x16x32_bf16 v[8:11], v[216:219], a[96:99], v[8:11]\n"
    "ds_read_b64_tr_b16 v[138:139], v252 offset:53248\n"
    "v_mfma_f32_16x16x32_bf16 v[12:15], v[220:223], a[96:99], v[12:15]\n"
    "buffer_load_dwordx4 a[44:47], v195, s[44:47], s79 offen sc1\n"
    "v_mfma_f32_16x16x32_bf16 v[108:111], v[220:223], a[108:111], v[108:111]\n"
    "v_mfma_f32_16x16x32_bf16 v[76:79], v[220:223], a[104:107], v[76:79]\n"
    "ds_read_b64_tr_b16 v[140:141], v253 offset:49152\n"
    "v_mfma_f32_16x16x32_bf16 v[44:47], v[220:223], a[100:103], v[44:47]\n"
    "s_waitcnt lgkmcnt(3)\n"
    "ds_read_b64_tr_b16 v[142:143], v253 offset:53248\n"
    "v_mfma_f32_16x16x32_bf16 v[48:51], v[128:131], a[100:103], v[48:51]\n"
    "v_mfma_f32_16x16x32_bf16 v[80:83], v[128:131], a[104:107], v[80:83]\n"
    "v_mfma_f32_16x16x32_bf16 v[112:115], v[128:131], a[108:111], v[112:115]\n"
    "ds_read_b64_tr_b16 v[208:209], v224 offset:57344\n"
    "v_mfma_f32_16x16x32_bf16 v[16:19], v[128:131], a[96:99], v[16:19]\n"
    "ds_read_b64_tr_b16 v[210:211], v224 offset:61440\n"
    "v_mfma_f32_16x16x32_bf16 v[20:23], v[132:135], a[96:99], v[20:23]\n"
    "buffer_load_dwordx4 a[52:55], v197, s[44:47], s79 offen sc1\n"
    "v_mfma_f32_16x16x32_bf16 v[116:119], v[132:135], a[108:111], v[116:119]\n"
    "v_mfma_f32_16x16x32_bf16 v[84:87], v[132:135], a[104:107], v[84:87]\n"
    "ds_read_b64_tr_b16 v[212:213], v225 offset:57344\n"
    "v_mfma_f32_16x16x32_bf16 v[52:55], v[132:135], a[100:103], v[52:55]\n"
    "s_waitcnt lgkmcnt(3)\n"
    "ds_read_b64_tr_b16 v[214:215], v225 offset:61440\n"
    "v_mfma_f32_16x16x32_bf16 v[56:59], v[136:139], a[100:103], v[56:59]\n"
    "v_mfma_f32_16x16x32_bf16 v[88:91], v[136:139], a[104:107], v[88:91]\n"
    "v_mfma_f32_16x16x32_bf16 v[120:123], v[136:139], a[108:111], v[120:123]\n"
    "ds_read_b64_tr_b16 v[216:217], v226 offset:57344\n"
    "v_mfma_f32_16x16x32_bf16 v[24:27], v[136:139], a[96:99], v[24:27]\n"
    "ds_read_b64_tr_b16 v[218:219], v226 offset:61440\n"
    "v_mfma_f32_16x16x32_bf16 v[28:31], v[140:143], a[96:99], v[28:31]\n"
    "buffer_load_dwordx4 a[60:63], v199, s[44:47], s79 offen sc1\n"
    "v_mfma_f32_16x16x32_bf16 v[124:127], v[140:143], a[108:111], v[124:127]\n"
    "v_mfma_f32_16x16x32_bf16 v[92:95], v[140:143], a[104:107], v[92:95]\n"
    "ds_read_b64_tr_b16 v[220:221], v227 offset:57344\n"
    "v_mfma_f32_16x16x32_bf16 v[60:63], v[140:143], a[100:103], v[60:63]\n"
    "s_waitcnt vmcnt(40) lgkmcnt(3)\n"
    "ds_read_b64_tr_b16 v[222:223], v227 offset:61440\n"
    "v_mfma_f32_16x16x32_bf16 v[32:35], v[208:211], a[116:119], v[32:35]\n"
    "v_mfma_f32_16x16x32_bf16 v[64:67], v[208:211], a[120:123], v[64:67]\n"
    "v_mfma_f32_16x16x32_bf16 v[96:99], v[208:211], a[124:127], v[96:99]\n"
    "ds_read_b64_tr_b16 v[128:129], v204 offset:57344\n"
    "v_mfma_f32_16x16x32_bf16 v[0:3], v[208:211], a[112:115], v[0:3]\n"
    "ds_read_b64_tr_b16 v[130:131], v204 offset:61440\n"
    "v_mfma_f32_16x16x32_bf16 v[4:7], v[212:215], a[112:115], v[4:7]\n"
    "buffer_load_dwordx4 a[0:3], v192, s[44:47], s78 offen sc1\n"
    "v_mfma_f32_16x16x32_bf16 v[100:103], v[212:215], a[124:127], v[100:103]\n"
    "v_mfma_f32_16x16x32_bf16 v[68:71], v[212:215], a[120:123], v[68:71]\n"
    "ds_read_b64_tr_b16 v[132:133], v241 offset:57344\n"
    "v_mfma_f32_16x16x32_bf16 v[36:39], v[212:215], a[116:119], v[36:39]\n"
    "s_waitcnt lgkmcnt(3)\n"
    "ds_read_b64_tr_b16 v[134:135], v241 offset:61440\n"
    "v_mfma_f32_16x16x32_bf16 v[40:43], v[216:219], a[116:119], v[40:43]\n"
    "v_mfma_f32_16x16x32_bf16 v[72:75], v[216:219], a[120:123], v[72:75]\n"
    "s_add_u32 m0, s85, 0x4000\n"
    "s_lshl_b32 s98, s79, 4\n"
    "v_mfma_f32_16x16x32_bf16 v[104:107], v[216:219], a[124:127], v[104:107]\n"
    "ds_read_b64_tr_b16 v[136:137], v252 offset:57344\n"
    "s_sub_u32 s98, s98, 0x1c0000\n"
    "v_mfma_f32_16x16x32_bf16 v[8:11], v[216:219], a[112:115], v[8:11]\n"
    "ds_read_b64_tr_b16 v[138:139], v252 offset:61440\n"
    "v_mfma_f32_16x16x32_bf16 v[12:15], v[220:223], a[112:115], v[12:15]\n"
    "buffer_load_dwordx4 a[8:11], v194, s[44:47], s78 offen sc1\n"
    "v_mfma_f32_16x16x32_bf16 v[108:111], v[220:223], a[124:127], v[108:111]\n"
    "v_mfma_f32_16x16x32_bf16 v[76:79], v[220:223], a[120:123], v[76:79]\n"
    "ds_read_b64_tr_b16 v[140:141], v253 offset:57344\n"
    "v_mfma_f32_16x16x32_bf16 v[44:47], v[220:223], a[116:119], v[44:47]\n"
    "s_waitcnt lgkmcnt(3)\n"
    "s_waitcnt vmcnt(19)\n"
    "v_mfma_f32_16x16x32_bf16 v[48:51], v[128:131], a[116:119], v[48:51]\n"
    "s_barrier\n"
    "ds_read_b64_tr_b16 v[142:143], v253 offset:61440\n"
    "v_mfma_f32_16x16x32_bf16 v[80:83], v[128:131], a[120:123], v[80:83]\n"
    "v_mfma_f32_16x16x32_bf16 v[112:115], v[128:131], a[124:127], v[112:115]\n"
    "ds_read_b64_tr_b16 v[208:209], v228\n"
    "v_mfma_f32_16x16x32_bf16 v[16:19], v[128:131], a[112:115], v[16:19]\n"
    "ds_read_b64_tr_b16 v[210:211], v228 offset:4096\n"
    "v_mfma_f32_16x16x32_bf16 v[20:23], v[132:135], a[112:115], v[20:23]\n"
    "ds_read_b64_tr_b16 v[212:213], v229\n"
    "buffer_load_dwordx4 a[16:19], v196, s[44:47], s78 offen sc1\n"
    "v_mfma_f32_16x16x32_bf16 v[116:119], v[132:135], a[124:127], v[116:119]\n"
    "ds_read_b64_tr_b16 v[214:215], v229 offset:4096\n"
    "buffer_load_dwordx4 v200, s[36:39], s98 offen lds\n"
    "v_mfma_f32_16x16x32_bf16 v[84:87], v[132:135], a[120:123], v[84:87]\n"
    "ds_read_b64_tr_b16 v[216:217], v230\n"
    "v_mfma_f32_16x16x32_bf16 v[52:55], v[132:135], a[116:119], v[52:55]\n"
    "s_waitcnt lgkmcnt(5)\n"
    "ds_read_b64_tr_b16 v[218:219], v230 offset:4096\n"
    "v_mfma_f32_16x16x32_bf16 v[56:59], v[136:139], a[116:119], v[56:59]\n"
    "ds_read_b64_tr_b16 v[220:221], v231\n"
    "v_mfma_f32_16x16x32_bf16 v[88:91], v[136:139], a[120:123], v[88:91]\n"
    "ds_read_b64_tr_b16 v[222:223], v231 offset:4096\n"
    "v_mfma_f32_16x16x32_bf16 v[120:123], v[136:139], a[124:127], v[120:123]\n"
    "ds_read_b64_tr_b16 v[128:129], v232\n"
    "v_mfma_f32_16x16x32_bf16 v[24:27], v[136:139], a[112:115], v[24:27]\n"
    "ds_read_b64_tr_b16 v[130:131], v232 offset:4096\n"
    "v_mfma_f32_16x16x32_bf16 v[28:31], v[140:143], a[112:115], v[28:31]\n"
    "ds_read_b64_tr_b16 v[132:133], v233\n"
    "buffer_load_dwordx4 a[24:27], v198, s[44:47], s78 offen sc1\n"
    "v_mfma_f32_16x16x32_bf16 v[124:127], v[140:143], a[124:127], v[124:127]\n"
    "ds_read_b64_tr_b16 v[134:135], v233 offset:4096\n"
    "buffer_load_dwordx4 v201, s[36:39], s98 offen offset:1024 lds\n"
    "v_mfma_f32_16x16x32_bf16 v[92:95], v[140:143], a[120:123], v[92:95]\n"
    "ds_read_b64_tr_b16 v[136:137], v234\n"
    "v_mfma_f32_16x16x32_bf16 v[60:63], v[140:143], a[116:119], v[60:63]\n"
    "s_waitcnt lgkmcnt(9)\n"
    "ds_read_b64_tr_b16 v[138:139], v234 offset:4096\n"
    "v_mfma_f32_16x16x32_bf16 v[32:35], v[208:211], a[132:135], v[32:35]\n"
    "ds_read_b64_tr_b16 v[140:141], v235\n"
    "buffer_load_dword v246, v245, s[44:47], 0 offen sc1\n"
    "v_mfma_f32_16x16x32_bf16 v[64:67], v[208:211], a[136:139], v[64:67]\n"
    "ds_read_b64_tr_b16 v[142:143], v235 offset:4096\n"
    "v_mbcnt_lo_u32_b32 v251, -1, 0\n"
    "v_mfma_f32_16x16x32_bf16 v[68:71], v[212:215], a[136:139], v[68:71]\n"
    "ds_read_b64_tr_b16 v[208:209], v228 offset:8192\n"
    "v_mbcnt_hi_u32_b32 v251, -1, v251\n"
    "v_mfma_f32_16x16x32_bf16 v[36:39], v[212:215], a[132:135], v[36:39]\n"
    "s_waitcnt lgkmcnt(9)\n"
    "ds_read_b64_tr_b16 v[210:211], v228 offset:12288\n"
    "v_mfma_f32_16x16x32_bf16 v[40:43], v[216:219], a[132:135], v[40:43]\n"
    "ds_read_b64_tr_b16 v[212:213], v229 offset:8192\n"
    "buffer_load_dwordx4 a[32:35], v192, s[44:47], s79 offen sc1\n"
    "v_mfma_f32_16x16x32_bf16 v[72:75], v[216:219], a[136:139], v[72:75]\n"
    "ds_read_b64_tr_b16 v[214:215], v229 offset:12288\n"
    "buffer_load_dwordx4 v202, s[36:39], s98 offen offset:2048 lds\n"
    "v_mfma_f32_16x16x32_bf16 v[76:79], v[220:223], a[136:139], v[76:79]\n"
    "ds_read_b64_tr_b16 v[216:217], v230 offset:8192\n"
    "v_mfma_f32_16x16x32_bf16 v[44:47], v[220:223], a[132:135], v[44:47]\n"
    "s_waitcnt lgkmcnt(9)\n"
    "ds_read_b64_tr_b16 v[218:219], v230 offset:12288\n"
    "v_mfma_f32_16x16x32_bf16 v[48:51], v[128:131], a[132:135], v[48:51]\n"
    "ds_read_b64_tr_b16 v[220:221], v231 offset:8192\n"
    "v_mfma_f32_16x16x32_bf16 v[80:83], v[128:131], a[136:139], v[80:83]\n"
    "ds_read_b64_tr_b16 v[222:223], v231 offset:12288\n"
    "v_mfma_f32_16x16x32_bf16 v[84:87], v[132:135], a[136:139], v[84:87]\n"
    "ds_read_b64_tr_b16 v[128:129], v232 offset:8192\n"
    "v_mfma_f32_16x16x32_bf16 v[52:55], v[132:135], a[132:135], v[52:55]\n"
    "s_waitcnt lgkmcnt(9)\n"
    "ds_read_b64_tr_b16 v[130:131], v232 offset:12288\n"
    "v_mfma_f32_16x16x32_bf16 v[56:59], v[136:139], a[132:135], v[56:59]\n"
    "ds_read_b64_tr_b16 v[132:133], v233 offset:8192\n"
    "buffer_load_dwordx4 a[40:43], v194, s[44:47], s79 offen sc1\n"
    "v_mfma_f32_16x16x32_bf16 v[88:91], v[136:139], a[136:139], v[88:91]\n"
    "ds_read_b64_tr_b16 v[134:135], v233 offset:12288\n"
    "buffer_load_dwordx4 v203, s[36:39], s98 offen offset:3072 lds\n"
    "v_mfma_f32_16x16x32_bf16 v[92:95], v[140:143], a[136:139], v[92:95]\n"
    "ds_read_b64_tr_b16 v[136:137], v234 offset:8192\n"
    "v_mfma_f32_16x16x32_bf16 v[60:63], v[140:143], a[132:135], v[60:63]\n"
    "s_waitcnt lgkmcnt(9)\n"
    "ds_read_b64_tr_b16 v[138:139], v234 offset:12288\n"
    "v_mfma_f32_16x16x32_bf16 v[32:35], v[208:211], a[148:151], v[32:35]\n"
    "ds_read_b64_tr_b16 v[140:141], v235 offset:8192\n"
    "v_mfma_f32_16x16x32_bf16 v[64:67], v[208:211], a[152:155], v[64:67]\n"
    "ds_read_b64_tr_b16 v[142:143], v235 offset:12288\n"
    "v_mfma_f32_16x16x32_bf16 v[68:71], v[212:215], a[152:155], v[68:71]\n"
    "ds_read_b64_tr_b16 v[208:209], v228 offset:16384\n"
    "v_mfma_f32_16x16x32_bf16 v[36:39], v[212:215], a[148:151], v[36:39]\n"
    "s_waitcnt lgkmcnt(9)\n"
    "ds_read_b64_tr_b16 v[210:211], v228 offset:20480\n"
    "v_mfma_f32_16x16x32_bf16 v[40:43], v[216:219], a[148:151], v[40:43]\n"
    "ds_read_b64_tr_b16 v[212:213], v229 offset:16384\n"
    "buffer_load_dwordx4 a[48:51], v196, s[44:47], s79 offen sc1\n"
    "v_mfma_f32_16x16x32_bf16 v[72:75], v[216:219], a[152:155], v[72:75]\n"
    "ds_read_b64_tr_b16 v[214:215], v229 offset:20480\n"
    "v_mfma_f32_16x16x32_bf16 v[76:79], v[220:223], a[152:155], v[76:79]\n"
    "ds_read_b64_tr_b16 v[216:217], v230 offset:16384\n"
    "v_mfma_f32_16x16x32_bf16 v[44:47], v[220:223], a[148:151], v[44:47]\n"
    "s_waitcnt lgkmcnt(9)\n"
    "ds_read_b64_tr_b16 v[218:219], v230 offset:20480\n"
    "v_mfma_f32_16x16x32_bf16 v[48:51], v[128:131], a[148:151], v[48:51]\n"
    "ds_read_b64_tr_b16 v[220:221], v231 offset:16384\n"
    "v_mfma_f32_16x16x32_bf16 v[80:83], v[128:131], a[152:155], v[80:83]\n"
    "ds_read_b64_tr_b16 v[222:223], v231 offset:20480\n"
    "v_mfma_f32_16x16x32_bf16 v[84:87], v[132:135], a[152:155], v[84:87]\n"
    "ds_read_b64_tr_b16 v[128:129], v232 offset:16384\n"
    "v_mfma_f32_16x16x32_bf16 v[52:55], v[132:135], a[148:151], v[52:55]\n"
    "s_waitcnt lgkmcnt(9)\n"
    "ds_read_b64_tr_b16 v[130:131], v232 offset:20480\n"
    "v_mfma_f32_16x16x32_bf16 v[56:59], v[136:139], a[148:151], v[56:59]\n"
    "ds_read_b64_tr_b16 v[132:133], v233 offset:16384\n"
    "buffer_load_dwordx4 a[56:59], v198, s[44:47], s79 offen sc1\n"
    "v_mfma_f32_16x16x32_bf16 v[88:91], v[136:139], a[152:155], v[88:91]\n"
    "ds_read_b64_tr_b16 v[134:135], v233 offset:20480\n"
    "v_mfma_f32_16x16x32_bf16 v[92:95], v[140:143], a[152:155], v[92:95]\n"
    "ds_read_b64_tr_b16 v[136:137], v234 offset:16384\n"
    "v_mfma_f32_16x16x32_bf16 v[60:63], v[140:143], a[148:151], v[60:63]\n"
    "s_waitcnt lgkmcnt(9)\n"
    "ds_read_b64_tr_b16 v[138:139], v234 offset:20480\n"
    "v_mfma_f32_16x16x32_bf16 v[32:35], v[208:211], a[164:167], v[32:35]\n"
    "ds_read_b64_tr_b16 v[140:141], v235 offset:16384\n"
    "s_waitcnt vmcnt(21)\n"
    "v_mfma_f32_16x16x32_bf16 v[64:67], v[208:211], a[168:171], v[64:67]\n"
    "ds_read_b64_tr_b16 v[142:143], v235 offset:20480\n"
    "v_permlane16_swap_b32 v176, v178\n"
    "v_mfma_f32_16x16x32_bf16 v[68:71], v[212:215], a[168:171], v[68:71]\n"
    "ds_read_b64_tr_b16 v[208:209], v228 offset:24576\n"
    "v_permlane16_swap_b32 v177, v179\n"
    "v_mfma_f32_16x16x32_bf16 v[36:39], v[212:215], a[164:167], v[36:39]\n"
    "s_waitcnt lgkmcnt(9)\n"
    "ds_read_b64_tr_b16 v[210:211], v228 offset:28672\n"
    "v_mfma_f32_16x16x32_bf16 v[40:43], v[216:219], a[164:167], v[40:43]\n"
    "ds_read_b64_tr_b16 v[212:213], v229 offset:24576\n"
    "buffer_load_dwordx4 a[128:131], v192, s[44:47], s68 offen sc1\n"
    "v_mfma_f32_16x16x32_bf16 v[72:75], v[216:219], a[168:171], v[72:75]\n"
    "ds_read_b64_tr_b16 v[214:215], v229 offset:28672\n"
    "v_permlane16_swap_b32 v180, v182\n"
    "v_mfma_f32_16x16x32_bf16 v[76:79], v[220:223], a[168:171], v[76:79]\n"
    "ds_read_b64_tr_b16 v[216:217], v230 offset:24576\n"
    "v_permlane16_swap_b32 v181, v183\n"
    "v_mfma_f32_16x16x32_bf16 v[44:47], v[220:223], a[164:167], v[44:47]\n"
    "s_waitcnt lgkmcnt(9)\n"
    "ds_read_b64_tr_b16 v[218:219], v230 offset:28672\n"
    "v_mfma_f32_16x16x32_bf16 v[48:51], v[128:131], a[164:167], v[48:51]\n"
    "ds_read_b64_tr_b16 v[220:221], v231 offset:24576\n"
    "v_permlane16_swap_b32 v184, v186\n"
    "v_mfma_f32_16x16x32_bf16 v[80:83], v[128:131], a[168:171], v[80:83]\n"
    "ds_read_b64_tr_b16 v[222:223], v231 offset:28672\n"
    "v_permlane16_swap_b32 v185, v187\n"
    "v_mfma_f32_16x16x32_bf16 v[84:87], v[132:135], a[168:171], v[84:87]\n"
    "ds_read_b64_tr_b16 v[128:129], v232 offset:24576\n"
    "v_permlane16_swap_b32 v188, v190\n"
    "v_mfma_f32_16x16x32_bf16 v[52:55], v[132:135], a[164:167], v[52:55]\n"
    "s_waitcnt lgkmcnt(9)\n"
    "ds_read_b64_tr_b16 v[130:131], v232 offset:28672\n"
    "v_mfma_f32_16x16x32_bf16 v[56:59], v[136:139], a[164:167], v[56:59]\n"
    "ds_read_b64_tr_b16 v[132:133], v233 offset:24576\n"
    "buffer_load_dwordx4 a[140:143], v195, s[44:47], s68 offen sc1\n"
    "v_mfma_f32_16x16x32_bf16 v[88:91], v[136:139], a[168:171], v[88:91]\n"
    "ds_read_b64_tr_b16 v[134:135], v233 offset:28672\n"
    "v_permlane16_swap_b32 v189, v191\n"
    "v_mfma_f32_16x16x32_bf16 v[92:95], v[140:143], a[168:171], v[92:95]\n"
    "ds_read_b64_tr_b16 v[136:137], v234 offset:24576\n"
    "v_mfma_f32_16x16x32_bf16 v[60:63], v[140:143], a[164:167], v[60:63]\n"
    "s_waitcnt lgkmcnt(9)\n"
    "ds_read_b64_tr_b16 v[138:139], v234 offset:28672\n"
    "v_mfma_f32_16x16x32_bf16 v[32:35], v[208:211], a[180:183], v[32:35]\n"
    "ds_read_b64_tr_b16 v[140:141], v235 offset:24576\n"
    "v_mfma_f32_16x16x32_bf16 v[64:67], v[208:211], a[184:187], v[64:67]\n"
    "ds_read_b64_tr_b16 v[142:143], v235 offset:28672\n"
    "v_mfma_f32_16x16x32_bf16 v[68:71], v[212:215], a[184:187], v[68:71]\n"
    "ds_read_b64_tr_b16 v[208:209], v228 offset:32768\n"
    "v_mfma_f32_16x16x32_bf16 v[36:39], v[212:215], a[180:183], v[36:39]\n"
    "s_waitcnt lgkmcnt(9)\n"
    "ds_read_b64_tr_b16 v[210:211], v228 offset:36864\n"
    "v_mfma_f32_16x16x32_bf16 v[40:43], v[216:219], a[180:183], v[40:43]\n"
    "ds_read_b64_tr_b16 v[212:213], v229 offset:32768\n"
    "buffer_load_dwordx4 a[144:147], v196, s[44:47], s68 offen sc1\n"
    "v_mfma_f32_16x16x32_bf16 v[72:75], v[216:219], a[184:187], v[72:75]\n"
    "ds_read_b64_tr_b16 v[214:215], v229 offset:36864\n"
    "v_mfma_f32_16x16x32_bf16 v[76:79], v[220:223], a[184:187], v[76:79]\n"
    "ds_read_b64_tr_b16 v[216:217], v230 offset:32768\n"
    "v_mfma_f32_16x16x32_bf16 v[44:47], v[220:223], a[180:183], v[44:47]\n"
    "s_waitcnt lgkmcnt(9)\n"
    "ds_read_b64_tr_b16 v[218:219], v230 offset:36864\n"
    "v_mfma_f32_16x16x32_bf16 v[48:51], v[128:131], a[180:183], v[48:51]\n"
    "ds_read_b64_tr_b16 v[220:221], v231 offset:32768\n"
    "v_mfma_f32_16x16x32_bf16 v[80:83], v[128:131], a[184:187], v[80:83]\n"
    "ds_read_b64_tr_b16 v[222:223], v231 offset:36864\n"
    "v_mfma_f32_16x16x32_bf16 v[84:87], v[132:135], a[184:187], v[84:87]\n"
    "ds_read_b64_tr_b16 v[128:129], v232 offset:32768\n"
    "v_mfma_f32_16x16x32_bf16 v[52:55], v[132:135], a[180:183], v[52:55]\n"
    "s_waitcnt lgkmcnt(9)\n"
    "ds_read_b64_tr_b16 v[130:131], v232 offset:36864\n"
    "v_mfma_f32_16x16x32_bf16 v[56:59], v[136:139], a[180:183], v[56:59]\n"
    "ds_read_b64_tr_b16 v[132:133], v233 offset:32768\n"
    "buffer_load_dwordx4 a[156:159], v199, s[44:47], s68 offen sc1\n"
    "v_mfma_f32_16x16x32_bf16 v[88:91], v[136:139], a[184:187], v[88:91]\n"
    "ds_read_b64_tr_b16 v[134:135], v233 offset:36864\n"
    "v_mfma_f32_16x16x32_bf16 v[92:95], v[140:143], a[184:187], v[92:95]\n"
    "ds_read_b64_tr_b16 v[136:137], v234 offset:32768\n"
    "v_mfma_f32_16x16x32_bf16 v[60:63], v[140:143], a[180:183], v[60:63]\n"
    "s_waitcnt lgkmcnt(9)\n"
    "ds_read_b64_tr_b16 v[138:139], v234 offset:36864\n"
    "v_mfma_f32_16x16x32_bf16 v[32:35], v[208:211], a[196:199], v[32:35]\n"
    "ds_read_b64_tr_b16 v[140:141], v235 offset:32768\n"
    "v_mfma_f32_16x16x32_bf16 v[64:67], v[208:211], a[200:203], v[64:67]\n"
    "ds_read_b64_tr_b16 v[142:143], v235 offset:36864\n"
    "v_mfma_f32_16x16x32_bf16 v[68:71], v[212:215], a[200:203], v[68:71]\n"
    "ds_read_b64_tr_b16 v[208:209], v228 offset:40960\n"
    "v_mfma_f32_16x16x32_bf16 v[36:39], v[212:215], a[196:199], v[36:39]\n"
    "s_waitcnt lgkmcnt(9)\n"
    "ds_read_b64_tr_b16 v[210:211], v228 offset:45056\n"
    "v_mfma_f32_16x16x32_bf16 v[40:43], v[216:219], a[196:199], v[40:43]\n"
    "ds_read_b64_tr_b16 v[212:213], v229 offset:40960\n"
    "buffer_load_dwordx4 a[160:163], v192, s[44:47], s69 offen sc1\n"
    "v_mfma_f32_16x16x32_bf16 v[72:75], v[216:219], a[200:203], v[72:75]\n"
    "ds_read_b64_tr_b16 v[214:215], v229 offset:45056\n"
    "v_mfma_f32_16x16x32_bf16 v[76:79], v[220:223], a[200:203], v[76:79]\n"
    "ds_read_b64_tr_b16 v[216:217], v230 offset:40960\n"
    "v_mfma_f32_16x16x32_bf16 v[44:47], v[220:223], a[196:199], v[44:47]\n"
    "s_waitcnt lgkmcnt(9)\n"
    "ds_read_b64_tr_b16 v[218:219], v230 offset:45056\n"
    "v_mfma_f32_16x16x32_bf16 v[48:51], v[128:131], a[196:199], v[48:51]\n"
    "ds_read_b64_tr_b16 v[220:221], v231 offset:40960\n"
    "v_mfma_f32_16x16x32_bf16 v[80:83], v[128:131], a[200:203], v[80:83]\n"
    "ds_read_b64_tr_b16 v[222:223], v231 offset:45056\n"
    "v_mfma_f32_16x16x32_bf16 v[84:87], v[132:135], a[200:203], v[84:87]\n"
    "ds_read_b64_tr_b16 v[128:129], v232 offset:40960\n"
    "v_mfma_f32_16x16x32_bf16 v[52:55], v[132:135], a[196:199], v[52:55]\n"
    "s_waitcnt lgkmcnt(9)\n"
    "ds_read_b64_tr_b16 v[130:131], v232 offset:45056\n"
    "v_mfma_f32_16x16x32_bf16 v[56:59], v[136:139], a[196:199], v[56:59]\n"
    "ds_read_b64_tr_b16 v[132:133], v233 offset:40960\n"
    "buffer_load_dwordx4 a[172:175], v195, s[44:47], s69 offen sc1\n"
    "v_mfma_f32_16x16x32_bf16 v[88:91], v[136:139], a[200:203], v[88:91]\n"
    "ds_read_b64_tr_b16 v[134:135], v233 offset:45056\n"
    "v_mfma_f32_16x16x32_bf16 v[92:95], v[140:143], a[200:203], v[92:95]\n"
    "ds_read_b64_tr_b16 v[136:137], v234 offset:40960\n"
    "v_mfma_f32_16x16x32_bf16 v[60:63], v[140:143], a[196:199], v[60:63]\n"
    "s_waitcnt lgkmcnt(9)\n"
    "ds_read_b64_tr_b16 v[138:139], v234 offset:45056\n"
    "v_mfma_f32_16x16x32_bf16 v[32:35], v[208:211], a[212:215], v[32:35]\n"
    "ds_read_b64_tr_b16 v[140:141], v235 offset:40960\n"
    "v_mfma_f32_16x16x32_bf16 v[64:67], v[208:211], a[216:219], v[64:67]\n"
    "ds_read_b64_tr_b16 v[142:143], v235 offset:45056\n"
    "v_mfma_f32_16x16x32_bf16 v[68:71], v[212:215], a[216:219], v[68:71]\n"
    "ds_read_b64_tr_b16 v[208:209], v228 offset:49152\n"
    "v_mfma_f32_16x16x32_bf16 v[36:39], v[212:215], a[212:215], v[36:39]\n"
    "s_waitcnt lgkmcnt(9)\n"
    "ds_read_b64_tr_b16 v[210:211], v228 offset:53248\n"
    "v_mfma_f32_16x16x32_bf16 v[40:43], v[216:219], a[212:215], v[40:43]\n"
    "ds_read_b64_tr_b16 v[212:213], v229 offset:49152\n"
    "buffer_load_dwordx4 a[176:179], v196, s[44:47], s69 offen sc1\n"
    "v_mfma_f32_16x16x32_bf16 v[72:75], v[216:219], a[216:219], v[72:75]\n"
    "ds_read_b64_tr_b16 v[214:215], v229 offset:53248\n"
    "v_mfma_f32_16x16x32_bf16 v[76:79], v[220:223], a[216:219], v[76:79]\n"
    "ds_read_b64_tr_b16 v[216:217], v230 offset:49152\n"
    "v_mfma_f32_16x16x32_bf16 v[44:47], v[220:223], a[212:215], v[44:47]\n"
    "s_waitcnt lgkmcnt(9)\n"
    "ds_read_b64_tr_b16 v[218:219], v230 offset:53248\n"
    "v_mfma_f32_16x16x32_bf16 v[48:51], v[128:131], a[212:215], v[48:51]\n"
    "ds_read_b64_tr_b16 v[220:221], v231 offset:49152\n"
    "v_mfma_f32_16x16x32_bf16 v[80:83], v[128:131], a[216:219], v[80:83]\n"
    "ds_read_b64_tr_b16 v[222:223], v231 offset:53248\n"
    "v_mfma_f32_16x16x32_bf16 v[84:87], v[132:135], a[216:219], v[84:87]\n"
    "ds_read_b64_tr_b16 v[128:129], v232 offset:49152\n"
    "v_mfma_f32_16x16x32_bf16 v[52:55], v[132:135], a[212:215], v[52:55]\n"
    "s_waitcnt lgkmcnt(9)\n"
    "ds_read_b64_tr_b16 v[130:131], v232 offset:53248\n"
    "v_mfma_f32_16x16x32_bf16 v[56:59], v[136:139], a[212:215], v[56:59]\n"
    "ds_read_b64_tr_b16 v[132:133], v233 offset:49152\n"
    "buffer_load_dwordx4 a[188:191], v199, s[44:47], s69 offen sc1\n"
    "v_mfma_f32_16x16x32_bf16 v[88:91], v[136:139], a[216:219], v[88:91]\n"
    "ds_read_b64_tr_b16 v[134:135], v233 offset:53248\n"
    "v_mfma_f32_16x16x32_bf16 v[92:95], v[140:143], a[216:219], v[92:95]\n"
    "ds_read_b64_tr_b16 v[136:137], v234 offset:49152\n"
    "v_mfma_f32_16x16x32_bf16 v[60:63], v[140:143], a[212:215], v[60:63]\n"
    "s_waitcnt lgkmcnt(9)\n"
    "ds_read_b64_tr_b16 v[138:139], v234 offset:53248\n"
    "v_mfma_f32_16x16x32_bf16 v[32:35], v[208:211], a[228:231], v[32:35]\n"
    "ds_read_b64_tr_b16 v[140:141], v235 offset:49152\n"
    "v_mfma_f32_16x16x32_bf16 v[64:67], v[208:211], a[232:235], v[64:67]\n"
    "ds_read_b64_tr_b16 v[142:143], v235 offset:53248\n"
    "v_mfma_f32_16x16x32_bf16 v[68:71], v[212:215], a[232:235], v[68:71]\n"
    "ds_read_b64_tr_b16 v[208:209], v228 offset:57344\n"
    "v_mfma_f32_16x16x32_bf16 v[36:39], v[212:215], a[228:231], v[36:39]\n"
    "s_waitcnt lgkmcnt(9)\n"
    "ds_read_b64_tr_b16 v[210:211], v228 offset:61440\n"
    "v_mfma_f32_16x16x32_bf16 v[40:43], v[216:219], a[228:231], v[40:43]\n"
    "ds_read_b64_tr_b16 v[212:213], v229 offset:57344\n"
    "buffer_load_dwordx4 a[192:195], v192, s[44:47], s76 offen sc1\n"
    "v_mfma_f32_16x16x32_bf16 v[72:75], v[216:219], a[232:235], v[72:75]\n"
    "ds_read_b64_tr_b16 v[214:215], v229 offset:61440\n"
    "v_mfma_f32_16x16x32_bf16 v[76:79], v[220:223], a[232:235], v[76:79]\n"
    "ds_read_b64_tr_b16 v[216:217], v230 offset:57344\n"
    "v_mfma_f32_16x16x32_bf16 v[44:47], v[220:223], a[228:231], v[44:47]\n"
    "s_waitcnt lgkmcnt(9)\n"
    "ds_read_b64_tr_b16 v[218:219], v230 offset:61440\n"
    "v_mfma_f32_16x16x32_bf16 v[48:51], v[128:131], a[228:231], v[48:51]\n"
    "ds_read_b64_tr_b16 v[220:221], v231 offset:57344\n"
    "v_mfma_f32_16x16x32_bf16 v[80:83], v[128:131], a[232:235], v[80:83]\n"
    "ds_read_b64_tr_b16 v[222:223], v231 offset:61440\n"
    "v_mfma_f32_16x16x32_bf16 v[84:87], v[132:135], a[232:235], v[84:87]\n"
    "ds_read_b64_tr_b16 v[128:129], v232 offset:57344\n"
    "v_mfma_f32_16x16x32_bf16 v[52:55], v[132:135], a[228:231], v[52:55]\n"
    "s_waitcnt lgkmcnt(9)\n"
    "ds_read_b64_tr_b16 v[130:131], v232 offset:61440\n"
    "v_mfma_f32_16x16x32_bf16 v[56:59], v[136:139], a[228:231], v[56:59]\n"
    "ds_read_b64_tr_b16 v[132:133], v233 offset:57344\n"
    "buffer_load_dwordx4 a[204:207], v195, s[44:47], s76 offen sc1\n"
    "v_mfma_f32_16x16x32_bf16 v[88:91], v[136:139], a[232:235], v[88:91]\n"
    "ds_read_b64_tr_b16 v[134:135], v233 offset:61440\n"
    "v_mfma_f32_16x16x32_bf16 v[92:95], v[140:143], a[232:235], v[92:95]\n"
    "ds_read_b64_tr_b16 v[136:137], v234 offset:57344\n"
    "v_mfma_f32_16x16x32_bf16 v[60:63], v[140:143], a[228:231], v[60:63]\n"
    "s_waitcnt lgkmcnt(9)\n"
    "s_waitcnt vmcnt(12)\n"
    "v_mfma_f32_16x16x32_bf16 v[32:35], v[208:211], a[244:247], v[32:35]\n"
    "s_barrier\n"
    "ds_read_b64_tr_b16 v[138:139], v234 offset:61440\n"
    "v_mfma_f32_16x16x32_bf16 v[64:67], v[208:211], a[248:251], v[64:67]\n"
    "ds_read_b64_tr_b16 v[140:141], v235 offset:57344\n"
    "ds_read_b64_tr_b16 v[142:143], v235 offset:61440\n"
    "v_mfma_f32_16x16x32_bf16 v[68:71], v[212:215], a[248:251], v[68:71]\n"
    "ds_read_b64_tr_b16 v[208:209], v224\n"
    "v_mfma_f32_16x16x32_bf16 v[36:39], v[212:215], a[244:247], v[36:39]\n"
    "s_waitcnt lgkmcnt(9)\n"
    "ds_read_b64_tr_b16 v[210:211], v224 offset:4096\n"
    "v_mfma_f32_16x16x32_bf16 v[40:43], v[216:219], a[244:247], v[40:43]\n"
    "ds_read_b64_tr_b16 v[212:213], v225\n"
    "buffer_load_dwordx4 a[208:211], v196, s[44:47], s76 offen sc1\n"
    "v_mfma_f32_16x16x32_bf16 v[72:75], v[216:219], a[248:251], v[72:75]\n"
    "ds_read_b64_tr_b16 v[214:215], v225 offset:4096\n"
    "v_mfma_f32_16x16x32_bf16 v[76:79], v[220:223], a[248:251], v[76:79]\n"
    "ds_read_b64_tr_b16 v[216:217], v224 offset:8192\n"
    "v_mfma_f32_16x16x32_bf16 v[44:47], v[220:223], a[244:247], v[44:47]\n"
    "s_waitcnt lgkmcnt(9)\n"
    "ds_read_b64_tr_b16 v[218:219], v224 offset:12288\n"
    "v_mfma_f32_16x16x32_bf16 v[48:51], v[128:131], a[244:247], v[48:51]\n"
    "ds_read_b64_tr_b16 v[220:221], v225 offset:8192\n"
    "v_mfma_f32_16x16x32_bf16 v[80:83], v[128:131], a[248:251], v[80:83]\n"
    "ds_read_b64_tr_b16 v[222:223], v225 offset:12288\n"
    "v_mfma_f32_16x16x32_bf16 v[84:87], v[132:135], a[248:251], v[84:87]\n"
    "ds_read_b64_tr_b16 v[128:129], v224 offset:16384\n"
    "v_mfma_f32_16x16x32_bf16 v[52:55], v[132:135], a[244:247], v[52:55]\n"
    "s_waitcnt lgkmcnt(9)\n"
    "ds_read_b64_tr_b16 v[130:131], v224 offset:20480\n"
    "v_mfma_f32_16x16x32_bf16 v[56:59], v[136:139], a[244:247], v[56:59]\n"
    "ds_read_b64_tr_b16 v[132:133], v225 offset:16384\n"
    "buffer_load_dwordx4 a[220:223], v199, s[44:47], s76 offen sc1\n"
    "v_mfma_f32_16x16x32_bf16 v[88:91], v[136:139], a[248:251], v[88:91]\n"
    "ds_read_b64_tr_b16 v[134:135], v225 offset:20480\n"
    "v_mfma_f32_16x16x32_bf16 v[92:95], v[140:143], a[248:251], v[92:95]\n"
    "ds_read_b64_tr_b16 v[136:137], v224 offset:24576\n"
    "v_mfma_f32_16x16x32_bf16 v[60:63], v[140:143], a[244:247], v[60:63]\n"
    "s_waitcnt lgkmcnt(9)\n"
    "ds_read_b64_tr_b16 v[138:139], v224 offset:28672\n"
    "v_mfma_f32_16x16x32_bf16 v[32:35], v[208:211], a[4:7], v[32:35]\n"
    "ds_read_b64_tr_b16 v[140:141], v225 offset:24576\n"
    "s_add_u32 s89, s51, 1\n"
    "v_mfma_f32_16x16x32_bf16 v[96:99], v[208:211], a[12:15], v[96:99]\n"
    "ds_read_b64_tr_b16 v[142:143], v225 offset:28672\n"
    "s_and_b32 s89, s89, 3\n"
    "v_mfma_f32_16x16x32_bf16 v[100:103], v[212:215], a[12:15], v[100:103]\n"
    "ds_read_b64_tr_b16 v[208:209], v226\n"
    "s_lshl_b32 s89, s89, 16\n"
    "v_mfma_f32_16x16x32_bf16 v[36:39], v[212:215], a[4:7], v[36:39]\n"
    "s_waitcnt lgkmcnt(9)\n"
    "ds_read_b64_tr_b16 v[210:211], v226 offset:4096\n"
    "v_mfma_f32_16x16x32_bf16 v[32:35], v[216:219], a[20:23], v[32:35]\n"
    "ds_read_b64_tr_b16 v[212:213], v227\n"
    "buffer_load_dwordx4 a[224:227], v192, s[44:47], s77 offen sc1\n"
    "v_mfma_f32_16x16x32_bf16 v[96:99], v[216:219], a[28:31], v[96:99]\n"
    "ds_read_b64_tr_b16 v[214:215], v227 offset:4096\n"
    "s_add_u32 s89, s89, s14\n"
    "v_mfma_f32_16x16x32_bf16 v[100:103], v[220:223], a[28:31], v[100:103]\n"
    "ds_read_b64_tr_b16 v[216:217], v226 offset:8192\n"
    "s_add_u32 s89, s89, 0x4000\n"
    "v_mfma_f32_16x16x32_bf16 v[36:39], v[220:223], a[20:23], v[36:39]\n"
    "s_waitcnt lgkmcnt(9)\n"
    "ds_read_b64_tr_b16 v[218:219], v226 offset:12288\n"
    "v_mfma_f32_16x16x32_bf16 v[32:35], v[128:131], a[36:39], v[32:35]\n"
    "ds_read_b64_tr_b16 v[220:221], v227 offset:8192\n"
    "v_mfma_f32_16x16x32_bf16 v[96:99], v[128:131], a[44:47], v[96:99]\n"
    "ds_read_b64_tr_b16 v[222:223], v227 offset:12288\n"
    "v_mfma_f32_16x16x32_bf16 v[100:103], v[132:135], a[44:47], v[100:103]\n"
    "ds_read_b64_tr_b16 v[128:129], v226 offset:16384\n"
    "v_mfma_f32_16x16x32_bf16 v[36:39], v[132:135], a[36:39], v[36:39]\n"
    "s_waitcnt lgkmcnt(9)\n"
    "ds_read_b64_tr_b16 v[130:131], v226 offset:20480\n"
    "v_mfma_f32_16x16x32_bf16 v[32:35], v[136:139], a[52:55], v[32:35]\n"
    "ds_read_b64_tr_b16 v[132:133], v227 offset:16384\n"
    "buffer_load_dwordx4 a[236:239], v195, s[44:47], s77 offen sc1\n"
    "v_mfma_f32_16x16x32_bf16 v[96:99], v[136:139], a[60:63], v[96:99]\n"
    "ds_read_b64_tr_b16 v[134:135], v227 offset:20480\n"
    "v_mfma_f32_16x16x32_bf16 v[100:103], v[140:143], a[60:63], v[100:103]\n"
    "ds_read_b64_tr_b16 v[136:137], v226 offset:24576\n"
    "v_mfma_f32_16x16x32_bf16 v[36:39], v[140:143], a[52:55], v[36:39]\n"
    "s_waitcnt lgkmcnt(9)\n"
    "ds_read_b64_tr_b16 v[138:139], v226 offset:28672\n"
    "v_mfma_f32_16x16x32_bf16 v[40:43], v[208:211], a[4:7], v[40:43]\n"
    "ds_read_b64_tr_b16 v[140:141], v227 offset:24576\n"
    "v_mfma_f32_16x16x32_bf16 v[104:107], v[208:211], a[12:15], v[104:107]\n"
    "ds_read_b64_tr_b16 v[142:143], v227 offset:28672\n"
    "v_mfma_f32_16x16x32_bf16 v[108:111], v[212:215], a[12:15], v[108:111]\n"
    "ds_read_b64_tr_b16 v[208:209], v204\n"
    "v_cvt_pk_bf16_f32 v144, v32, v33\n"
    "v_mfma_f32_16x16x32_bf16 v[44:47], v[212:215], a[4:7], v[44:47]\n"
    "s_waitcnt lgkmcnt(9)\n"
    "ds_read_b64_tr_b16 v[210:211], v204 offset:4096\n"
    "v_mfma_f32_16x16x32_bf16 v[40:43], v[216:219], a[20:23], v[40:43]\n"
    "ds_read_b64_tr_b16 v[212:213], v241\n"
    "buffer_load_dwordx4 a[240:243], v196, s[44:47], s77 offen sc1\n"
    "v_mfma_f32_16x16x32_bf16 v[104:107], v[216:219], a[28:31], v[104:107]\n"
    "ds_read_b64_tr_b16 v[214:215], v241 offset:4096\n"
    "v_cvt_pk_bf16_f32 v145, v34, v35\n"
    "v_mfma_f32_16x16x32_bf16 v[108:111], v[220:223], a[28:31], v[108:111]\n"
    "ds_read_b64_tr_b16 v[216:217], v204 offset:8192\n"
    "v_cvt_pk_bf16_f32 v146, v36, v37\n"
    "v_mfma_f32_16x16x32_bf16 v[44:47], v[220:223], a[20:23], v[44:47]\n"
    "s_waitcnt lgkmcnt(9)\n"
    "ds_read_b64_tr_b16 v[218:219], v204 offset:12288\n"
    "v_mfma_f32_16x16x32_bf16 v[40:43], v[128:131], a[36:39], v[40:43]\n"
    "ds_read_b64_tr_b16 v[220:221], v241 offset:8192\n"
    "v_cvt_pk_bf16_f32 v147, v38, v39\n"
    "v_mfma_f32_16x16x32_bf16 v[104:107], v[128:131], a[44:47], v[104:107]\n"
    "ds_read_b64_tr_b16 v[222:223], v241 offset:12288\n"
    "buffer_store_dwordx4 v[144:147], v240, s[44:47], s89 offen sc1\n"
    "v_mfma_f32_16x16x32_bf16 v[108:111], v[132:135], a[44:47], v[108:111]\n"
    "ds_read_b64_tr_b16 v[128:129], v204 offset:16384\n"
    "v_mfma_f32_16x16x32_bf16 v[44:47], v[132:135], a[36:39], v[44:47]\n"
    "s_waitcnt lgkmcnt(9)\n"
    "ds_read_b64_tr_b16 v[130:131], v204 offset:20480\n"
    "v_mfma_f32_16x16x32_bf16 v[40:43], v[136:139], a[52:55], v[40:43]\n"
    "ds_read_b64_tr_b16 v[132:133], v241 offset:16384\n"
    "buffer_load_dwordx4 a[252:255], v199, s[44:47], s77 offen sc1\n"
    "v_mfma_f32_16x16x32_bf16 v[104:107], v[136:139], a[60:63], v[104:107]\n"
    "ds_read_b64_tr_b16 v[134:135], v241 offset:20480\n"
    "v_mfma_f32_16x16x32_bf16 v[108:111], v[140:143], a[60:63], v[108:111]\n"
    "ds_read_b64_tr_b16 v[136:137], v204 offset:24576\n"
    "v_mfma_f32_16x16x32_bf16 v[44:47], v[140:143], a[52:55], v[44:47]\n"
    "s_waitcnt lgkmcnt(9)\n"
    "ds_read_b64_tr_b16 v[138:139], v204 offset:28672\n"
    "v_mfma_f32_16x16x32_bf16 v[48:51], v[208:211], a[4:7], v[48:51]\n"
    "ds_read_b64_tr_b16 v[140:141], v241 offset:24576\n"
    "v_mfma_f32_16x16x32_bf16 v[112:115], v[208:211], a[12:15], v[112:115]\n"
    "ds_read_b64_tr_b16 v[142:143], v241 offset:28672\n"
    "v_mfma_f32_16x16x32_bf16 v[116:119], v[212:215], a[12:15], v[116:119]\n"
    "ds_read_b64_tr_b16 v[208:209], v252\n"
    "v_cvt_pk_bf16_f32 v148, v40, v41\n"
    "v_mfma_f32_16x16x32_bf16 v[52:55], v[212:215], a[4:7], v[52:55]\n"
    "s_waitcnt lgkmcnt(9)\n"
    "ds_read_b64_tr_b16 v[210:211], v252 offset:4096\n"
    "v_mfma_f32_16x16x32_bf16 v[48:51], v[216:219], a[20:23], v[48:51]\n"
    "ds_read_b64_tr_b16 v[212:213], v253\n"
    "v_cvt_pk_bf16_f32 v149, v42, v43\n"
    "v_mfma_f32_16x16x32_bf16 v[112:115], v[216:219], a[28:31], v[112:115]\n"
    "ds_read_b64_tr_b16 v[214:215], v253 offset:4096\n"
    "v_cvt_pk_bf16_f32 v150, v44, v45\n"
    "v_mfma_f32_16x16x32_bf16 v[116:119], v[220:223], a[28:31], v[116:119]\n"
    "ds_read_b64_tr_b16 v[216:217], v252 offset:8192\n"
    "v_cvt_pk_bf16_f32 v151, v46, v47\n"
    "v_mfma_f32_16x16x32_bf16 v[52:55], v[220:223], a[20:23], v[52:55]\n"
    "s_waitcnt lgkmcnt(9)\n"
    "ds_read_b64_tr_b16 v[218:219], v252 offset:12288\n"
    "v_mfma_f32_16x16x32_bf16 v[48:51], v[128:131], a[36:39], v[48:51]\n"
    "ds_read_b64_tr_b16 v[220:221], v253 offset:8192\n"
    "buffer_store_dwordx4 v[148:151], v240, s[44:47], s89 offen offset:1024 sc1\n"
    "v_mfma_f32_16x16x32_bf16 v[112:115], v[128:131], a[44:47], v[112:115]\n"
    "ds_read_b64_tr_b16 v[222:223], v253 offset:12288\n"
    "v_mfma_f32_16x16x32_bf16 v[116:119], v[132:135], a[44:47], v[116:119]\n"
    "ds_read_b64_tr_b16 v[128:129], v252 offset:16384\n"
    "v_mfma_f32_16x16x32_bf16 v[52:55], v[132:135], a[36:39], v[52:55]\n"
    "s_waitcnt lgkmcnt(9)\n"
    "ds_read_b64_tr_b16 v[130:131], v252 offset:20480\n"
    "v_mfma_f32_16x16x32_bf16 v[48:51], v[136:139], a[52:55], v[48:51]\n"
    "ds_read_b64_tr_b16 v[132:133], v253 offset:16384\n"
    "v_mfma_f32_16x16x32_bf16 v[112:115], v[136:139], a[60:63], v[112:115]\n"
    "ds_read_b64_tr_b16 v[134:135], v253 offset:20480\n"
    "v_mfma_f32_16x16x32_bf16 v[116:119], v[140:143], a[60:63], v[116:119]\n"
    "ds_read_b64_tr_b16 v[136:137], v252 offset:24576\n"
    "v_mfma_f32_16x16x32_bf16 v[52:55], v[140:143], a[52:55], v[52:55]\n"
    "s_waitcnt lgkmcnt(9)\n"
    "ds_read_b64_tr_b16 v[138:139], v252 offset:28672\n"
    "v_mfma_f32_16x16x32_bf16 v[56:59], v[208:211], a[4:7], v[56:59]\n"
    "ds_read_b64_tr_b16 v[140:141], v253 offset:24576\n"
    "v_mfma_f32_16x16x32_bf16 v[120:123], v[208:211], a[12:15], v[120:123]\n"
    "ds_read_b64_tr_b16 v[142:143], v253 offset:28672\n"
    "v_mfma_f32_16x16x32_bf16 v[124:127], v[212:215], a[12:15], v[124:127]\n"
    "ds_read_b64_tr_b16 v[208:209], v224\n"
    "v_cvt_pk_bf16_f32 v152, v48, v49\n"
    "v_mfma_f32_16x16x32_bf16 v[60:63], v[212:215], a[4:7], v[60:63]\n"
    "s_waitcnt lgkmcnt(9)\n"
    "ds_read_b64_tr_b16 v[210:211], v224 offset:4096\n"
    "v_mfma_f32_16x16x32_bf16 v[56:59], v[216:219], a[20:23], v[56:59]\n"
    "ds_read_b64_tr_b16 v[212:213], v225\n"
    "v_cvt_pk_bf16_f32 v153, v50, v51\n"
    "v_mfma_f32_16x16x32_bf16 v[120:123], v[216:219], a[28:31], v[120:123]\n"
    "ds_read_b64_tr_b16 v[214:215], v225 offset:4096\n"
    "v_cvt_pk_bf16_f32 v154, v52, v53\n"
    "v_mfma_f32_16x16x32_bf16 v[124:127], v[220:223], a[28:31], v[124:127]\n"
    "ds_read_b64_tr_b16 v[216:217], v224 offset:8192\n"
    "v_cvt_pk_bf16_f32 v155, v54, v55\n"
    "v_mfma_f32_16x16x32_bf16 v[60:63], v[220:223], a[20:23], v[60:63]\n"
    "s_waitcnt lgkmcnt(9)\n"
    "ds_read_b64_tr_b16 v[218:219], v224 offset:12288\n"
    "v_mfma_f32_16x16x32_bf16 v[56:59], v[128:131], a[36:39], v[56:59]\n"
    "ds_read_b64_tr_b16 v[220:221], v225 offset:8192\n"
    "buffer_store_dwordx4 v[152:155], v240, s[44:47], s89 offen offset:2048 sc1\n"
    "v_mfma_f32_16x16x32_bf16 v[120:123], v[128:131], a[44:47], v[120:123]\n"
    "ds_read_b64_tr_b16 v[222:223], v225 offset:12288\n"
    "v_mfma_f32_16x16x32_bf16 v[124:127], v[132:135], a[44:47], v[124:127]\n"
    "ds_read_b64_tr_b16 v[128:129], v224 offset:16384\n"
    "v_mfma_f32_16x16x32_bf16 v[60:63], v[132:135], a[36:39], v[60:63]\n"
    "s_waitcnt lgkmcnt(9)\n"
    "ds_read_b64_tr_b16 v[130:131], v224 offset:20480\n"
    "v_mfma_f32_16x16x32_bf16 v[56:59], v[136:139], a[52:55], v[56:59]\n"
    "ds_read_b64_tr_b16 v[132:133], v225 offset:16384\n"
    "v_mfma_f32_16x16x32_bf16 v[120:123], v[136:139], a[60:63], v[120:123]\n"
    "ds_read_b64_tr_b16 v[134:135], v225 offset:20480\n"
    "v_mfma_f32_16x16x32_bf16 v[124:127], v[140:143], a[60:63], v[124:127]\n"
    "ds_read_b64_tr_b16 v[136:137], v224 offset:24576\n"
    "v_mfma_f32_16x16x32_bf16 v[60:63], v[140:143], a[52:55], v[60:63]\n"
    "s_waitcnt lgkmcnt(9)\n"
    "ds_read_b64_tr_b16 v[138:139], v224 offset:28672\n"
    "v_mfma_f32_16x16x32_bf16 v[64:67], v[208:211], a[8:11], v[64:67]\n"
    "ds_read_b64_tr_b16 v[140:141], v225 offset:24576\n"
    "s_add_u32 s90, s51, 2\n"
    "v_mfma_f32_16x16x32_bf16 v[0:3], v[208:211], a[0:3], v[0:3]\n"
    "ds_read_b64_tr_b16 v[142:143], v225 offset:28672\n"
    "s_and_b32 s90, s90, 3\n"
    "v_mfma_f32_16x16x32_bf16 v[4:7], v[212:215], a[0:3], v[4:7]\n"
    "ds_read_b64_tr_b16 v[208:209], v226\n"
    "s_lshl_b32 s90, s90, 16\n"
    "v_mfma_f32_16x16x32_bf16 v[68:71], v[212:215], a[8:11], v[68:71]\n"
    "s_waitcnt lgkmcnt(9)\n"
    "ds_read_b64_tr_b16 v[210:211], v226 offset:4096\n"
    "v_mfma_f32_16x16x32_bf16 v[64:67], v[216:219], a[24:27], v[64:67]\n"
    "ds_read_b64_tr_b16 v[212:213], v227\n"
    "s_add_u32 s90, s90, s14\n"
    "v_mfma_f32_16x16x32_bf16 v[0:3], v[216:219], a[16:19], v[0:3]\n"
    "ds_read_b64_tr_b16 v[214:215], v227 offset:4096\n"
    "s_add_u32 s90, s90, 0x8000\n"
    "v_mfma_f32_16x16x32_bf16 v[4:7], v[220:223], a[16:19], v[4:7]\n"
    "ds_read_b64_tr_b16 v[216:217], v226 offset:8192\n"
    "v_cvt_pk_bf16_f32 v156, v56, v57\n"
    "v_mfma_f32_16x16x32_bf16 v[68:71], v[220:223], a[24:27], v[68:71]\n"
    "s_waitcnt lgkmcnt(9)\n"
    "ds_read_b64_tr_b16 v[218:219], v226 offset:12288\n"
    "v_mfma_f32_16x16x32_bf16 v[64:67], v[128:131], a[40:43], v[64:67]\n"
    "ds_read_b64_tr_b16 v[220:221], v227 offset:8192\n"
    "v_cvt_pk_bf16_f32 v157, v58, v59\n"
    "v_mfma_f32_16x16x32_bf16 v[0:3], v[128:131], a[32:35], v[0:3]\n"
    "ds_read_b64_tr_b16 v[222:223], v227 offset:12288\n"
    "v_cvt_pk_bf16_f32 v158, v60, v61\n"
    "v_mfma_f32_16x16x32_bf16 v[4:7], v[132:135], a[32:35], v[4:7]\n"
    "ds_read_b64_tr_b16 v[128:129], v226 offset:16384\n"
    "v_cvt_pk_bf16_f32 v159, v62, v63\n"
    "v_mfma_f32_16x16x32_bf16 v[68:71], v[132:135], a[40:43], v[68:71]\n"
    "s_waitcnt vmcnt(19) lgkmcnt(9)\n"
    "ds_read_b64_tr_b16 v[130:131], v226 offset:20480\n"
    "v_mfma_f32_16x16x32_bf16 v[64:67], v[136:139], a[56:59], v[64:67]\n"
    "ds_read_b64_tr_b16 v[132:133], v227 offset:16384\n"
    "buffer_store_dwordx4 v[156:159], v240, s[44:47], s89 offen offset:3072 sc1\n"
    "v_mfma_f32_16x16x32_bf16 v[0:3], v[136:139], a[48:51], v[0:3]\n"
    "ds_read_b64_tr_b16 v[134:135], v227 offset:20480\n"
    "v_mfma_f32_16x16x32_bf16 v[4:7], v[140:143], a[48:51], v[4:7]\n"
    "ds_read_b64_tr_b16 v[136:137], v226 offset:24576\n"
    "v_mfma_f32_16x16x32_bf16 v[68:71], v[140:143], a[56:59], v[68:71]\n"
    "s_waitcnt lgkmcnt(9)\n"
    "ds_read_b64_tr_b16 v[138:139], v226 offset:28672\n"
    "v_mfma_f32_16x16x32_bf16 v[72:75], v[208:211], a[8:11], v[72:75]\n"
    "ds_read_b64_tr_b16 v[140:141], v227 offset:24576\n"
    "v_mfma_f32_16x16x32_bf16 v[8:11], v[208:211], a[0:3], v[8:11]\n"
    "ds_read_b64_tr_b16 v[142:143], v227 offset:28672\n"
    "v_mfma_f32_16x16x32_bf16 v[12:15], v[212:215], a[0:3], v[12:15]\n"
    "ds_read_b64_tr_b16 v[208:209], v204\n"
    "v_cvt_pk_bf16_f32 v48, v64, v65\n"
    "v_mfma_f32_16x16x32_bf16 v[76:79], v[212:215], a[8:11], v[76:79]\n"
    "s_waitcnt lgkmcnt(9)\n"
    "ds_read_b64_tr_b16 v[210:211], v204 offset:4096\n"
    "v_mfma_f32_16x16x32_bf16 v[72:75], v[216:219], a[24:27], v[72:75]\n"
    "ds_read_b64_tr_b16 v[212:213], v241\n"
    "v_cvt_pk_bf16_f32 v49, v66, v67\n"
    "v_mfma_f32_16x16x32_bf16 v[8:11], v[216:219], a[16:19], v[8:11]\n"
    "ds_read_b64_tr_b16 v[214:215], v241 offset:4096\n"
    "v_cvt_pk_bf16_f32 v50, v68, v69\n"
    "v_mfma_f32_16x16x32_bf16 v[12:15], v[220:223], a[16:19], v[12:15]\n"
    "ds_read_b64_tr_b16 v[216:217], v204 offset:8192\n"
    "v_cvt_pk_bf16_f32 v51, v70, v71\n"
    "v_mfma_f32_16x16x32_bf16 v[76:79], v[220:223], a[24:27], v[76:79]\n"
    "s_waitcnt lgkmcnt(9)\n"
    "ds_read_b64_tr_b16 v[218:219], v204 offset:12288\n"
    "v_mfma_f32_16x16x32_bf16 v[72:75], v[128:131], a[40:43], v[72:75]\n"
    "ds_read_b64_tr_b16 v[220:221], v241 offset:8192\n"
    "buffer_store_dwordx4 v[48:51], v240, s[44:47], s90 offen sc1\n"
    "v_mfma_f32_16x16x32_bf16 v[8:11], v[128:131], a[32:35], v[8:11]\n"
    "ds_read_b64_tr_b16 v[222:223], v241 offset:12288\n"
    "v_mfma_f32_16x16x32_bf16 v[12:15], v[132:135], a[32:35], v[12:15]\n"
    "ds_read_b64_tr_b16 v[128:129], v204 offset:16384\n"
    "v_mfma_f32_16x16x32_bf16 v[76:79], v[132:135], a[40:43], v[76:79]\n"
    "s_waitcnt lgkmcnt(9)\n"
    "ds_read_b64_tr_b16 v[130:131], v204 offset:20480\n"
    "v_mfma_f32_16x16x32_bf16 v[72:75], v[136:139], a[56:59], v[72:75]\n"
    "ds_read_b64_tr_b16 v[132:133], v241 offset:16384\n"
    "v_mfma_f32_16x16x32_bf16 v[8:11], v[136:139], a[48:51], v[8:11]\n"
    "ds_read_b64_tr_b16 v[134:135], v241 offset:20480\n"
    "v_mfma_f32_16x16x32_bf16 v[12:15], v[140:143], a[48:51], v[12:15]\n"
    "ds_read_b64_tr_b16 v[136:137], v204 offset:24576\n"
    "v_mfma_f32_16x16x32_bf16 v[76:79], v[140:143], a[56:59], v[76:79]\n"
    "s_waitcnt lgkmcnt(9)\n"
    "ds_read_b64_tr_b16 v[138:139], v204 offset:28672\n"
    "v_mfma_f32_16x16x32_bf16 v[80:83], v[208:211], a[8:11], v[80:83]\n"
    "ds_read_b64_tr_b16 v[140:141], v241 offset:24576\n"
    "v_mfma_f32_16x16x32_bf16 v[16:19], v[208:211], a[0:3], v[16:19]\n"
    "ds_read_b64_tr_b16 v[142:143], v241 offset:28672\n"
    "v_mfma_f32_16x16x32_bf16 v[20:23], v[212:215], a[0:3], v[20:23]\n"
    "ds_read_b64_tr_b16 v[208:209], v252\n"
    "v_cvt_pk_bf16_f32 v52, v72, v73\n"
    "v_mfma_f32_16x16x32_bf16 v[84:87], v[212:215], a[8:11], v[84:87]\n"
    "s_waitcnt lgkmcnt(9)\n"
    "ds_read_b64_tr_b16 v[210:211], v252 offset:4096\n"
    "v_mfma_f32_16x16x32_bf16 v[80:83], v[216:219], a[24:27], v[80:83]\n"
    "ds_read_b64_tr_b16 v[212:213], v253\n"
    "v_cvt_pk_bf16_f32 v53, v74, v75\n"
    "v_mfma_f32_16x16x32_bf16 v[16:19], v[216:219], a[16:19], v[16:19]\n"
    "ds_read_b64_tr_b16 v[214:215], v253 offset:4096\n"
    "v_cvt_pk_bf16_f32 v54, v76, v77\n"
    "v_mfma_f32_16x16x32_bf16 v[20:23], v[220:223], a[16:19], v[20:23]\n"
    "ds_read_b64_tr_b16 v[216:217], v252 offset:8192\n"
    "v_cvt_pk_bf16_f32 v55, v78, v79\n"
    "v_mfma_f32_16x16x32_bf16 v[84:87], v[220:223], a[24:27], v[84:87]\n"
    "s_waitcnt lgkmcnt(9)\n"
    "ds_read_b64_tr_b16 v[218:219], v252 offset:12288\n"
    "v_mfma_f32_16x16x32_bf16 v[80:83], v[128:131], a[40:43], v[80:83]\n"
    "ds_read_b64_tr_b16 v[220:221], v253 offset:8192\n"
    "buffer_store_dwordx4 v[52:55], v240, s[44:47], s90 offen offset:1024 sc1\n"
    "v_mfma_f32_16x16x32_bf16 v[16:19], v[128:131], a[32:35], v[16:19]\n"
    "ds_read_b64_tr_b16 v[222:223], v253 offset:12288\n"
    "v_mfma_f32_16x16x32_bf16 v[20:23], v[132:135], a[32:35], v[20:23]\n"
    "ds_read_b64_tr_b16 v[128:129], v252 offset:16384\n"
    "v_mfma_f32_16x16x32_bf16 v[84:87], v[132:135], a[40:43], v[84:87]\n"
    "s_waitcnt lgkmcnt(9)\n"
    "ds_read_b64_tr_b16 v[130:131], v252 offset:20480\n"
    "v_mfma_f32_16x16x32_bf16 v[80:83], v[136:139], a[56:59], v[80:83]\n"
    "ds_read_b64_tr_b16 v[132:133], v253 offset:16384\n"
    "v_mfma_f32_16x16x32_bf16 v[16:19], v[136:139], a[48:51], v[16:19]\n"
    "ds_read_b64_tr_b16 v[134:135], v253 offset:20480\n"
    "v_mfma_f32_16x16x32_bf16 v[20:23], v[140:143], a[48:51], v[20:23]\n"
    "ds_read_b64_tr_b16 v[136:137], v252 offset:24576\n"
    "v_mfma_f32_16x16x32_bf16 v[84:87], v[140:143], a[56:59], v[84:87]\n"
    "s_waitcnt lgkmcnt(9)\n"
    "ds_read_b64_tr_b16 v[138:139], v252 offset:28672\n"
    "v_mfma_f32_16x16x32_bf16 v[88:91], v[208:211], a[8:11], v[88:91]\n"
    "ds_read_b64_tr_b16 v[140:141], v253 offset:24576\n"
    "v_mfma_f32_16x16x32_bf16 v[24:27], v[208:211], a[0:3], v[24:27]\n"
    "ds_read_b64_tr_b16 v[142:143], v253 offset:28672\n"
    "v_mfma_f32_16x16x32_bf16 v[28:31], v[212:215], a[0:3], v[28:31]\n"
    "ds_read_b64_tr_b16 v[208:209], v228\n"
    "v_cvt_pk_bf16_f32 v56, v80, v81\n"
    "v_mfma_f32_16x16x32_bf16 v[92:95], v[212:215], a[8:11], v[92:95]\n"
    "s_waitcnt lgkmcnt(9)\n"
    "ds_read_b64_tr_b16 v[210:211], v228 offset:4096\n"
    "v_mfma_f32_16x16x32_bf16 v[88:91], v[216:219], a[24:27], v[88:91]\n"
    "ds_read_b64_tr_b16 v[212:213], v229\n"
    "v_cvt_pk_bf16_f32 v57, v82, v83\n"
    "v_mfma_f32_16x16x32_bf16 v[24:27], v[216:219], a[16:19], v[24:27]\n"
    "ds_read_b64_tr_b16 v[214:215], v229 offset:4096\n"
    "v_cvt_pk_bf16_f32 v58, v84, v85\n"
    "v_mfma_f32_16x16x32_bf16 v[28:31], v[220:223], a[16:19], v[28:31]\n"
    "ds_read_b64_tr_b16 v[216:217], v228 offset:8192\n"
    "v_cvt_pk_bf16_f32 v59, v86, v87\n"
    "v_mfma_f32_16x16x32_bf16 v[92:95], v[220:223], a[24:27], v[92:95]\n"
    "s_waitcnt lgkmcnt(9)\n"
    "ds_read_b64_tr_b16 v[218:219], v228 offset:12288\n"
    "v_mfma_f32_16x16x32_bf16 v[88:91], v[128:131], a[40:43], v[88:91]\n"
    "ds_read_b64_tr_b16 v[220:221], v229 offset:8192\n"
    "buffer_store_dwordx4 v[56:59], v240, s[44:47], s90 offen offset:2048 sc1\n"
    "v_mfma_f32_16x16x32_bf16 v[24:27], v[128:131], a[32:35], v[24:27]\n"
    "ds_read_b64_tr_b16 v[222:223], v229 offset:12288\n"
    "v_mfma_f32_16x16x32_bf16 v[28:31], v[132:135], a[32:35], v[28:31]\n"
    "ds_read_b64_tr_b16 v[128:129], v228 offset:16384\n"
    "v_mfma_f32_16x16x32_bf16 v[92:95], v[132:135], a[40:43], v[92:95]\n"
    "s_waitcnt lgkmcnt(9)\n"
    "ds_read_b64_tr_b16 v[130:131], v228 offset:20480\n"
    "v_mfma_f32_16x16x32_bf16 v[88:91], v[136:139], a[56:59], v[88:91]\n"
    "ds_read_b64_tr_b16 v[132:133], v229 offset:16384\n"
    "s_waitcnt vmcnt(3)\n"
    "v_mfma_f32_16x16x32_bf16 v[24:27], v[136:139], a[48:51], v[24:27]\n"
    "ds_read_b64_tr_b16 v[134:135], v229 offset:20480\n"
    "v_mov_b32 v250, 0x80000000\n"
    "v_mfma_f32_16x16x32_bf16 v[28:31], v[140:143], a[48:51], v[28:31]\n"
    "ds_read_b64_tr_b16 v[136:137], v228 offset:24576\n"
    "v_cmp_eq_u32 vcc, 1, v251\n"
    "v_mfma_f32_16x16x32_bf16 v[92:95], v[140:143], a[56:59], v[92:95]\n"
    "s_waitcnt lgkmcnt(9)\n"
    "ds_read_b64_tr_b16 v[138:139], v228 offset:28672\n"
    "v_mfma_f32_16x16x32_bf16 v[96:99], v[208:211], a[140:143], v[96:99]\n"
    "ds_read_b64_tr_b16 v[140:141], v229 offset:24576\n"
    "v_cndmask_b32 v250, v250, v205, vcc\n"
    "v_mfma_f32_16x16x32_bf16 v[0:3], v[208:211], a[128:131], v[0:3]\n"
    "ds_read_b64_tr_b16 v[142:143], v229 offset:28672\n"
    "buffer_store_dword v248, v250, s[44:47], 0 offen sc1\n"
    "v_mfma_f32_16x16x32_bf16 v[4:7], v[212:215], a[128:131], v[4:7]\n"
    "ds_read_b64_tr_b16 v[208:209], v228 offset:32768\n"
    "s_add_u32 s93, s51, 3\n"
    "v_mfma_f32_16x16x32_bf16 v[100:103], v[212:215], a[140:143], v[100:103]\n"
    "s_waitcnt lgkmcnt(9)\n"
    "ds_read_b64_tr_b16 v[210:211], v228 offset:36864\n"
    "v_mfma_f32_16x16x32_bf16 v[96:99], v[216:219], a[156:159], v[96:99]\n"
    "ds_read_b64_tr_b16 v[212:213], v229 offset:32768\n"
    "s_and_b32 s93, s93, 3\n"
    "v_mfma_f32_16x16x32_bf16 v[0:3], v[216:219], a[144:147], v[0:3]\n"
    "ds_read_b64_tr_b16 v[214:215], v229 offset:36864\n"
    "s_lshl_b32 s93, s93, 16\n"
    "v_mfma_f32_16x16x32_bf16 v[4:7], v[220:223], a[144:147], v[4:7]\n"
    "ds_read_b64_tr_b16 v[216:217], v228 offset:40960\n"
    "s_add_u32 s93, s93, s14\n"
    "v_mfma_f32_16x16x32_bf16 v[100:103], v[220:223], a[156:159], v[100:103]\n"
    "s_waitcnt lgkmcnt(9)\n"
    "ds_read_b64_tr_b16 v[218:219], v228 offset:45056\n"
    "v_mfma_f32_16x16x32_bf16 v[96:99], v[128:131], a[172:175], v[96:99]\n"
    "ds_read_b64_tr_b16 v[220:221], v229 offset:40960\n"
    "s_add_u32 s93, s93, 0xc000\n"
    "v_mfma_f32_16x16x32_bf16 v[0:3], v[128:131], a[160:163], v[0:3]\n"
    "ds_read_b64_tr_b16 v[222:223], v229 offset:45056\n"
    "v_cvt_pk_bf16_f32 v60, v88, v89\n"
    "v_mfma_f32_16x16x32_bf16 v[4:7], v[132:135], a[160:163], v[4:7]\n"
    "ds_read_b64_tr_b16 v[128:129], v228 offset:49152\n"
    "v_cvt_pk_bf16_f32 v61, v90, v91\n"
    "v_mfma_f32_16x16x32_bf16 v[100:103], v[132:135], a[172:175], v[100:103]\n"
    "s_waitcnt lgkmcnt(9)\n"
    "ds_read_b64_tr_b16 v[130:131], v228 offset:53248\n"
    "v_mfma_f32_16x16x32_bf16 v[96:99], v[136:139], a[188:191], v[96:99]\n"
    "ds_read_b64_tr_b16 v[132:133], v229 offset:49152\n"
    "v_cvt_pk_bf16_f32 v62, v92, v93\n"
    "v_mfma_f32_16x16x32_bf16 v[0:3], v[136:139], a[176:179], v[0:3]\n"
    "ds_read_b64_tr_b16 v[134:135], v229 offset:53248\n"
    "v_cvt_pk_bf16_f32 v63, v94, v95\n"
    "v_mfma_f32_16x16x32_bf16 v[4:7], v[140:143], a[176:179], v[4:7]\n"
    "ds_read_b64_tr_b16 v[136:137], v228 offset:57344\n"
    "buffer_store_dwordx4 v[60:63], v240, s[44:47], s90 offen offset:3072 sc1\n"
    "v_mfma_f32_16x16x32_bf16 v[100:103], v[140:143], a[188:191], v[100:103]\n"
    "s_waitcnt lgkmcnt(9)\n"
    "ds_read_b64_tr_b16 v[138:139], v228 offset:61440\n"
    "v_mfma_f32_16x16x32_bf16 v[96:99], v[208:211], a[204:207], v[96:99]\n"
    "ds_read_b64_tr_b16 v[140:141], v229 offset:57344\n"
    "v_mfma_f32_16x16x32_bf16 v[0:3], v[208:211], a[192:195], v[0:3]\n"
    "ds_read_b64_tr_b16 v[142:143], v229 offset:61440\n"
    "v_mfma_f32_16x16x32_bf16 v[4:7], v[212:215], a[192:195], v[4:7]\n"
    "ds_read_b64_tr_b16 v[208:209], v230\n"
    "v_mfma_f32_16x16x32_bf16 v[100:103], v[212:215], a[204:207], v[100:103]\n"
    "s_waitcnt lgkmcnt(9)\n"
    "ds_read_b64_tr_b16 v[210:211], v230 offset:4096\n"
    "v_mfma_f32_16x16x32_bf16 v[96:99], v[216:219], a[220:223], v[96:99]\n"
    "ds_read_b64_tr_b16 v[212:213], v231\n"
    "v_mfma_f32_16x16x32_bf16 v[0:3], v[216:219], a[208:211], v[0:3]\n"
    "ds_read_b64_tr_b16 v[214:215], v231 offset:4096\n"
    "v_mfma_f32_16x16x32_bf16 v[4:7], v[220:223], a[208:211], v[4:7]\n"
    "ds_read_b64_tr_b16 v[216:217], v230 offset:8192\n"
    "v_mfma_f32_16x16x32_bf16 v[100:103], v[220:223], a[220:223], v[100:103]\n"
    "s_waitcnt lgkmcnt(9)\n"
    "ds_read_b64_tr_b16 v[218:219], v230 offset:12288\n"
    "v_mfma_f32_16x16x32_bf16 v[96:99], v[128:131], a[236:239], v[96:99]\n"
    "ds_read_b64_tr_b16 v[220:221], v231 offset:8192\n"
    "v_mfma_f32_16x16x32_bf16 v[0:3], v[128:131], a[224:227], v[0:3]\n"
    "ds_read_b64_tr_b16 v[222:223], v231 offset:12288\n"
    "v_mfma_f32_16x16x32_bf16 v[4:7], v[132:135], a[224:227], v[4:7]\n"
    "ds_read_b64_tr_b16 v[128:129], v230 offset:16384\n"
    "v_mfma_f32_16x16x32_bf16 v[100:103], v[132:135], a[236:239], v[100:103]\n"
    "s_waitcnt lgkmcnt(9)\n"
    "ds_read_b64_tr_b16 v[130:131], v230 offset:20480\n"
    "v_mfma_f32_16x16x32_bf16 v[96:99], v[136:139], a[252:255], v[96:99]\n"
    "ds_read_b64_tr_b16 v[132:133], v231 offset:16384\n"
    "v_mfma_f32_16x16x32_bf16 v[0:3], v[136:139], a[240:243], v[0:3]\n"
    "ds_read_b64_tr_b16 v[134:135], v231 offset:20480\n"
    "v_mfma_f32_16x16x32_bf16 v[4:7], v[140:143], a[240:243], v[4:7]\n"
    "ds_read_b64_tr_b16 v[136:137], v230 offset:24576\n"
    "v_mfma_f32_16x16x32_bf16 v[100:103], v[140:143], a[252:255], v[100:103]\n"
    "s_waitcnt lgkmcnt(9)\n"
    "ds_read_b64_tr_b16 v[138:139], v230 offset:28672\n"
    "v_mfma_f32_16x16x32_bf16 v[104:107], v[208:211], a[140:143], v[104:107]\n"
    "ds_read_b64_tr_b16 v[140:141], v231 offset:24576\n"
    "v_mfma_f32_16x16x32_bf16 v[8:11], v[208:211], a[128:131], v[8:11]\n"
    "ds_read_b64_tr_b16 v[142:143], v231 offset:28672\n"
    "v_mfma_f32_16x16x32_bf16 v[12:15], v[212:215], a[128:131], v[12:15]\n"
    "ds_read_b64_tr_b16 v[208:209], v230 offset:32768\n"
    "v_cvt_pk_bf16_f32 v96, v96, v97\n"
    "v_mfma_f32_16x16x32_bf16 v[108:111], v[212:215], a[140:143], v[108:111]\n"
    "s_waitcnt lgkmcnt(9)\n"
    "ds_read_b64_tr_b16 v[210:211], v230 offset:36864\n"
    "v_mfma_f32_16x16x32_bf16 v[104:107], v[216:219], a[156:159], v[104:107]\n"
    "ds_read_b64_tr_b16 v[212:213], v231 offset:32768\n"
    "v_cvt_pk_bf16_f32 v97, v98, v99\n"
    "v_mfma_f32_16x16x32_bf16 v[8:11], v[216:219], a[144:147], v[8:11]\n"
    "ds_read_b64_tr_b16 v[214:215], v231 offset:36864\n"
    "v_cvt_pk_bf16_f32 v98, v100, v101\n"
    "v_mfma_f32_16x16x32_bf16 v[12:15], v[220:223], a[144:147], v[12:15]\n"
    "ds_read_b64_tr_b16 v[216:217], v230 offset:40960\n"
    "v_cvt_pk_bf16_f32 v99, v102, v103\n"
    "v_mfma_f32_16x16x32_bf16 v[108:111], v[220:223], a[156:159], v[108:111]\n"
    "s_waitcnt lgkmcnt(9)\n"
    "ds_read_b64_tr_b16 v[218:219], v230 offset:45056\n"
    "v_mfma_f32_16x16x32_bf16 v[104:107], v[128:131], a[172:175], v[104:107]\n"
    "ds_read_b64_tr_b16 v[220:221], v231 offset:40960\n"
    "buffer_store_dwordx4 v[96:99], v240, s[44:47], s93 offen sc1\n"
    "v_mfma_f32_16x16x32_bf16 v[8:11], v[128:131], a[160:163], v[8:11]\n"
    "ds_read_b64_tr_b16 v[222:223], v231 offset:45056\n"
    "v_mfma_f32_16x16x32_bf16 v[12:15], v[132:135], a[160:163], v[12:15]\n"
    "ds_read_b64_tr_b16 v[128:129], v230 offset:49152\n"
    "v_mfma_f32_16x16x32_bf16 v[108:111], v[132:135], a[172:175], v[108:111]\n"
    "s_waitcnt lgkmcnt(9)\n"
    "ds_read_b64_tr_b16 v[130:131], v230 offset:53248\n"
    "v_mfma_f32_16x16x32_bf16 v[104:107], v[136:139], a[188:191], v[104:107]\n"
    "ds_read_b64_tr_b16 v[132:133], v231 offset:49152\n"
    "v_mfma_f32_16x16x32_bf16 v[8:11], v[136:139], a[176:179], v[8:11]\n"
    "ds_read_b64_tr_b16 v[134:135], v231 offset:53248\n"
    "v_mfma_f32_16x16x32_bf16 v[12:15], v[140:143], a[176:179], v[12:15]\n"
    "ds_read_b64_tr_b16 v[136:137], v230 offset:57344\n"
    "v_mfma_f32_16x16x32_bf16 v[108:111], v[140:143], a[188:191], v[108:111]\n"
    "s_waitcnt lgkmcnt(9)\n"
    "ds_read_b64_tr_b16 v[138:139], v230 offset:61440\n"
    "v_mfma_f32_16x16x32_bf16 v[104:107], v[208:211], a[204:207], v[104:107]\n"
    "ds_read_b64_tr_b16 v[140:141], v231 offset:57344\n"
    "v_mfma_f32_16x16x32_bf16 v[8:11], v[208:211], a[192:195], v[8:11]\n"
    "ds_read_b64_tr_b16 v[142:143], v231 offset:61440\n"
    "v_mfma_f32_16x16x32_bf16 v[12:15], v[212:215], a[192:195], v[12:15]\n"
    "ds_read_b64_tr_b16 v[208:209], v232\n"
    "v_mfma_f32_16x16x32_bf16 v[108:111], v[212:215], a[204:207], v[108:111]\n"
    "s_waitcnt lgkmcnt(9)\n"
    "ds_read_b64_tr_b16 v[210:211], v232 offset:4096\n"
    "v_mfma_f32_16x16x32_bf16 v[104:107], v[216:219], a[220:223], v[104:107]\n"
    "ds_read_b64_tr_b16 v[212:213], v233\n"
    "v_mfma_f32_16x16x32_bf16 v[8:11], v[216:219], a[208:211], v[8:11]\n"
    "ds_read_b64_tr_b16 v[214:215], v233 offset:4096\n"
    "v_mfma_f32_16x16x32_bf16 v[12:15], v[220:223], a[208:211], v[12:15]\n"
    "ds_read_b64_tr_b16 v[216:217], v232 offset:8192\n"
    "v_mfma_f32_16x16x32_bf16 v[108:111], v[220:223], a[220:223], v[108:111]\n"
    "s_waitcnt lgkmcnt(9)\n"
    "ds_read_b64_tr_b16 v[218:219], v232 offset:12288\n"
    "v_mfma_f32_16x16x32_bf16 v[104:107], v[128:131], a[236:239], v[104:107]\n"
    "ds_read_b64_tr_b16 v[220:221], v233 offset:8192\n"
    "v_mfma_f32_16x16x32_bf16 v[8:11], v[128:131], a[224:227], v[8:11]\n"
    "ds_read_b64_tr_b16 v[222:223], v233 offset:12288\n"
    "v_mfma_f32_16x16x32_bf16 v[12:15], v[132:135], a[224:227], v[12:15]\n"
    "ds_read_b64_tr_b16 v[128:129], v232 offset:16384\n"
    "v_mfma_f32_16x16x32_bf16 v[108:111], v[132:135], a[236:239], v[108:111]\n"
    "s_waitcnt lgkmcnt(9)\n"
    "ds_read_b64_tr_b16 v[130:131], v232 offset:20480\n"
    "v_mfma_f32_16x16x32_bf16 v[104:107], v[136:139], a[252:255], v[104:107]\n"
    "ds_read_b64_tr_b16 v[132:133], v233 offset:16384\n"
    "s_waitcnt vmcnt(1)\n"
    "v_mfma_f32_16x16x32_bf16 v[8:11], v[136:139], a[240:243], v[8:11]\n"
    "ds_read_b64_tr_b16 v[134:135], v233 offset:20480\n"
    "v_mov_b32 v250, 0x80000000\n"
    "v_mfma_f32_16x16x32_bf16 v[12:15], v[140:143], a[240:243], v[12:15]\n"
    "ds_read_b64_tr_b16 v[136:137], v232 offset:24576\n"
    "v_cmp_eq_u32 vcc, 2, v251\n"
    "v_mfma_f32_16x16x32_bf16 v[108:111], v[140:143], a[252:255], v[108:111]\n"
    "s_waitcnt lgkmcnt(9)\n"
    "ds_read_b64_tr_b16 v[138:139], v232 offset:28672\n"
    "v_mfma_f32_16x16x32_bf16 v[112:115], v[208:211], a[140:143], v[112:115]\n"
    "ds_read_b64_tr_b16 v[140:141], v233 offset:24576\n"
    "v_cndmask_b32 v250, v250, v205, vcc\n"
    "v_mfma_f32_16x16x32_bf16 v[16:19], v[208:211], a[128:131], v[16:19]\n"
    "ds_read_b64_tr_b16 v[142:143], v233 offset:28672\n"
    "buffer_store_dword v248, v250, s[44:47], 0 offen sc1\n"
    "v_mfma_f32_16x16x32_bf16 v[20:23], v[212:215], a[128:131], v[20:23]\n"
    "ds_read_b64_tr_b16 v[208:209], v232 offset:32768\n"
    "v_cvt_pk_bf16_f32 v100, v104, v105\n"
    "v_mfma_f32_16x16x32_bf16 v[116:119], v[212:215], a[140:143], v[116:119]\n"
    "s_waitcnt lgkmcnt(9)\n"
    "ds_read_b64_tr_b16 v[210:211], v232 offset:36864\n"
    "v_mfma_f32_16x16x32_bf16 v[112:115], v[216:219], a[156:159], v[112:115]\n"
    "ds_read_b64_tr_b16 v[212:213], v233 offset:32768\n"
    "v_cvt_pk_bf16_f32 v101, v106, v107\n"
    "v_mfma_f32_16x16x32_bf16 v[16:19], v[216:219], a[144:147], v[16:19]\n"
    "ds_read_b64_tr_b16 v[214:215], v233 offset:36864\n"
    "v_cvt_pk_bf16_f32 v102, v108, v109\n"
    "v_mfma_f32_16x16x32_bf16 v[20:23], v[220:223], a[144:147], v[20:23]\n"
    "ds_read_b64_tr_b16 v[216:217], v232 offset:40960\n"
    "v_cvt_pk_bf16_f32 v103, v110, v111\n"
    "v_mfma_f32_16x16x32_bf16 v[116:119], v[220:223], a[156:159], v[116:119]\n"
    "s_waitcnt lgkmcnt(9)\n"
    "ds_read_b64_tr_b16 v[218:219], v232 offset:45056\n"
    "v_mfma_f32_16x16x32_bf16 v[112:115], v[128:131], a[172:175], v[112:115]\n"
    "ds_read_b64_tr_b16 v[220:221], v233 offset:40960\n"
    "buffer_store_dwordx4 v[100:103], v240, s[44:47], s93 offen offset:1024 sc1\n"
    "v_mfma_f32_16x16x32_bf16 v[16:19], v[128:131], a[160:163], v[16:19]\n"
    "ds_read_b64_tr_b16 v[222:223], v233 offset:45056\n"
    "v_mfma_f32_16x16x32_bf16 v[20:23], v[132:135], a[160:163], v[20:23]\n"
    "ds_read_b64_tr_b16 v[128:129], v232 offset:49152\n"
    "v_mfma_f32_16x16x32_bf16 v[116:119], v[132:135], a[172:175], v[116:119]\n"
    "s_waitcnt lgkmcnt(9)\n"
    "ds_read_b64_tr_b16 v[130:131], v232 offset:53248\n"
    "v_mfma_f32_16x16x32_bf16 v[112:115], v[136:139], a[188:191], v[112:115]\n"
    "ds_read_b64_tr_b16 v[132:133], v233 offset:49152\n"
    "v_mfma_f32_16x16x32_bf16 v[16:19], v[136:139], a[176:179], v[16:19]\n"
    "ds_read_b64_tr_b16 v[134:135], v233 offset:53248\n"
    "v_mfma_f32_16x16x32_bf16 v[20:23], v[140:143], a[176:179], v[20:23]\n"
    "ds_read_b64_tr_b16 v[136:137], v232 offset:57344\n"
    "v_mfma_f32_16x16x32_bf16 v[116:119], v[140:143], a[188:191], v[116:119]\n"
    "s_waitcnt lgkmcnt(9)\n"
    "ds_read_b64_tr_b16 v[138:139], v232 offset:61440\n"
    "v_mfma_f32_16x16x32_bf16 v[112:115], v[208:211], a[204:207], v[112:115]\n"
    "ds_read_b64_tr_b16 v[140:141], v233 offset:57344\n"
    "v_mfma_f32_16x16x32_bf16 v[16:19], v[208:211], a[192:195], v[16:19]\n"
    "ds_read_b64_tr_b16 v[142:143], v233 offset:61440\n"
    "v_mfma_f32_16x16x32_bf16 v[20:23], v[212:215], a[192:195], v[20:23]\n"
    "ds_read_b64_tr_b16 v[208:209], v234\n"
    "v_mfma_f32_16x16x32_bf16 v[116:119], v[212:215], a[204:207], v[116:119]\n"
    "s_waitcnt lgkmcnt(9)\n"
    "ds_read_b64_tr_b16 v[210:211], v234 offset:4096\n"
    "v_mfma_f32_16x16x32_bf16 v[112:115], v[216:219], a[220:223], v[112:115]\n"
    "ds_read_b64_tr_b16 v[212:213], v235\n"
    "v_mfma_f32_16x16x32_bf16 v[16:19], v[216:219], a[208:211], v[16:19]\n"
    "ds_read_b64_tr_b16 v[214:215], v235 offset:4096\n"
    "v_mfma_f32_16x16x32_bf16 v[20:23], v[220:223], a[208:211], v[20:23]\n"
    "ds_read_b64_tr_b16 v[216:217], v234 offset:8192\n"
    "v_mfma_f32_16x16x32_bf16 v[116:119], v[220:223], a[220:223], v[116:119]\n"
    "s_waitcnt lgkmcnt(9)\n"
    "ds_read_b64_tr_b16 v[218:219], v234 offset:12288\n"
    "v_mfma_f32_16x16x32_bf16 v[112:115], v[128:131], a[236:239], v[112:115]\n"
    "ds_read_b64_tr_b16 v[220:221], v235 offset:8192\n"
    "buffer_load_dwordx4 v[160:163], v243, s[44:47], 0 offen sc1\n"
    "v_mfma_f32_16x16x32_bf16 v[16:19], v[128:131], a[224:227], v[16:19]\n"
    "ds_read_b64_tr_b16 v[222:223], v235 offset:12288\n"
    "v_mfma_f32_16x16x32_bf16 v[20:23], v[132:135], a[224:227], v[20:23]\n"
    "ds_read_b64_tr_b16 v[128:129], v234 offset:16384\n"
    "v_mfma_f32_16x16x32_bf16 v[116:119], v[132:135], a[236:239], v[116:119]\n"
    "s_waitcnt lgkmcnt(9)\n"
    "ds_read_b64_tr_b16 v[130:131], v234 offset:20480\n"
    "v_mfma_f32_16x16x32_bf16 v[112:115], v[136:139], a[252:255], v[112:115]\n"
    "ds_read_b64_tr_b16 v[132:133], v235 offset:16384\n"
    "v_mfma_f32_16x16x32_bf16 v[16:19], v[136:139], a[240:243], v[16:19]\n"
    "ds_read_b64_tr_b16 v[134:135], v235 offset:20480\n"
    "v_mfma_f32_16x16x32_bf16 v[20:23], v[140:143], a[240:243], v[20:23]\n"
    "ds_read_b64_tr_b16 v[136:137], v234 offset:24576\n"
    "v_mfma_f32_16x16x32_bf16 v[116:119], v[140:143], a[252:255], v[116:119]\n"
    "s_waitcnt lgkmcnt(9)\n"
    "ds_read_b64_tr_b16 v[138:139], v234 offset:28672\n"
    "v_mfma_f32_16x16x32_bf16 v[120:123], v[208:211], a[140:143], v[120:123]\n"
    "ds_read_b64_tr_b16 v[140:141], v235 offset:24576\n"
    "v_mfma_f32_16x16x32_bf16 v[24:27], v[208:211], a[128:131], v[24:27]\n"
    "ds_read_b64_tr_b16 v[142:143], v235 offset:28672\n"
    "v_mfma_f32_16x16x32_bf16 v[28:31], v[212:215], a[128:131], v[28:31]\n"
    "ds_read_b64_tr_b16 v[208:209], v234 offset:32768\n"
    "v_cvt_pk_bf16_f32 v104, v112, v113\n"
    "v_mfma_f32_16x16x32_bf16 v[124:127], v[212:215], a[140:143], v[124:127]\n"
    "s_waitcnt lgkmcnt(9)\n"
    "ds_read_b64_tr_b16 v[210:211], v234 offset:36864\n"
    "v_mfma_f32_16x16x32_bf16 v[120:123], v[216:219], a[156:159], v[120:123]\n"
    "ds_read_b64_tr_b16 v[212:213], v235 offset:32768\n"
    "v_cvt_pk_bf16_f32 v105, v114, v115\n"
    "v_mfma_f32_16x16x32_bf16 v[24:27], v[216:219], a[144:147], v[24:27]\n"
    "ds_read_b64_tr_b16 v[214:215], v235 offset:36864\n"
    "v_cvt_pk_bf16_f32 v106, v116, v117\n"
    "v_mfma_f32_16x16x32_bf16 v[28:31], v[220:223], a[144:147], v[28:31]\n"
    "ds_read_b64_tr_b16 v[216:217], v234 offset:40960\n"
    "v_cvt_pk_bf16_f32 v107, v118, v119\n"
    "v_mfma_f32_16x16x32_bf16 v[124:127], v[220:223], a[156:159], v[124:127]\n"
    "s_waitcnt lgkmcnt(9)\n"
    "ds_read_b64_tr_b16 v[218:219], v234 offset:45056\n"
    "v_mfma_f32_16x16x32_bf16 v[120:123], v[128:131], a[172:175], v[120:123]\n"
    "ds_read_b64_tr_b16 v[220:221], v235 offset:40960\n"
    "v_min_u32 v104, -2, v104\n"
    "v_mfma_f32_16x16x32_bf16 v[24:27], v[128:131], a[160:163], v[24:27]\n"
    "ds_read_b64_tr_b16 v[222:223], v235 offset:45056\n"
    "v_min_u32 v105, -2, v105\n"
    "v_mfma_f32_16x16x32_bf16 v[28:31], v[132:135], a[160:163], v[28:31]\n"
    "ds_read_b64_tr_b16 v[128:129], v234 offset:49152\n"
    "v_min_u32 v106, -2, v106\n"
    "v_mfma_f32_16x16x32_bf16 v[124:127], v[132:135], a[172:175], v[124:127]\n"
    "s_waitcnt lgkmcnt(9)\n"
    "ds_read_b64_tr_b16 v[130:131], v234 offset:53248\n"
    "v_mfma_f32_16x16x32_bf16 v[120:123], v[136:139], a[188:191], v[120:123]\n"
    "ds_read_b64_tr_b16 v[132:133], v235 offset:49152\n"
    "v_min_u32 v107, -2, v107\n"
    "v_mfma_f32_16x16x32_bf16 v[24:27], v[136:139], a[176:179], v[24:27]\n"
    "ds_read_b64_tr_b16 v[134:135], v235 offset:53248\n"
    "buffer_store_dwordx4 v[104:107], v240, s[44:47], s93 offen offset:2048 sc1\n"
    "v_mfma_f32_16x16x32_bf16 v[28:31], v[140:143], a[176:179], v[28:31]\n"
    "ds_read_b64_tr_b16 v[136:137], v234 offset:57344\n"
    "v_mfma_f32_16x16x32_bf16 v[124:127], v[140:143], a[188:191], v[124:127]\n"
    "s_waitcnt lgkmcnt(9)\n"
    "ds_read_b64_tr_b16 v[138:139], v234 offset:61440\n"
    "v_mfma_f32_16x16x32_bf16 v[120:123], v[208:211], a[204:207], v[120:123]\n"
    "ds_read_b64_tr_b16 v[140:141], v235 offset:57344\n"
    "v_mfma_f32_16x16x32_bf16 v[24:27], v[208:211], a[192:195], v[24:27]\n"
    "ds_read_b64_tr_b16 v[142:143], v235 offset:61440\n"
    "v_mfma_f32_16x16x32_bf16 v[28:31], v[212:215], a[192:195], v[28:31]\n"
    "v_mfma_f32_16x16x32_bf16 v[124:127], v[212:215], a[204:207], v[124:127]\n"
    "s_waitcnt lgkmcnt(8)\n"
    "v_mfma_f32_16x16x32_bf16 v[120:123], v[216:219], a[220:223], v[120:123]\n"
    "v_mfma_f32_16x16x32_bf16 v[24:27], v[216:219], a[208:211], v[24:27]\n"
    "v_mfma_f32_16x16x32_bf16 v[28:31], v[220:223], a[208:211], v[28:31]\n"
    "v_mfma_f32_16x16x32_bf16 v[124:127], v[220:223], a[220:223], v[124:127]\n"
    "s_waitcnt lgkmcnt(4)\n"
    "v_mfma_f32_16x16x32_bf16 v[120:123], v[128:131], a[236:239], v[120:123]\n"
    "v_mfma_f32_16x16x32_bf16 v[24:27], v[128:131], a[224:227], v[24:27]\n"
    "v_mfma_f32_16x16x32_bf16 v[28:31], v[132:135], a[224:227], v[28:31]\n"
    "v_mfma_f32_16x16x32_bf16 v[124:127], v[132:135], a[236:239], v[124:127]\n"
    "s_waitcnt lgkmcnt(0)\n"
    "v_mfma_f32_16x16x32_bf16 v[120:123], v[136:139], a[252:255], v[120:123]\n"
    "s_waitcnt vmcnt(2)\n"
    "v_mov_b32 v250, 0x80000000\n"
    "v_mfma_f32_16x16x32_bf16 v[24:27], v[136:139], a[240:243], v[24:27]\n"
    "v_cmp_eq_u32 vcc, 3, v251\n"
    "s_nop 1\n"
    "v_cndmask_b32 v250, v250, v205, vcc\n"
    "v_mfma_f32_16x16x32_bf16 v[28:31], v[140:143], a[240:243], v[28:31]\n"
    "buffer_store_dword v248, v250, s[44:47], 0 offen sc1\n"
    "v_mfma_f32_16x16x32_bf16 v[124:127], v[140:143], a[252:255], v[124:127]\n"
    "v_cvt_pk_bf16_f32 v108, v120, v121\n"
    "v_cvt_pk_bf16_f32 v109, v122, v123\n"
    "s_nop 5\n"
    "v_cvt_pk_bf16_f32 v110, v124, v125\n"
    "v_cvt_pk_bf16_f32 v111, v126, v127\n"
    "v_min_u32 v108, -2, v108\n"
    "v_min_u32 v109, -2, v109\n"
    "v_min_u32 v110, -2, v110\n"
    "v_min_u32 v111, -2, v111\n"
    "buffer_store_dwordx4 v[108:111], v240, s[44:47], s93 offen offset:3072 sc1\n"
    "v_dot2_f32_bf16 v0, v176, v236, v0\n"
    "v_dot2_f32_bf16 v1, v176, v237, v1\n"
    "v_dot2_f32_bf16 v2, v177, v236, v2\n"
    "v_dot2_f32_bf16 v3, v177, v237, v3\n"
    "v_dot2_f32_bf16 v4, v178, v236, v4\n"
    "v_dot2_f32_bf16 v5, v178, v237, v5\n"
    "v_dot2_f32_bf16 v6, v179, v236, v6\n"
    "v_dot2_f32_bf16 v7, v179, v237, v7\n"
    "v_dot2_f32_bf16 v8, v180, v236, v8\n"
    "v_dot2_f32_bf16 v9, v180, v237, v9\n"
    "v_dot2_f32_bf16 v10, v181, v236, v10\n"
    "v_dot2_f32_bf16 v11, v181, v237, v11\n"
    "v_dot2_f32_bf16 v12, v182, v236, v12\n"
    "v_dot2_f32_bf16 v13, v182, v237, v13\n"
    "v_dot2_f32_bf16 v14, v183, v236, v14\n"
    "v_dot2_f32_bf16 v15, v183, v237, v15\n"
    "v_dot2_f32_bf16 v16, v184, v236, v16\n"
    "v_dot2_f32_bf16 v17, v184, v237, v17\n"
    "v_dot2_f32_bf16 v18, v185, v236, v18\n"
    "v_dot2_f32_bf16 v19, v185, v237, v19\n"
    "v_dot2_f32_bf16 v20, v186, v236, v20\n"
    "v_dot2_f32_bf16 v21, v186, v237, v21\n"
    "v_dot2_f32_bf16 v22, v187, v236, v22\n"
    "v_dot2_f32_bf16 v23, v187, v237, v23\n"
    "v_dot2_f32_bf16 v24, v188, v236, v24\n"
    "v_dot2_f32_bf16 v25, v188, v237, v25\n"
    "v_dot2_f32_bf16 v26, v189, v236, v26\n"
    "v_dot2_f32_bf16 v27, v189, v237, v27\n"
    "v_dot2_f32_bf16 v28, v190, v236, v28\n"
    "v_dot2_f32_bf16 v29, v190, v237, v29\n"
    "v_dot2_f32_bf16 v30, v191, v236, v30\n"
    "v_dot2_f32_bf16 v31, v191, v237, v31\n"
    "v_xor_b32 v249, v246, v244\n"
    "v_and_b32 v250, 0x80, v246\n"
    "v_cmp_lt_u32 vcc, 0xff, v249\n"
    "v_cmp_le_u32 s[96:97], 64, v250\n"
    "s_and_b32 s96, vcc_lo, s96\n"
    "s_andn2_b32 s96, s80, s96\n"
    "s_cbranch_scc1 .Lslow_exit\n"
    ".Lret_exit:\n"
    "s_waitcnt vmcnt(3)\n"
    "v_readfirstlane_b32 s92, v161\n"
    "v_readfirstlane_b32 s94, v162\n"
    "s_nop 0\n"
    "s_min_u32 s92, s92, s94\n"
    "s_cmp_eq_u32 s92, 0\n"
    "s_cbranch_scc1 .Lslow_x12\n"
    ".Lret_x12:\n"
    "s_add_u32 s99, s15, 0x4000\n"
    "buffer_load_dwordx4 v[32:35], v240, s[44:47], s99 offen sc1\n"
    "buffer_load_dwordx4 v[36:39], v240, s[44:47], s99 offen offset:1024 sc1\n"
    "buffer_load_dwordx4 v[40:43], v240, s[44:47], s99 offen offset:2048 sc1\n"
    "buffer_load_dwordx4 v[44:47], v240, s[44:47], s99 offen offset:3072 sc1\n"
    "s_add_u32 s99, s15, 0x8000\n"
    "buffer_load_dwordx4 v[48:51], v240, s[44:47], s99 offen sc1\n"
    "buffer_load_dwordx4 v[52:55], v240, s[44:47], s99 offen offset:1024 sc1\n"
    "buffer_load_dwordx4 v[56:59], v240, s[44:47], s99 offen offset:2048 sc1\n"
    "buffer_load_dwordx4 v[60:63], v240, s[44:47], s99 offen offset:3072 sc1\n"
    ".Lhave12:\n"
    "buffer_load_dwordx4 v[164:167], v243, s[44:47], 0 offen sc1\n"
    "s_waitcnt vmcnt(0)\n"
    "v_readfirstlane_b32 s92, v167\n"
    "s_nop 0\n"
    "s_cmp_eq_u32 s92, 0\n"
    "s_cbranch_scc1 .Lslow_x3\n"
    ".Lret_x3:\n"
    "s_add_u32 s99, s15, 0xc000\n"
    "buffer_load_dwordx4 v[64:67], v240, s[44:47], s99 offen sc1\n"
    "buffer_load_dwordx4 v[68:71], v240, s[44:47], s99 offen offset:1024 sc1\n"
    "s_add_u32 s99, s15, 0xc000\n"
    "buffer_load_dwordx4 v[72:75], v240, s[44:47], s99 offen offset:2048 sc1\n"
    "buffer_load_dwordx4 v[76:79], v240, s[44:47], s99 offen offset:3072 sc1\n"
    "v_dot2_f32_bf16 v0, v32, v236, v0\n"
    "v_dot2_f32_bf16 v1, v32, v237, v1\n"
    "v_dot2_f32_bf16 v2, v33, v236, v2\n"
    "v_dot2_f32_bf16 v3, v33, v237, v3\n"
    "v_dot2_f32_bf16 v4, v34, v236, v4\n"
    "v_dot2_f32_bf16 v5, v34, v237, v5\n"
    "v_dot2_f32_bf16 v6, v35, v236, v6\n"
    "v_dot2_f32_bf16 v7, v35, v237, v7\n"
    "v_dot2_f32_bf16 v8, v36, v236, v8\n"
    "v_dot2_f32_bf16 v9, v36, v237, v9\n"
    "v_dot2_f32_bf16 v10, v37, v236, v10\n"
    "v_dot2_f32_bf16 v11, v37, v237, v11\n"
    "v_dot2_f32_bf16 v12, v38, v236, v12\n"
    "v_dot2_f32_bf16 v13, v38, v237, v13\n"
    "v_dot2_f32_bf16 v14, v39, v236, v14\n"
    "v_dot2_f32_bf16 v15, v39, v237, v15\n"
    "v_dot2_f32_bf16 v16, v40, v236, v16\n"
    "v_dot2_f32_bf16 v17, v40, v237, v17\n"
    "v_dot2_f32_bf16 v18, v41, v236, v18\n"
    "v_dot2_f32_bf16 v19, v41, v237, v19\n"
    "v_dot2_f32_bf16 v20, v42, v236, v20\n"
    "v_dot2_f32_bf16 v21, v42, v237, v21\n"
    "v_dot2_f32_bf16 v22, v43, v236, v22\n"
    "v_dot2_f32_bf16 v23, v43, v237, v23\n"
    "v_dot2_f32_bf16 v24, v44, v236, v24\n"
    "v_dot2_f32_bf16 v25, v44, v237, v25\n"
    "v_dot2_f32_bf16 v26, v45, v236, v26\n"
    "v_dot2_f32_bf16 v27, v45, v237, v27\n"
    "v_dot2_f32_bf16 v28, v46, v236, v28\n"
    "v_dot2_f32_bf16 v29, v46, v237, v29\n"
    "v_dot2_f32_bf16 v30, v47, v236, v30\n"
    "v_dot2_f32_bf16 v31, v47, v237, v31\n"
    "v_dot2_f32_bf16 v0, v48, v236, v0\n"
    "v_dot2_f32_bf16 v1, v48, v237, v1\n"
    "v_dot2_f32_bf16 v2, v49, v236, v2\n"
    "v_dot2_f32_bf16 v3, v49, v237, v3\n"
    "v_dot2_f32_bf16 v4, v50, v236, v4\n"
    "v_dot2_f32_bf16 v5, v50, v237, v5\n"
    "v_dot2_f32_bf16 v6, v51, v236, v6\n"
    "v_dot2_f32_bf16 v7, v51, v237, v7\n"
    "v_dot2_f32_bf16 v8, v52, v236, v8\n"
    "v_dot2_f32_bf16 v9, v52, v237, v9\n"
    "v_dot2_f32_bf16 v10, v53, v236, v10\n"
    "v_dot2_f32_bf16 v11, v53, v237, v11\n"
    "v_dot2_f32_bf16 v12, v54, v236, v12\n"
    "v_dot2_f32_bf16 v13, v54, v237, v13\n"
    "v_dot2_f32_bf16 v14, v55, v236, v14\n"
    "v_dot2_f32_bf16 v15, v55, v237, v15\n"
    "v_dot2_f32_bf16 v16, v56, v236, v16\n"
    "v_dot2_f32_bf16 v17, v56, v237, v17\n"
    "v_dot2_f32_bf16 v18, v57, v236, v18\n"
    "v_dot2_f32_bf16 v19, v57, v237, v19\n"
    "v_dot2_f32_bf16 v20, v58, v236, v20\n"
    "v_dot2_f32_bf16 v21, v58, v237, v21\n"
    "v_dot2_f32_bf16 v22, v59, v236, v22\n"
    "v_dot2_f32_bf16 v23, v59, v237, v23\n"
    "v_dot2_f32_bf16 v24, v60, v236, v24\n"
    "v_dot2_f32_bf16 v25, v60, v237, v25\n"
    "v_dot2_f32_bf16 v26, v61, v236, v26\n"
    "v_dot2_f32_bf16 v27, v61, v237, v27\n"
    "v_dot2_f32_bf16 v28, v62, v236, v28\n"
    "v_dot2_f32_bf16 v29, v62, v237, v29\n"
    "v_dot2_f32_bf16 v30, v63, v236, v30\n"
    "v_dot2_f32_bf16 v31, v63, v237, v31\n"
    "s_waitcnt vmcnt(3)\n"
    "v_dot2_f32_bf16 v0, v64, v236, v0\n"
    "v_dot2_f32_bf16 v1, v64, v237, v1\n"
    "v_dot2_f32_bf16 v2, v65, v236, v2\n"
    "v_dot2_f32_bf16 v3, v65, v237, v3\n"
    "v_dot2_f32_bf16 v4, v66, v236, v4\n"
    "v_dot2_f32_bf16 v5, v66, v237, v5\n"
    "v_dot2_f32_bf16 v6, v67, v236, v6\n"
    "v_dot2_f32_bf16 v7, v67, v237, v7\n"
    "v_cvt_pk_bf16_f32 v96, v0, v1\n"
    "v_cvt_pk_bf16_f32 v97, v2, v3\n"
    "v_cvt_pk_bf16_f32 v98, v4, v5\n"
    "v_cvt_pk_bf16_f32 v99, v6, v7\n"
    "s_nop 0\n"
    "v_permlane16_swap_b32 v96, v98\n"
    "v_permlane16_swap_b32 v97, v99\n"
    "buffer_store_dwordx4 v[96:99], v242, s[40:43], s16 offen\n"
    "s_waitcnt vmcnt(3)\n"
    "v_dot2_f32_bf16 v8, v68, v236, v8\n"
    "v_dot2_f32_bf16 v9, v68, v237, v9\n"
    "v_dot2_f32_bf16 v10, v69, v236, v10\n"
    "v_dot2_f32_bf16 v11, v69, v237, v11\n"
    "v_dot2_f32_bf16 v12, v70, v236, v12\n"
    "v_dot2_f32_bf16 v13, v70, v237, v13\n"
    "v_dot2_f32_bf16 v14, v71, v236, v14\n"
    "v_dot2_f32_bf16 v15, v71, v237, v15\n"
    "v_cvt_pk_bf16_f32 v100, v8, v9\n"
    "v_cvt_pk_bf16_f32 v101, v10, v11\n"
    "v_cvt_pk_bf16_f32 v102, v12, v13\n"
    "v_cvt_pk_bf16_f32 v103, v14, v15\n"
    "s_nop 0\n"
    "v_permlane16_swap_b32 v100, v102\n"
    "v_permlane16_swap_b32 v101, v103\n"
    "buffer_store_dwordx4 v[100:103], v242, s[40:43], s16 offen offset:64\n"
    "s_waitcnt vmcnt(2)\n"
    "v_max3_u32 v249, v72, v73, v74\n"
    "v_max3_u32 v249, v249, v75, v76\n"
    "v_max3_u32 v249, v249, v77, v78\n"
    "v_max_u32 v249, v249, v79\n"
    "v_cmp_eq_u32 vcc, -1, v249\n"
    "s_cmp_lg_u64 vcc, 0\n"
    "s_cbranch_scc1 .Lslow_arm\n"
    ".Lret_arm:\n"
    "v_dot2_f32_bf16 v16, v72, v236, v16\n"
    "v_dot2_f32_bf16 v17, v72, v237, v17\n"
    "v_dot2_f32_bf16 v18, v73, v236, v18\n"
    "v_dot2_f32_bf16 v19, v73, v237, v19\n"
    "v_dot2_f32_bf16 v20, v74, v236, v20\n"
    "v_dot2_f32_bf16 v21, v74, v237, v21\n"
    "v_dot2_f32_bf16 v22, v75, v236, v22\n"
    "v_dot2_f32_bf16 v23, v75, v237, v23\n"
    "v_cvt_pk_bf16_f32 v104, v16, v17\n"
    "v_cvt_pk_bf16_f32 v105, v18, v19\n"
    "v_cvt_pk_bf16_f32 v106, v20, v21\n"
    "v_cvt_pk_bf16_f32 v107, v22, v23\n"
    "s_nop 0\n"
    "v_permlane16_swap_b32 v104, v106\n"
    "v_permlane16_swap_b32 v105, v107\n"
    "buffer_store_dwordx4 v[104:107], v242, s[40:43], s16 offen offset:128\n"
    "v_dot2_f32_bf16 v24, v76, v236, v24\n"
    "v_dot2_f32_bf16 v25, v76, v237, v25\n"
    "v_dot2_f32_bf16 v26, v77, v236, v26\n"
    "v_dot2_f32_bf16 v27, v77, v237, v27\n"
    "v_dot2_f32_bf16 v28, v78, v236, v28\n"
    "v_dot2_f32_bf16 v29, v78, v237, v29\n"
    "v_dot2_f32_bf16 v30, v79, v236, v30\n"
    "v_dot2_f32_bf16 v31, v79, v237, v31\n"
    "v_cvt_pk_bf16_f32 v108, v24, v25\n"
    "v_cvt_pk_bf16_f32 v109, v26, v27\n"
    "v_cvt_pk_bf16_f32 v110, v28, v29\n"
    "v_cvt_pk_bf16_f32 v111, v30, v31\n"
    "s_nop 0\n"
    "v_permlane16_swap_b32 v108, v110\n"
    "v_permlane16_swap_b32 v109, v111\n"
    "buffer_store_dwordx4 v[108:111], v242, s[40:43], s16 offen offset:192\n"
    "s_waitcnt vmcnt(0)\n"
    "s_endpgm\n"
    ".Lslow_xn:\n"
    "s_mov_b32 s95, 0\n"
    ".Lspin_xn:\n"
    "buffer_load_dword v246, v245, s[44:47], 0 offen sc1\n"
    "s_add_u32 s95, s95, 1\n"
    "s_and_b32 s97, s95, 3\n"
    "s_cbranch_scc1 .Lnobump_xn\n"
    "v_mov_b32 v249, 0x100\n"
    "buffer_atomic_add v249, v247, s[44:47], 0 offen\n"
    ".Lnobump_xn:\n"
    "s_waitcnt vmcnt(0)\n"
    "v_xor_b32 v249, v246, v244\n"
    "v_and_b32 v250, 63, v246\n"
    "v_cmp_lt_u32 vcc, 0xff, v249\n"
    "v_cmp_le_u32 s[96:97], 1, v250\n"
    "s_and_b32 s96, vcc_lo, s96\n"
    "s_andn2_b32 s96, s81, s96\n"
    "s_cbranch_scc0 .Lret_xn\n"
    "s_cmp_lt_u32 s95, 0x20000\n"
    "s_cbranch_scc1 .Lspin_xn\n"
    "s_branch .Lret_xn\n"
    ".Lslow_kv:\n"
    "s_mov_b32 s95, 0\n"
    ".Lspin_kv:\n"
    "buffer_load_dword v246, v245, s[44:47], 0 offen sc1\n"
    "s_add_u32 s95, s95, 1\n"
    "s_and_b32 s97, s95, 3\n"
    "s_cbranch_scc1 .Lnobump_kv\n"
    "v_mov_b32 v249, 0x100\n"
    "buffer_atomic_add v249, v247, s[44:47], 0 offen\n"
    ".Lnobump_kv:\n"
    "s_waitcnt vmcnt(0)\n"
    "v_xor_b32 v249, v246, v244\n"
    "v_and_b32 v250, 63, v246\n"
    "v_cmp_lt_u32 vcc, 0xff, v249\n"
    "v_cmp_le_u32 s[96:97], 2, v250\n"
    "s_and_b32 s96, vcc_lo, s96\n"
    "s_andn2_b32 s96, s82, s96\n"
    "s_cbranch_scc0 .Lret_kv\n"
    "s_cmp_lt_u32 s95, 0x20000\n"
    "s_cbranch_scc1 .Lspin_kv\n"
    "s_branch .Lret_kv\n"
    ".Lslow_h23:\n"
    "s_mov_b32 s95, 0\n"
    ".Lspin_h23:\n"
    "buffer_load_dword v246, v245, s[44:47], 0 offen sc1\n"
    "s_add_u32 s95, s95, 1\n"
    "s_and_b32 s97, s95, 3\n"
    "s_cbranch_scc1 .Lnobump_h23\n"
    "v_mov_b32 v249, 0x100\n"
    "buffer_atomic_add v249, v247, s[44:47], 0 offen\n"
    ".Lnobump_h23:\n"
    "s_waitcnt vmcnt(0)\n"
    "v_xor_b32 v249, v246, v244\n"
    "v_and_b32 v250, 63, v246\n"
    "v_cmp_lt_u32 vcc, 0xff, v249\n"
    "v_cmp_le_u32 s[96:97], 4, v250\n"
    "s_and_b32 s96, vcc_lo, s96\n"
    "s_andn2_b32 s96, s83, s96\n"
    "s_andn2_b32 s97, s80, vcc_lo\n"
    "s_or_b32 s96, s96, s97\n"
    "s_cbranch_scc0 .Lret_h23\n"
    "s_cmp_lt_u32 s95, 0x20000\n"
    "s_cbranch_scc1 .Lspin_h23\n"
    "s_branch .Lret_h23\n"
    ".Lslow_o:\n"
    "s_mov_b32 s95, 0\n"
    ".Lspin_o:\n"
    "buffer_load_dword v246, v245, s[44:47], 0 offen sc1\n"
    "s_add_u32 s95, s95, 1\n"
    "s_and_b32 s97, s95, 3\n"
    "s_cbranch_scc1 .Lnobump_o\n"
    "v_mov_b32 v249, 0x100\n"
    "buffer_atomic_add v249, v247, s[44:47], 0 offen\n"
    ".Lnobump_o:\n"
    "s_waitcnt vmcnt(0)\n"
    "v_xor_b32 v249, v246, v244\n"
    "v_and_b32 v250, 63, v246\n"
    "v_cmp_lt_u32 vcc, 0xff, v249\n"
    "v_cmp_le_u32 s[96:97], 5, v250\n"
    "s_and_b32 s96, vcc_lo, s96\n"
    "s_andn2_b32 s96, s84, s96\n"
    "s_cbranch_scc0 .Lret_o\n"
    "s_cmp_lt_u32 s95, 0x20000\n"
    "s_cbranch_scc1 .Lspin_o\n"
    "s_branch .Lret_o\n"
    ".Lslow_h01:\n"
    "s_mov_b32 s95, 0\n"
    ".Lspin_h01:\n"
    "buffer_load_dword v246, v245, s[44:47], 0 offen sc1\n"
    "s_add_u32 s95, s95, 1\n"
    "s_and_b32 s97, s95, 3\n"
    "s_cbranch_scc1 .Lnobump_h01\n"
    "v_mov_b32 v249, 0x100\n"
    "buffer_atomic_add v249, v247, s[44:47], 0 offen\n"
    ".Lnobump_h01:\n"
    "s_waitcnt vmcnt(0)\n"
    "v_xor_b32 v249, v246, v244\n"
    "v_and_b32 v250, 63, v246\n"
    "v_cmp_lt_u32 vcc, 0xff, v249\n"
    "v_cmp_le_u32 s[96:97], 3, v250\n"
    "s_and_b32 s96, vcc_lo, s96\n"
    "s_andn2_b32 s96, s83, s96\n"
    "s_cbranch_scc0 .Lret_h01\n"
    "s_cmp_lt_u32 s95, 0x20000\n"
    "s_cbranch_scc1 .Lspin_h01\n"
    "s_branch .Lret_h01\n"
    ".Lslow_exit:\n"
    "s_mov_b32 s95, 0\n"
    ".Lspin_exit:\n"
    "buffer_load_dword v246, v245, s[44:47], 0 offen sc1\n"
    "s_add_u32 s95, s95, 1\n"
    "s_and_b32 s97, s95, 3\n"
    "s_cbranch_scc1 .Lnobump_exit\n"
    "v_mov_b32 v249, 0x100\n"
    "buffer_atomic_add v249, v247, s[44:47], 0 offen\n"
    ".Lnobump_exit:\n"
    "s_waitcnt vmcnt(0)\n"
    "v_xor_b32 v249, v246, v244\n"
    "v_and_b32 v250, 0x80, v246\n"
    "v_cmp_lt_u32 vcc, 0xff, v249\n"
    "v_cmp_le_u32 s[96:97], 64, v250\n"
    "s_and_b32 s96, vcc_lo, s96\n"
    "s_andn2_b32 s96, s80, s96\n"
    "s_cbranch_scc0 .Lret_exit\n"
    "s_cmp_lt_u32 s95, 0x20000\n"
    "s_cbranch_scc1 .Lspin_exit\n"
    "s_branch .Lret_exit\n"
    ".Lslow_x12:\n"
    "s_mov_b32 s95, 0\n"
    ".Lspin_x12:\n"
    "buffer_load_dwordx4 v[160:163], v243, s[44:47], 0 offen sc1\n"
    "s_add_u32 s95, s95, 1\n"
    "s_and_b32 s97, s95, 3\n"
    "s_cbranch_scc1 .Lnobump_x12\n"
    "v_mov_b32 v249, 0x100\n"
    "buffer_atomic_add v249, v247, s[44:47], 0 offen\n"
    ".Lnobump_x12:\n"
    "s_waitcnt vmcnt(0)\n"
    "v_readfirstlane_b32 s92, v161\n"
    "v_readfirstlane_b32 s94, v162\n"
    "s_nop 0\n"
    "s_min_u32 s92, s92, s94\n"
    "s_cmp_eq_u32 s92, 0\n"
    "s_cbranch_scc0 .Lret_x12\n"
    "s_cmp_lt_u32 s95, 0x20000\n"
    "s_cbranch_scc1 .Lspin_x12\n"
    "s_branch .Lret_x12\n"
    ".Lslow_x3:\n"
    "s_mov_b32 s95, 0\n"
    ".Lspin_x3:\n"
    "buffer_load_dwordx4 v[164:167], v243, s[44:47], 0 offen sc1\n"
    "s_add_u32 s95, s95, 1\n"
    "s_and_b32 s97, s95, 3\n"
    "s_cbranch_scc1 .Lnobump_x3\n"
    "v_mov_b32 v249, 0x100\n"
    "buffer_atomic_add v249, v247, s[44:47], 0 offen\n"
    ".Lnobump_x3:\n"
    "s_waitcnt vmcnt(0)\n"
    "v_readfirstlane_b32 s92, v167\n"
    "s_nop 0\n"
    "s_cmp_eq_u32 s92, 0\n"
    "s_cbranch_scc0 .Lret_x3\n"
    "s_cmp_lt_u32 s95, 0x20000\n"
    "s_cbranch_scc1 .Lspin_x3\n"
    "s_branch .Lret_x3\n"
    ".Lslow_arm:\n"
    "s_mov_b32 s95, 0\n"
    ".Lspin_arm:\n"
    "s_add_u32 s99, s15, 0xc000\n"
    "buffer_load_dwordx4 v[72:75], v240, s[44:47], s99 offen offset:2048 sc1\n"
    "buffer_load_dwordx4 v[76:79], v240, s[44:47], s99 offen offset:3072 sc1\n"
    "s_add_u32 s95, s95, 1\n"
    "s_and_b32 s97, s95, 3\n"
    "s_cbranch_scc1 .Lnobump_arm\n"
    "v_mov_b32 v249, 0x100\n"
    "buffer_atomic_add v249, v247, s[44:47], 0 offen\n"
    ".Lnobump_arm:\n"
    "s_waitcnt vmcnt(0)\n"
    "v_max3_u32 v249, v72, v73, v74\n"
    "v_max3_u32 v249, v249, v75, v76\n"
    "v_max3_u32 v249, v249, v77, v78\n"
    "v_max_u32 v249, v249, v79\n"
    "v_cmp_eq_u32 vcc, -1, v249\n"
    "s_cmp_lg_u64 vcc, 0\n"
    "s_cbranch_scc0 .Lret_arm\n"
    "s_cmp_lt_u32 s95, 0x20000\n"
    "s_cbranch_scc1 .Lspin_arm\n"
    "s_branch .Lret_arm\n"
    ".Lend:\n"
    :
    : "s"(kargs), "s"(blockIdx.x), "s"(wave)
    :
      "memory", "vcc", "scc", "s4", "s5", "s6", "s7", "s8", "s9", "s10", "s11", "s12", "s13", "s14", "s15", "s16", "s17", "s18", "s19", "s20", "s21", "s22", "s23", "s24",
      "s25", "s26", "s27", "s28", "s29", "s30", "s31", "s33", "s34", "s35", "s36", "s37", "s38", "s39", "s40", "s41", "s42", "s43", "s44", "s45", "s46", "s47", "s48", "s49",
      "s50", "s51", "s52", "s53", "s54", "s55", "s56", "s57", "s58", "s59", "s60", "s61", "s62", "s63", "s64", "s65", "s66", "s67", "s68", "s69", "s70", "s71", "s72", "s73",
      "s74", "s75", "s76", "s77", "s78", "s79", "s80", "s81", "s82", "s83", "s84", "s85", "s86", "s87", "s88", "s89", "s90", "s91", "s92", "s93", "s94", "s95", "s96", "s97",
      "s98", "s99", "v0", "v1", "v2", "v3", "v4", "v5", "v6", "v7", "v8", "v9", "v10", "v11", "v12", "v13", "v14", "v15", "v16", "v17", "v18", "v19", "v20", "v21",
      "v22", "v23", "v24", "v25", "v26", "v27", "v28", "v29", "v30", "v31", "v32", "v33", "v34", "v35", "v36", "v37", "v38", "v39", "v40", "v41", "v42", "v43", "v44", "v45",
      "v46", "v47", "v48", "v49", "v50", "v51", "v52", "v53", "v54", "v55", "v56", "v57", "v58", "v59", "v60", "v61", "v62", "v63", "v64", "v65", "v66", "v67", "v68", "v69",
      "v70", "v71", "v72", "v73", "v74", "v75", "v76", "v77", "v78", "v79", "v80", "v81", "v82", "v83", "v84", "v85", "v86", "v87", "v88", "v89", "v90", "v91", "v92", "v93",
      "v94", "v95", "v96", "v97", "v98", "v99", "v100", "v101", "v102", "v103", "v104", "v105", "v106", "v107", "v108", "v109", "v110", "v111", "v112", "v113", "v114", "v115", "v116", "v117",
      "v118", "v119", "v120", "v121", "v122", "v123", "v124", "v125", "v126", "v127", "v128", "v129", "v130", "v131", "v132", "v133", "v134", "v135", "v136", "v137", "v138", "v139", "v140", "v141",
      "v142", "v143", "v144", "v145", "v146", "v147", "v148", "v149", "v150", "v151", "v152", "v153", "v154", "v155", "v156", "v157", "v158", "v159", "v160", "v161", "v162", "v163", "v164", "v165",
      "v166", "v167", "v168", "v169", "v170", "v171", "v172", "v173", "v174", "v175", "v176", "v177", "v178", "v179", "v180", "v181", "v182", "v183", "v184", "v185", "v186", "v187", "v188", "v189",
      "v190", "v191", "v192", "v193", "v194", "v195", "v196", "v197", "v198", "v199", "v200", "v201", "v202", "v203", "v204", "v205", "v206", "v207", "v208", "v209", "v210", "v211", "v212", "v213",
      "v214", "v215", "v216", "v217", "v218", "v219", "v220", "v221", "v222", "v223", "v224", "v225", "v226", "v227", "v228", "v229", "v230", "v231", "v232", "v233", "v234", "v235", "v236", "v237",
      "v238", "v239", "v240", "v241", "v242", "v243", "v244", "v245", "v246", "v247", "v248", "v249", "v250", "v251", "v252", "v253", "v254", "v255", "a0", "a1", "a2", "a3", "a4", "a5",
      "a6", "a7", "a8", "a9", "a10", "a11", "a12", "a13", "a14", "a15", "a16", "a17", "a18", "a19", "a20", "a21", "a22", "a23", "a24", "a25", "a26", "a27", "a28", "a29",
      "a30", "a31", "a32", "a33", "a34", "a35", "a36", "a37", "a38", "a39", "a40", "a41", "a42", "a43", "a44", "a45", "a46", "a47", "a48", "a49", "a50", "a51", "a52", "a53",
      "a54", "a55", "a56", "a57", "a58", "a59", "a60", "a61", "a62", "a63", "a64", "a65", "a66", "a67", "a68", "a69", "a70", "a71", "a72", "a73", "a74", "a75", "a76", "a77",
      "a78", "a79", "a80", "a81", "a82", "a83", "a84", "a85", "a86", "a87", "a88", "a89", "a90", "a91", "a92", "a93", "a94", "a95", "a96", "a97", "a98", "a99", "a100", "a101",
      "a102", "a103", "a104", "a105", "a106", "a107", "a108", "a109", "a110", "a111", "a112", "a113", "a114", "a115", "a116", "a117", "a118", "a119", "a120", "a121", "a122", "a123", "a124", "a125",
      "a126", "a127", "a128", "a129", "a130", "a131", "a132", "a133", "a134", "a135", "a136", "a137", "a138", "a139", "a140", "a141", "a142", "a143", "a144", "a145", "a146", "a147", "a148", "a149",
      "a150", "a151", "a152", "a153", "a154", "a155", "a156", "a157", "a158", "a159", "a160", "a161", "a162", "a163", "a164", "a165", "a166", "a167", "a168", "a169", "a170", "a171", "a172", "a173",
      "a174", "a175", "a176", "a177", "a178", "a179", "a180", "a181", "a182", "a183", "a184", "a185", "a186", "a187", "a188", "a189", "a190", "a191", "a192", "a193", "a194", "a195", "a196", "a197",
      "a198", "a199", "a200", "a201", "a202", "a203", "a204", "a205", "a206", "a207", "a208", "a209", "a210", "a211", "a212", "a213", "a214", "a215", "a216", "a217", "a218", "a219", "a220", "a221",
      "a222", "a223", "a224", "a225", "a226", "a227", "a228", "a229", "a230", "a231", "a232", "a233", "a234", "a235", "a236", "a237", "a238", "a239", "a240", "a241", "a242", "a243", "a244", "a245",
      "a246", "a247", "a248", "a249", "a250", "a251", "a252", "a253", "a254", "a255"
  );
}

extern "C" void kernel_launch(void* const* d_in, const int* in_sizes, int n_in, void* d_out, int out_size, void* d_ws,
                              size_t ws_size, hipStream_t stream) {
  fused_block_kernel<<<256, 256, 0, stream>>>(d_in[0], d_in[1], d_in[2], d_in[4], d_in[5], d_out, d_ws);
}
```
